```python
import torch

def _softmax(x, axis=-1):
    return torch.softmax(x, dim=axis)
def _gelu(x):
    return torch.nn.functional.gelu(x, approximate="tanh")
def _silu(x):
    return torch.nn.functional.silu(x)
def _sigmoid(x):
    return torch.sigmoid(x)
def _layernorm(x, axis=-1, eps=1e-5):
    m = x.mean(dim=axis, keepdim=True)
    v = x.var(dim=axis, keepdim=True, unbiased=False)
    return (x - m) / torch.sqrt(v + eps)
def _scatter_add(tgt, idx, src, axis=0):
    return tgt.clone().index_add_(axis, idx, src)
def _segment_sum(src, seg, n):
    out = torch.zeros((n,) + src.shape[1:], dtype=src.dtype)
    return out.index_add_(0, seg, src)
def _pad(x, pw):
    flat = tuple(v for pair in reversed(pw) for v in pair)
    return torch.nn.functional.pad(x, flat)

kernel_name = 'interleave_layers__grouped_kv_mqa__dense_activation_gelu__prompt'

def setup_inputs():

    _w = (lambda a: (lambda t: t.to(torch.bfloat16) if t.is_floating_point() else t)(torch.as_tensor(a)))
    def _A_setup():
        B, S, D, H = 1, 2048, 512, 32
        Dh = D // H
        return {
            "x": _w((lambda *s: torch.randn(*s, dtype=torch.bfloat16) * 0.05)(B, S, D)),
            "wq": _w((lambda *s: torch.randn(*s, dtype=torch.bfloat16) * 0.05)(D, H * Dh)),
            "wk": _w((lambda *s: torch.randn(*s, dtype=torch.bfloat16) * 0.05)(D, Dh)),
            "wv": _w((lambda *s: torch.randn(*s, dtype=torch.bfloat16) * 0.05)(D, Dh)),
            "wo": _w((lambda *s: torch.randn(*s, dtype=torch.bfloat16) * 0.05)(H * Dh, D)),
        }
    _A_in = _A_setup()

    def _B_setup():
        B, S, D, F = 1, 2048, 512, 1376
        return {
            "x": _w((lambda *s: torch.randn(*s, dtype=torch.bfloat16) * 0.05)(B, S, D)),
            "w1": _w((lambda *s: torch.randn(*s, dtype=torch.bfloat16) * 0.8)(D, F) * (D ** -0.5)),
            "b1": _w((lambda *s: torch.randn(*s, dtype=torch.bfloat16) * 12.8)(F) * 0.01),
            "w2": _w((lambda *s: torch.randn(*s, dtype=torch.bfloat16) * 0.2)(F, D) * (F ** -0.5)),
            "b2": _w((lambda *s: torch.randn(*s, dtype=torch.bfloat16) * 1.6)(D) * 0.01),
        }
    _B_in = _B_setup()

    return {
        "A_x": _A_in["x"],
        "A_wq": _A_in["wq"],
        "A_wk": _A_in["wk"],
        "A_wv": _A_in["wv"],
        "A_wo": _A_in["wo"],
        "B_x": _B_in["x"],
        "B_w1": _B_in["w1"],
        "B_b1": _B_in["b1"],
        "B_w2": _B_in["w2"],
        "B_b2": _B_in["b2"],
    }

def reference(A_x, A_wq, A_wk, A_wv, A_wo, B_x, B_w1, B_b1, B_w2, B_b2):
    def _A_ref(x, wq, wk, wv, wo):
        B, S, D = x.shape
        H = 32
        Dh = D // H
        q = (lambda x, *d: x.permute(*d))((x @ wq).reshape(B, S, H, Dh), 0, 2, 1, 3)
        k = x @ wk
        v = x @ wv
        scores = torch.einsum("bhqd,bkd->bhqk", q, k) * (Dh ** -0.5)
        p = _softmax(scores, axis=-1)
        o = torch.einsum("bhqk,bkd->bhqd", p, v)
        return (lambda x, *d: x.permute(*d))(o, 0, 2, 1, 3).reshape(B, S, H * Dh) @ wo

    def _B_ref(x, w1, b1, w2, b2):
        h = torch.einsum("bsd,df->bsf", x, w1) + b1
        h = _gelu(h)
        return torch.einsum("bsf,fd->bsd", h, w2) + b2

    x = A_x
    x = x + _A_ref(_layernorm(x, axis=-1), A_wq, A_wk, A_wv, A_wo)
    x = x + _B_ref(_layernorm(x, axis=-1), B_w1, B_b1, B_w2, B_b2)
    x = x + _A_ref(_layernorm(x, axis=-1), A_wq, A_wk, A_wv, A_wo)
    x = x + _B_ref(_layernorm(x, axis=-1), B_w1, B_b1, B_w2, B_b2)
    return x
```

```cpp
#include <hip/hip_runtime.h>
#include <hip/hip_bf16.h>
typedef __hip_bfloat16 bf16;
constexpr int S = 2048, D = 512, H = 32, DH = 16, F = 1376;
__device__ inline float b2f(bf16 x) { return __bfloat162float(x); }
__global__ void ln_kernel(const float* x, float* y) {
    int t = blockIdx.x; __shared__ float red[2][256];
    float s = 0, s2 = 0;
    for (int i = threadIdx.x; i < D; i += 256) { float v = x[t * D + i]; s += v; }
    red[0][threadIdx.x] = s; __syncthreads();
    for (int o = 128; o > 0; o >>= 1) { if (threadIdx.x < o) red[0][threadIdx.x] += red[0][threadIdx.x + o]; __syncthreads(); }
    float m = red[0][0] / D;
    for (int i = threadIdx.x; i < D; i += 256) { float v = x[t * D + i] - m; s2 += v * v; }
    red[1][threadIdx.x] = s2; __syncthreads();
    for (int o = 128; o > 0; o >>= 1) { if (threadIdx.x < o) red[1][threadIdx.x] += red[1][threadIdx.x + o]; __syncthreads(); }
    float r = rsqrtf(red[1][0] / D + 1e-5f);
    for (int i = threadIdx.x; i < D; i += 256) y[t * D + i] = (x[t * D + i] - m) * r;
}
__global__ void gemm_kernel(const float* A, const bf16* W, const bf16* bias, const float* res, float* C, int M, int N, int K, int gelu, int roundout) {
    int n = blockIdx.x * 64 + (threadIdx.x & 63), m = blockIdx.y * 4 + (threadIdx.x >> 6);
    if (n >= N) return;
    float acc = 0;
    for (int k = 0; k < K; ++k) acc += A[m * K + k] * b2f(W[k * N + n]);
    if (bias) acc += b2f(bias[n]);
    if (gelu) { float u = 0.7978845608f * (acc + 0.044715f * acc * acc * acc); acc = 0.5f * acc * (1.f + tanhf(u)); }
    if (res) acc += res[m * N + n];
    if (roundout) acc = b2f(__float2bfloat16(acc));
    C[m * N + n] = acc;
}
__global__ void attn_kernel(const float* q, const float* k, const float* v, float* o) {
    int t = blockIdx.x, h = threadIdx.x;
    float qq[DH]; for (int d = 0; d < DH; ++d) qq[d] = q[t * D + h * DH + d] * 0.25f;
    float m = -1e30f, l = 0, acc[DH]; for (int d = 0; d < DH; ++d) acc[d] = 0;
    for (int j = 0; j < S; ++j) {
        float s = 0; for (int d = 0; d < DH; ++d) s += qq[d] * k[j * DH + d];
        float mn = fmaxf(m, s), a = __expf(m - mn), p = __expf(s - mn);
        l = l * a + p; for (int d = 0; d < DH; ++d) acc[d] = acc[d] * a + p * v[j * DH + d];
        m = mn;
    }
    for (int d = 0; d < DH; ++d) o[t * D + h * DH + d] = acc[d] / l;
}
__global__ void cvt_in(const bf16* x, float* y, int n) { int i = blockIdx.x * 256 + threadIdx.x; if (i < n) y[i] = b2f(x[i]); }
__global__ void cvt_out(const float* x, bf16* y, int n) { int i = blockIdx.x * 256 + threadIdx.x; if (i < n) y[i] = __float2bfloat16(x[i]); }
extern "C" void kernel_launch(void* const* d_in, const int* in_sizes, int n_in, void* d_out, int out_size, void* d_ws, size_t ws_size, hipStream_t stream) {
    const bf16 *x = (const bf16*)d_in[0], *wq = (const bf16*)d_in[1], *wk = (const bf16*)d_in[2], *wv = (const bf16*)d_in[3], *wo = (const bf16*)d_in[4];
    const bf16 *w1 = (const bf16*)d_in[6], *b1 = (const bf16*)d_in[7], *w2 = (const bf16*)d_in[8], *b2 = (const bf16*)d_in[9];
    float* ws = (float*)d_ws;
    float *X = ws, *Y = X + S * D, *Q = Y + S * D, *K = Q + S * D, *V = K + S * DH, *O = V + S * DH, *Hh = O + S * D, *X2 = Hh + S * F;
    cvt_in<<<S * D / 256, 256, 0, stream>>>(x, X, S * D);
    for (int layer = 0; layer < 2; ++layer) {
        ln_kernel<<<S, 256, 0, stream>>>(X, Y);
        gemm_kernel<<<dim3(D / 64, S / 4), 256, 0, stream>>>(Y, wq, nullptr, nullptr, Q, S, D, D, 0, 0);
        gemm_kernel<<<dim3(1, S / 4), 256, 0, stream>>>(Y, wk, nullptr, nullptr, K, S, DH, D, 0, 0);
        gemm_kernel<<<dim3(1, S / 4), 256, 0, stream>>>(Y, wv, nullptr, nullptr, V, S, DH, D, 0, 0);
        attn_kernel<<<S, 32, 0, stream>>>(Q, K, V, O);
        gemm_kernel<<<dim3(D / 64, S / 4), 256, 0, stream>>>(O, wo, nullptr, X, X2, S, D, D, 0, 1);
        ln_kernel<<<S, 256, 0, stream>>>(X2, Y);
        gemm_kernel<<<dim3((F + 63) / 64, S / 4), 256, 0, stream>>>(Y, w1, b1, nullptr, Hh, S, F, D, 1, 0);
        gemm_kernel<<<dim3(D / 64, S / 4), 256, 0, stream>>>(Hh, w2, b2, X2, X, S, D, F, 0, 1);
    }
    cvt_out<<<S * D / 256, 256, 0, stream>>>(X, (bf16*)d_out, S * D);
}
```

```cpp
#include <hip/hip_runtime.h>
#include <hip/hip_bf16.h>
#include <stdint.h>

#define GAS __attribute__((address_space(1)))
#define LAS __attribute__((address_space(3)))
typedef __bf16 bf16x8 __attribute__((ext_vector_type(8)));
typedef __bf16 bf16x2 __attribute__((ext_vector_type(2)));
typedef float f32x16 __attribute__((ext_vector_type(16)));
typedef float f32x4 __attribute__((ext_vector_type(4)));
typedef float f32x2 __attribute__((ext_vector_type(2)));
typedef unsigned u32x4 __attribute__((ext_vector_type(4)));
typedef GAS unsigned gu32;
#define RLX_AGENT __ATOMIC_RELAXED, __HIP_MEMORY_SCOPE_AGENT

namespace {

constexpr int S = 2048, D = 512, F = 1376;
constexpr int KSD = 32;
constexpr int KSF = 86;
constexpr int KSFP = 88;
constexpr float LN_EPS = 1e-5f;
constexpr float QSCALE = 0.25f * 1.4426950408889634f;
constexpr float SAFE_BOUND = 50.f;

constexpr int OFF_CNT = 0;
constexpr int CNT_BYTES = 65536;
constexpr int OFF_WQ = OFF_CNT + CNT_BYTES;
constexpr int OFF_WKV = OFF_WQ + 16 * KSD * 1024;
constexpr int OFF_WO = OFF_WKV + KSD * 1024;
constexpr int OFF_W1 = OFF_WO + 16 * KSD * 1024;
constexpr int OFF_W2 = OFF_W1 + 43 * KSD * 1024;
constexpr int OFF_CSQ = OFF_W2 + 16 * KSFP * 1024;
constexpr int OFF_CSKV = OFF_CSQ + 512 * 4;
constexpr int OFF_CS1 = OFF_CSKV + 128 * 4;
constexpr int OFF_B1F = OFF_CS1 + 1408 * 4;
constexpr int OFF_B2F = OFF_B1F + 1408 * 4;
constexpr int OFF_KMAX = OFF_B2F + 512 * 4;
constexpr int OFF_KF = (OFF_KMAX + 2 * 64 * 4 + 1023) / 1024 * 1024;
constexpr int OFF_VF = OFF_KF + 2 * 65536;
constexpr int OFF_O = OFF_VF + 2 * 65536;
constexpr int ACT_BYTES = 64 * KSD * 1024;
constexpr int OFF_X1 = OFF_O + 2 * ACT_BYTES;
constexpr int OFF_X2 = OFF_X1 + 2 * ACT_BYTES;
constexpr int H_BYTES = 64 * KSF * 1024;
constexpr int OFF_H = OFF_X2 + ACT_BYTES;
constexpr int WS_USED = OFF_H + 2 * H_BYTES;
constexpr int CW = 64;
constexpr int CNT_O = 0, CNT_X1 = 1, CNT_H = 2, CNT_X2 = 3;
__device__ __forceinline__ int cnt_word(int layer, int kind, int c) { return ((layer * 4 + kind) * 32 + c) * CW; }
constexpr int OFF_CNT2 = WS_USED;
constexpr int CNT2_BYTES = 33 * 256;
constexpr int WS_TOTAL = OFF_CNT2 + CNT2_BYTES;
constexpr unsigned SPIN_CAP = 1u << 15;

constexpr int L_K = 0, L_V = 65536, L_ONES = 131072, L_XCH = 135424;
constexpr int L_RED = 0, L_ACT = 0, L_W2 = 65536, L_KVRED = 65536;
constexpr int L_STAT = 155648, L_TAB = 159744;
constexpr int LDS_BYTES = 163840;
constexpr int W2_REG = 11;

struct Params {
    const uint16_t *x, *wq, *wk, *wv, *wo, *w1, *b1, *w2, *b2;
    uint16_t* out;
    unsigned char* ws;
};

__device__ __forceinline__ f32x16 mfma(u32x4 a, u32x4 b, f32x16 c) {
    return __builtin_amdgcn_mfma_f32_32x32x16_bf16(__builtin_bit_cast(bf16x8, a), __builtin_bit_cast(bf16x8, b), c, 0, 0, 0);
}
__device__ __forceinline__ unsigned pk(float a, float b) { f32x2 v = {a, b}; return __builtin_bit_cast(unsigned, __builtin_convertvector(v, bf16x2)); }
__device__ __forceinline__ float bflo(unsigned u) { return __uint_as_float(u << 16); }
__device__ __forceinline__ float bfhi(unsigned u) { return __uint_as_float(u & 0xffff0000u); }
__device__ __forceinline__ float bf2f(uint16_t v) { return __uint_as_float((unsigned)v << 16); }
__device__ __forceinline__ float dot2(unsigned a, unsigned b, float c) {
    return __builtin_amdgcn_fdot2_f32_bf16(__builtin_bit_cast(bf16x2, a), __builtin_bit_cast(bf16x2, b), c, false);
}
__device__ __forceinline__ float half_sum(float v) {
    auto r = __builtin_amdgcn_permlane32_swap(__float_as_uint(v), __float_as_uint(v), false, false);
    return __uint_as_float(r[0]) + __uint_as_float(r[1]);
}
__device__ __forceinline__ float half_max(float v) {
    auto r = __builtin_amdgcn_permlane32_swap(__float_as_uint(v), __float_as_uint(v), false, false);
    return fmaxf(__uint_as_float(r[0]), __uint_as_float(r[1]));
}
__device__ __forceinline__ u32x4 swap_order(u32x4 a) {
    auto r0 = __builtin_amdgcn_permlane32_swap(a[0], a[2], false, false);
    auto r1 = __builtin_amdgcn_permlane32_swap(a[1], a[3], false, false);
    return (u32x4){r0[0], r1[0], r0[1], r1[1]};
}
__device__ __forceinline__ float wave_max(float v) {
#pragma unroll
    for (int m = 1; m < 32; m <<= 1) v = fmaxf(v, __shfl_xor(v, m));
    return half_max(v);
}
#define SBAR() __builtin_amdgcn_sched_barrier(0)
#define LDS_BARRIER() asm volatile("s_waitcnt lgkmcnt(0)\n\ts_barrier" ::: "memory")
#define DRAIN() asm volatile("s_waitcnt vmcnt(0)" ::: "memory")
constexpr int AUX_SC1 = 16;
typedef __amdgpu_buffer_rsrc_t rsrc_t;
__device__ __forceinline__ u32x4 ld_sc1(rsrc_t r, int voff, int soff) { return __builtin_amdgcn_raw_buffer_load_b128(r, voff, soff, AUX_SC1); }
__device__ __forceinline__ u32x4 ld_plain(rsrc_t r, int voff, int soff) { return __builtin_amdgcn_raw_buffer_load_b128(r, voff, soff, 0); }
__device__ __forceinline__ void st_sc1(u32x4 v, rsrc_t r, int voff, int soff) { __builtin_amdgcn_raw_buffer_store_b128(v, r, voff, soff, AUX_SC1); }
__device__ __forceinline__ u32x4 lds_ld(LAS char* l, int off) { return *(LAS u32x4*)(l + off); }
__device__ __forceinline__ void lds_st(LAS char* l, int off, u32x4 v) { *(LAS u32x4*)(l + off) = v; }
__device__ __forceinline__ f32x4 lds_ldf(LAS char* l, int off) { return *(LAS f32x4*)(l + off); }
__device__ __forceinline__ void lds_stf(LAS char* l, int off, f32x4 v) { *(LAS f32x4*)(l + off) = v; }

__device__ __forceinline__ u32x4 gather_frag(const uint16_t* w, int ld, int k0, int col) {
    unsigned e[8];
#pragma unroll
    for (int j = 0; j < 8; ++j) e[j] = w[(size_t)(k0 + j) * ld + col];
    return (u32x4){e[0] | (e[1] << 16), e[2] | (e[3] << 16), e[4] | (e[5] << 16), e[6] | (e[7] << 16)};
}
__device__ __forceinline__ float frag_sum(u32x4 f) {
    return ((bflo(f[0]) + bfhi(f[0])) + (bflo(f[1]) + bfhi(f[1]))) + ((bflo(f[2]) + bfhi(f[2])) + (bflo(f[3]) + bfhi(f[3])));
}

__global__ void __launch_bounds__(512) prep_kernel(Params p) {
    __shared__ __attribute__((aligned(16))) char lds_[49152 + 4096];
    LAS char* lds = (LAS char*)lds_;
    const int b = blockIdx.x, tid = threadIdx.x, lane = tid & 63, r = lane & 31, h = lane >> 5;
    const int w = __builtin_amdgcn_readfirstlane(tid >> 6);
    unsigned char* ws = p.ws;
    if (b < 64) {
        u32x4 xf[4], wf[4];
#pragma unroll
        for (int i = 0; i < 4; ++i) xf[i] = *(const u32x4*)(p.x + (size_t)(32 * b + r) * D + 16 * (4 * w + i) + 8 * h);
        const uint16_t* wsrc = (r < 16) ? p.wk : p.wv;
#pragma unroll
        for (int i = 0; i < 4; ++i) wf[i] = gather_frag(wsrc, 16, 16 * (4 * w + i) + 8 * h, r & 15);
        float s1 = 0.f, s2 = 0.f;
#pragma unroll
        for (int i = 0; i < 4; ++i)
#pragma unroll
            for (int d = 0; d < 4; ++d) { s1 = dot2(xf[i][d], 0x3f803f80u, s1); s2 = dot2(xf[i][d], xf[i][d], s2); }
        s1 = half_sum(s1); s2 = half_sum(s2);
        LAS f32x2* st = (LAS f32x2*)(lds + 49152);
        if (h == 0) st[w * 32 + r] = (f32x2){s1, s2};
        __syncthreads();
        float t1 = 0.f, t2 = 0.f;
#pragma unroll
        for (int i = 0; i < 8; ++i) { f32x2 v = st[i * 32 + r]; t1 += v[0]; t2 += v[1]; }
        const float mu = t1 * (1.f / D), var = fmaxf(t2 * (1.f / D) - mu * mu, 0.f), rstd = 1.f / sqrtf(var + LN_EPS);
        f32x16 ak = {}, av = {};
#pragma unroll
        for (int i = 0; i < 4; ++i) {
            u32x4 xn;
#pragma unroll
            for (int d = 0; d < 4; ++d) xn[d] = pk((bflo(xf[i][d]) - mu) * rstd, (bfhi(xf[i][d]) - mu) * rstd);
            ak = mfma(wf[i], xn, ak);
            av = mfma(xn, wf[i], av);
        }
        lds_stf(lds, w * 2048 + lane * 16, (f32x4){ak[0], ak[1], ak[2], ak[3]});
        lds_stf(lds, w * 2048 + 1024 + lane * 16, (f32x4){ak[4], ak[5], ak[6], ak[7]});
#pragma unroll
        for (int g = 0; g < 4; ++g) lds_stf(lds, 16384 + w * 4096 + g * 1024 + lane * 16, (f32x4){av[4 * g], av[4 * g + 1], av[4 * g + 2], av[4 * g + 3]});
        __syncthreads();
        if (w < 3) {
            f32x4 a = {}, c = {};
            const int base = (w == 0) ? 0 : 16384 + (w - 1) * 2048, stride = (w == 0) ? 2048 : 4096;
#pragma unroll
            for (int i = 0; i < 8; ++i) { a += lds_ldf(lds, base + i * stride + lane * 16); c += lds_ldf(lds, base + i * stride + 1024 + lane * 16); }
            const u32x4 fr = {pk(a[0], a[1]), pk(a[2], a[3]), pk(c[0], c[1]), pk(c[2], c[3])};
            if (w == 0) {
                *(u32x4*)(ws + OFF_KF + b * 1024 + lane * 16) = fr;
                float n2 = (a[0] * a[0] + a[1] * a[1]) + (a[2] * a[2] + a[3] * a[3]) + (c[0] * c[0] + c[1] * c[1]) + (c[2] * c[2] + c[3] * c[3]);
                n2 = wave_max(half_sum(n2));
                if (lane == 0) *(float*)(ws + OFF_KMAX + b * 4) = n2;
            } else if (r >= 16) {
                *(u32x4*)(ws + OFF_VF + (2 * b + (w - 1)) * 512 + h * 256 + (r - 16) * 16) = fr;
            }
        }
    } else if (b < 124) {
        const int u = b - 64;
        const uint16_t* src; int ld, col, dst, cs;
        if (u < 16) { src = p.wq; ld = 512; col = 32 * u + r; dst = OFF_WQ + u * KSD * 1024; cs = OFF_CSQ + u * 128; }
        else if (u == 16) { src = (r < 16) ? p.wk : p.wv; ld = 16; col = r & 15; dst = OFF_WKV; cs = OFF_CSKV; }
        else { src = p.w1; ld = F; col = 32 * (u - 17) + r; dst = OFF_W1 + (u - 17) * KSD * 1024; cs = OFF_CS1 + (u - 17) * 128; }
        u32x4 f[4];
#pragma unroll
        for (int i = 0; i < 4; ++i) f[i] = gather_frag(src, ld, 16 * (4 * w + i) + 8 * h, col);
        float s = 0.f;
#pragma unroll
        for (int i = 0; i < 4; ++i) { *(u32x4*)(ws + dst + (4 * w + i) * 1024 + lane * 16) = f[i]; s += frag_sum(f[i]); }
        s = half_sum(s);
        LAS float* st = (LAS float*)lds;
        if (h == 0) st[w * 32 + r] = s;
        __syncthreads();
        if (tid < 32) { float t = 0.f;
#pragma unroll
            for (int i = 0; i < 8; ++i) t += st[i * 32 + tid];
            *(float*)(ws + cs + tid * 4) = t; }
    } else if (b < 156) {
        const int u = b - 124, cb = u >> 1;
#pragma unroll
        for (int i = 0; i < 2; ++i) { const int s = 16 * (u & 1) + 2 * w + i;
            *(u32x4*)(ws + OFF_WO + (cb * KSD + s) * 1024 + lane * 16) = gather_frag(p.wo, D, 16 * s + 8 * h, 32 * cb + r); }
    } else if (b < 244) {
        const int u = b - 156;
#pragma unroll
        for (int i = 0; i < 2; ++i) { const int f = 16 * u + 2 * w + i, cb = f / KSFP, s = f % KSFP;
            u32x4 v = {0u, 0u, 0u, 0u};
            if (s < KSF) v = gather_frag(p.w2, D, 16 * s + 8 * h, 32 * cb + r);
            *(u32x4*)(ws + OFF_W2 + f * 1024 + lane * 16) = v; }
    } else if (b < 252) {
        *(u32x4*)(ws + OFF_CNT + (b - 244) * 8192 + tid * 16) = (u32x4){0u, 0u, 0u, 0u};
    } else if (b == 252) {
        for (int i = tid; i < CNT2_BYTES / 4; i += 512) *(unsigned*)(ws + OFF_CNT2 + i * 4) = 0u;
    } else if (b == 253) {
        for (int i = tid; i < 1408; i += 512) *(float*)(ws + OFF_B1F + i * 4) = i < F ? bf2f(p.b1[i]) : 0.f;
        *(float*)(ws + OFF_B2F + tid * 4) = bf2f(p.b2[tid]);
    }
}

__device__ __forceinline__ void publish(gu32* cnt) {
    DRAIN();
    LDS_BARRIER();
    if (threadIdx.x == 0) __hip_atomic_fetch_add(cnt, 1u, RLX_AGENT);
}
__device__ __forceinline__ void wait_cnt(gu32* cnt, unsigned want, gu32* tmo) {
    if (threadIdx.x == 0) {
        unsigned spins = 0;
        while (__hip_atomic_load(cnt, RLX_AGENT) < want) {
            if (++spins > SPIN_CAP) { __hip_atomic_store(tmo, 1u, RLX_AGENT); break; }
        }
    }
    __builtin_amdgcn_fence(__ATOMIC_ACQUIRE, "wavefront");
    LDS_BARRIER();
}
__device__ __forceinline__ void glds16(const void* g, unsigned lds_dst) {
    unsigned keep;
    asm volatile("s_mov_b32 %0, m0\n\ts_mov_b32 m0, %2\n\ts_nop 0\n\tglobal_load_lds_dwordx4 %1, off\n\ts_mov_b32 m0, %0" : "=&s"(keep) : "v"(g), "s"(lds_dst) : "memory");
}

__device__ __forceinline__ void reduce4(LAS char* lds, const f32x16& aA, const f32x16& aB, int tb, int kq, int lane, float (&o)[8]) {
    const int base = L_RED + tb * 32768 + lane * 16;
    { const int a = base + (((kq ^ 1) * 4 + kq) * 2048);
      lds_stf(lds, a, (f32x4){aA[8], aA[9], aA[10], aA[11]}); lds_stf(lds, a + 1024, (f32x4){aA[12], aA[13], aA[14], aA[15]}); }
    { const int a = base + (((kq ^ 2) * 4 + kq) * 2048);
      lds_stf(lds, a, (f32x4){aB[0], aB[1], aB[2], aB[3]}); lds_stf(lds, a + 1024, (f32x4){aB[4], aB[5], aB[6], aB[7]}); }
    { const int a = base + (((kq ^ 3) * 4 + kq) * 2048);
      lds_stf(lds, a, (f32x4){aB[8], aB[9], aB[10], aB[11]}); lds_stf(lds, a + 1024, (f32x4){aB[12], aB[13], aB[14], aB[15]}); }
    LDS_BARRIER();
    f32x4 lo = {aA[0], aA[1], aA[2], aA[3]}, hi = {aA[4], aA[5], aA[6], aA[7]};
#pragma unroll
    for (int x = 1; x < 4; ++x) { const int a = base + ((kq * 4 + (kq ^ x)) * 2048); lo += lds_ldf(lds, a); hi += lds_ldf(lds, a + 1024); }
#pragma unroll
    for (int i = 0; i < 4; ++i) { o[i] = lo[i]; o[4 + i] = hi[i]; }
}

#define A_E1(a) "v_exp_f32 v" #a ", v" #a "\n\t"
#define A_E4(a, b, c, d) A_E1(a) A_E1(b) A_E1(c) A_E1(d)
#define A_CVT(d, a, b) "v_cvt_pk_bf16_f32 v" #d ", v" #a ", v" #b "\n\t"
#define A_DSK(off) "ds_read_b128 v[100:103], v120 offset:" #off "\n\t"
#define A_DSV0(off) "ds_read_b128 v[104:107], v121 offset:" #off "\n\t"
#define A_DSV1(off) "ds_read_b128 v[108:111], v121 offset:" #off "\n\t"
#define A_WAIT(n) "s_waitcnt lgkmcnt(" #n ")\n\t"
#define A_MSB "v_mfma_f32_32x32x16_bf16 v[48:63], v[100:103], v[156:159], v[124:139]\n\t"
#define A_MSA "v_mfma_f32_32x32x16_bf16 v[32:47], v[100:103], v[96:99], v[64:79]\n\t"
#define A_MPV(O, V, P) "v_mfma_f32_32x32x16_bf16 " O ", " V ", " P ", " O "\n\t"
#define A_OA "v[80:95]"
#define A_OB "v[140:155]"
#define A_V0 "v[104:107]"
#define A_V1 "v[108:111]"
#define A_P0 "v[112:115]"
#define A_P1 "v[116:119]"
#define A_STEP(koff, v1off, v0off) \
    A_MSB A_DSK(koff) A_E4(32, 33, 34, 35) \
    A_MPV(A_OB, A_V1, A_P1) A_DSV1(v1off) A_E4(36, 37, 38, 39) \
    A_CVT(112, 32, 33) A_CVT(113, 34, 35) A_CVT(114, 36, 37) A_CVT(115, 38, 39) \
    A_E4(40, 41, 42, 43) \
    A_WAIT(2) A_MPV(A_OA, A_V0, A_P0) A_E4(44, 45, 46, 47) \
    A_CVT(116, 40, 41) A_CVT(117, 42, 43) A_CVT(118, 44, 45) A_CVT(119, 46, 47) \
    A_WAIT(1) A_MSA A_E4(48, 49, 50, 51) \
    A_WAIT(0) A_MPV(A_OA, A_V1, A_P1) A_E4(52, 53, 54, 55) \
    A_CVT(112, 48, 49) A_CVT(113, 50, 51) A_CVT(114, 52, 53) A_CVT(115, 54, 55) \
    A_E4(56, 57, 58, 59) \
    A_MPV(A_OB, A_V0, A_P0) A_DSV0(v0off) A_E4(60, 61, 62, 63) \
    A_CVT(116, 56, 57) A_CVT(117, 58, 59) A_CVT(118, 60, 61) A_CVT(119, 62, 63)

__device__ __forceinline__ void attn_loop(LAS char* lds, u32x4 qa, u32x4 qb, float ma, float mb, unsigned kaddr, unsigned vaddr, unsigned vstride, f32x16& oa, f32x16& ob) {
    f32x16 negma, negmb;
#pragma unroll
    for (int i = 0; i < 16; ++i) { negma[i] = -ma; negmb[i] = -mb; }
    u32x4 kf = lds_ld(lds, kaddr), v0 = lds_ld(lds, vaddr), v1 = v0, p0, p1 = {0u, 0u, 0u, 0u};
    f32x16 sa = mfma(kf, qa, negma), sb = sa;
    oa = (f32x16){}; ob = (f32x16){};
    unsigned cnt = 8;
    const unsigned lbase = (unsigned)(uintptr_t)lds;
    kaddr += lbase; vaddr += lbase;
    asm volatile(
        "s_nop 15\n\t"
        "ATT_LOOP_%=:\n\t"
        A_STEP(1024, 512, 1024)
        A_STEP(2048, 1536, 2048)
        A_STEP(3072, 2560, 3072)
        A_STEP(4096, 3584, 4096)
        "v_add_u32 v120, 4096, v120\n\t"
        "v_add_u32 v121, v122, v121\n\t"
        "s_sub_u32 %[cnt], %[cnt], 1\n\t"
        "s_cmp_lg_u32 %[cnt], 0\n\t"
        "s_cbranch_scc1 ATT_LOOP_%=\n\t"
        "s_waitcnt lgkmcnt(0)\n\t"
        A_MPV(A_OB, A_V1, A_P1)
        "s_nop 15\n\t"
        : "+{v[32:47]}"(sa), "+{v[48:63]}"(sb), "+{v[80:95]}"(oa), "+{v[140:155]}"(ob), "+{v[100:103]}"(kf), "+{v[104:107]}"(v0), "+{v[108:111]}"(v1),
          "={v[112:115]}"(p0), "+{v[116:119]}"(p1), "+{v120}"(kaddr), "+{v121}"(vaddr), [cnt] "+s"(cnt)
        : "{v[64:79]}"(negma), "{v[124:139]}"(negmb), "{v[96:99]}"(qa), "{v[156:159]}"(qb), "{v122}"(vstride)
        : "memory", "scc");
}

__global__ void __launch_bounds__(512, 2) main_kernel(Params p) {
    extern __shared__ __attribute__((aligned(16))) char lds_dyn[];
    LAS char* lds = (LAS char*)lds_dyn;
    const unsigned lbase = (unsigned)(uintptr_t)lds;
    const int tid0 = threadIdx.x, w0 = __builtin_amdgcn_readfirstlane(tid0 >> 6);
    const int kk0 = blockIdx.x >> 3, c0 = (blockIdx.x & 7) * 4 + (kk0 >> 3), j0 = kk0 & 7;
    const rsrc_t rw = __builtin_amdgcn_make_buffer_rsrc(p.ws, 0, WS_TOTAL, 0x00020000);
    const rsrc_t rx = __builtin_amdgcn_make_buffer_rsrc((void*)p.x, 0, S * D * 2, 0x00020000);
    gu32* cnt = (gu32*)(p.ws + OFF_CNT);
    gu32* cnt2 = (gu32*)(p.ws + OFF_CNT2);
    gu32* tmo = cnt2 + 32 * CW;
#define PHASE_IDS() \
    int tid = tid0, w = w0, c = c0, j = j0; \
    asm volatile("" : "+v"(tid)); asm volatile("" : "+s"(w), "+s"(c), "+s"(j)); \
    const int lane = tid & 63, r = lane & 31, h = lane >> 5, tb = w >> 2, kq = w & 3, tbg = 2 * c + tb; \
    const int vl = lane * 16;                                     \
    const int vw = (lane ^ ((kq & 1) << 4)) * 16;                 \
    const int cbA = 2 * j + (kq >> 1), cbB = 2 * j + ((kq >> 1) ^ 1); \
    const int sown = 4 * j + kq;                                  \
    (void)r; (void)h; (void)tbg; (void)vl; (void)vw; (void)cbA; (void)cbB; (void)sown; (void)tb;

    u32x4 resid;
    { PHASE_IDS(); resid = swap_order(ld_plain(rx, r * 1024 + h * 16, (64 * c + 32 * tb) * 1024 + sown * 32)); }

#pragma unroll 1
    for (int L = 0; L < 2; ++L) {
        u32x4 qown; float q2own;
        {
            PHASE_IDS();
            if (L == 1) wait_cnt(cnt + cnt_word(0, CNT_X2, c), 8u, tmo);
            const rsrc_t ra = L == 0 ? rx : rw;
            const int avo = L == 0 ? r * 1024 + h * 16 : vl;
            const int aso = L == 0 ? (64 * c + 32 * tb) * 1024 + kq * 8 * 32 : OFF_X2 + (tbg * KSD + kq * 8) * 1024;
            const int astr = L == 0 ? 32 : 1024;
            u32x4 xa[8], wA[8], wB[8];
#pragma unroll
            for (int i = 0; i < 8; ++i) xa[i] = ld_sc1(ra, avo, aso + i * astr);
#pragma unroll
            for (int i = 0; i < 8; ++i) { wA[i] = ld_plain(rw, vw, OFF_WQ + (cbA * KSD + kq * 8 + i) * 1024); wB[i] = ld_plain(rw, vw, OFF_WQ + (cbB * KSD + kq * 8 + i) * 1024); }
            const bool kvteam = (L == 1) && (j == tb);
            const f32x4 cs0 = *(const GAS f32x4*)(p.ws + OFF_CSQ + (16 * sown + 4 * h) * 4), cs1 = *(const GAS f32x4*)(p.ws + OFF_CSQ + (16 * sown + 8 + 4 * h) * 4);
            SBAR();
            float s1 = 0.f, s2 = 0.f;
            f32x16 aA = {}, aB = {};
            if (kvteam) {
                u32x4 wk[8];
#pragma unroll
                for (int i = 0; i < 8; ++i) wk[i] = ld_plain(rw, vl, OFF_WKV + (kq * 8 + i) * 1024);
                f32x16 ak = {}, av = {};
#pragma unroll
                for (int i = 0; i < 8; ++i) { ak = mfma(wk[i], xa[i], ak); av = mfma(xa[i], wk[i], av); }
                const int a = L_KVRED + kq * 6144 + vl;
                lds_stf(lds, a, (f32x4){ak[0], ak[1], ak[2], ak[3]}); lds_stf(lds, a + 1024, (f32x4){ak[4], ak[5], ak[6], ak[7]});
#pragma unroll
                for (int g = 0; g < 4; ++g) lds_stf(lds, a + 2048 + g * 1024, (f32x4){av[4 * g], av[4 * g + 1], av[4 * g + 2], av[4 * g + 3]});
            }
#pragma unroll
            for (int i = 0; i < 8; ++i) {
#pragma unroll
                for (int d = 0; d < 4; ++d) { s1 = dot2(xa[i][d], 0x3f803f80u, s1); s2 = dot2(xa[i][d], xa[i][d], s2); }
                aA = mfma(wA[i], xa[i], aA); aB = mfma(wB[i], xa[i], aB);
            }
            s1 = half_sum(s1); s2 = half_sum(s2);
            LAS f32x2* st = (LAS f32x2*)(lds + L_STAT);
            if (h == 0) st[(tb * 8 + kq) * 32 + r] = (f32x2){s1, s2};
            float q[8];
            reduce4(lds, aA, aB, tb, kq, lane, q);
            float t1 = 0.f, t2 = 0.f;
#pragma unroll
            for (int i = 0; i < 4; ++i) { f32x2 v = st[(tb * 8 + i) * 32 + r]; t1 += v[0]; t2 += v[1]; }
            const float mu = t1 * (1.f / D), rstd = 1.f / sqrtf(fmaxf(t2 * (1.f / D) - mu * mu, 0.f) + LN_EPS);
            if (kvteam && kq < 3) {
                f32x4 a = {}, e = {};
                const int base = L_KVRED + vl + (kq == 0 ? 0 : 2048 + (kq - 1) * 2048);
#pragma unroll
                for (int i = 0; i < 4; ++i) { a += lds_ldf(lds, base + i * 6144); e += lds_ldf(lds, base + i * 6144 + 1024); }
                if (kq == 0) {
                    const f32x4 k0 = *(const GAS f32x4*)(p.ws + OFF_CSKV + (4 * h) * 4), k1 = *(const GAS f32x4*)(p.ws + OFF_CSKV + (8 + 4 * h) * 4);
#pragma unroll
                    for (int i = 0; i < 4; ++i) { a[i] = (a[i] - mu * k0[i]) * rstd; e[i] = (e[i] - mu * k1[i]) * rstd; }
                    st_sc1((u32x4){pk(a[0], a[1]), pk(a[2], a[3]), pk(e[0], e[1]), pk(e[2], e[3])}, rw, vl, OFF_KF + 65536 + tbg * 1024);
                    float n2 = (a[0] * a[0] + a[1] * a[1]) + (a[2] * a[2] + a[3] * a[3]) + (e[0] * e[0] + e[1] * e[1]) + (e[2] * e[2] + e[3] * e[3]);
                    n2 = wave_max(half_sum(n2));
                    if (lane == 0) __hip_atomic_store((gu32*)(p.ws + OFF_KMAX + (64 + tbg) * 4), __float_as_uint(n2), RLX_AGENT);
                } else {
                    const float csv = *(const GAS float*)(p.ws + OFF_CSKV + r * 4);
                    const int g = kq - 1;
#pragma unroll
                    for (int i = 0; i < 8; ++i) {
                        const int tok = 16 * g + (i & 3) + 8 * (i >> 2) + 4 * h;
                        float u1 = 0.f, u2 = 0.f;
#pragma unroll
                        for (int x = 0; x < 4; ++x) { f32x2 v = st[(tb * 8 + x) * 32 + tok]; u1 += v[0]; u2 += v[1]; }
                        const float m2 = u1 * (1.f / D), r2 = 1.f / sqrtf(fmaxf(u2 * (1.f / D) - m2 * m2, 0.f) + LN_EPS);
                        if (i < 4) a[i] = (a[i] - m2 * csv) * r2; else e[i - 4] = (e[i - 4] - m2 * csv) * r2;
                    }
                    if (r >= 16) st_sc1((u32x4){pk(a[0], a[1]), pk(a[2], a[3]), pk(e[0], e[1]), pk(e[2], e[3])}, rw, h * 256 + (r - 16) * 16, OFF_VF + 65536 + (2 * tbg + g) * 512);
                }
            }
            if (L == 1 && j < 2) {
                DRAIN();
                LDS_BARRIER();
                if (tid < 32) __hip_atomic_fetch_add(cnt2 + tid * CW, 1u, RLX_AGENT);
            }
            float n2 = 0.f;
#pragma unroll
            for (int i = 0; i < 4; ++i) { q[i] = (q[i] - mu * cs0[i]) * (rstd * QSCALE); q[4 + i] = (q[4 + i] - mu * cs1[i]) * (rstd * QSCALE); }
#pragma unroll
            for (int i = 0; i < 8; ++i) n2 += q[i] * q[i];
            q2own = half_sum(n2);
            qown = (u32x4){pk(q[0], q[1]), pk(q[2], q[3]), pk(q[4], q[5]), pk(q[6], q[7])};
        }
        u32x4 woA[8], woB[8];
        {
            PHASE_IDS();
            lds_st(lds, L_XCH + w * 2048 + vl, qown);
            *(LAS float*)(lds + L_XCH + w * 2048 + 1024 + lane * 4) = q2own;
            if (tid < 272) lds_st(lds, L_ONES + tid * 16, (u32x4){0x3f803f80u, 0x3f803f80u, 0x3f803f80u, 0x3f803f80u});
            if (L == 1) wait_cnt(cnt2 + c * CW, 64u, tmo); else LDS_BARRIER();
            u32x4 stg[16];
#pragma unroll
            for (int i = 0; i < 8; ++i) stg[i] = ld_sc1(rw, tid * 16, OFF_KF + L * 65536 + i * 8192);
#pragma unroll
            for (int i = 0; i < 8; ++i) stg[8 + i] = ld_sc1(rw, tid * 16, OFF_VF + L * 65536 + i * 8192);
            const float km2 = wave_max(__uint_as_float(__builtin_amdgcn_raw_buffer_load_b32(rw, lane * 4, OFF_KMAX + L * 256, AUX_SC1)));
            const u32x4 qpar = lds_ld(lds, L_XCH + (w ^ 1) * 2048 + vl);
            const float q2par = *(LAS float*)(lds + L_XCH + (w ^ 1) * 2048 + 1024 + lane * 4);
#pragma unroll
            for (int i = 0; i < 8; ++i) lds_st(lds, L_K + i * 8192 + tid * 16, stg[i]);
#pragma unroll
            for (int i = 0; i < 8; ++i) lds_st(lds, L_V + i * 8192 + tid * 16, stg[8 + i]);
            SBAR();
#pragma unroll
            for (int i = 0; i < 8; ++i) { woA[i] = ld_plain(rw, vw, OFF_WO + (cbA * KSD + kq * 8 + i) * 1024); woB[i] = ld_plain(rw, vw, OFF_WO + (cbB * KSD + kq * 8 + i) * 1024); }
            SBAR();
            LDS_BARRIER();
            float ma = sqrtf(q2own * km2) * 1.02f, mb = sqrtf(q2par * km2) * 1.02f;
            if (__any(!(ma <= SAFE_BOUND) || !(mb <= SAFE_BOUND))) {
                float xa = -INFINITY, xb = -INFINITY;
#pragma unroll 1
                for (int t = 0; t < 64; ++t) {
                    const u32x4 kf = lds_ld(lds, L_K + t * 1024 + vl);
                    const f32x16 sa = mfma(kf, qown, (f32x16){}), sb = mfma(kf, qpar, (f32x16){});
#pragma unroll
                    for (int i = 0; i < 16; ++i) { xa = fmaxf(xa, sa[i]); xb = fmaxf(xb, sb[i]); }
                }
                ma = half_max(xa); mb = half_max(xb);
            }
            const int kh = kq & 1;
            const bool real = r < 16;
            const unsigned kaddr = L_K + kh * 32768 + vl;
            const unsigned vaddr = real ? (unsigned)(L_V + kh * 32768 + h * 256 + r * 16) : (unsigned)(L_ONES + ((r >= 20 && r < 28) ? 64 : 0));
            f32x16 oa, ob;
            attn_loop(lds, qown, qpar, ma, mb, kaddr, vaddr, real ? 4096u : 0u, oa, ob);
            const int xo = L_XCH + w * 2304;
            LDS_BARRIER();
            lds_stf(lds, xo + vl, (f32x4){ob[0], ob[1], ob[2], ob[3]}); lds_stf(lds, xo + 1024 + vl, (f32x4){ob[4], ob[5], ob[6], ob[7]});
            *(LAS float*)(lds + xo + 2048 + lane * 4) = ob[8];
            LDS_BARRIER();
            const int xi = L_XCH + (w ^ 1) * 2304;
            const f32x4 e0 = lds_ldf(lds, xi + vl), e1 = lds_ldf(lds, xi + 1024 + vl);
            const float rl = 1.f / (oa[8] + *(LAS float*)(lds + xi + 2048 + lane * 4));
            const u32x4 of = {pk((oa[0] + e0[0]) * rl, (oa[1] + e0[1]) * rl), pk((oa[2] + e0[2]) * rl, (oa[3] + e0[3]) * rl),
                              pk((oa[4] + e1[0]) * rl, (oa[5] + e1[1]) * rl), pk((oa[6] + e1[2]) * rl, (oa[7] + e1[3]) * rl)};
            st_sc1(swap_order(of), rw, vl, OFF_O + L * ACT_BYTES + (tbg * KSD + sown) * 1024);
            publish(cnt + cnt_word(L, CNT_O, c));
        }
#define W1_IDS() \
    const int ncb = j < 3 ? 6 : 5, cb0 = j < 3 ? 6 * j : 18 + 5 * (j - 3);     \
    const int cb1 = w < 4 ? w : 4 + ((w - 4) >> 1); \
    const bool d_active = cb1 < ncb; \
    const int cbg1 = cb0 + (d_active ? cb1 : 0); (void)cbg1;
        u32x4 w1f[32];
        {
            PHASE_IDS(); W1_IDS();
            {
                const unsigned char* src = p.ws + OFF_W2 + vl;
#pragma unroll
                for (int i = 0; i < 11; ++i) { const int f = w * 11 + i, qq = f / 22, rem = f % 22, cb = rem / 11, ks = rem % 11;
                    glds16(src + ((2 * j + cb) * KSFP + qq * 22 + W2_REG + ks) * 1024, lbase + L_W2 + f * 1024); }
            }
            wait_cnt(cnt + cnt_word(L, CNT_O, c), 8u, tmo);
            u32x4 xa[8];
#pragma unroll
            for (int i = 0; i < 8; ++i) xa[i] = ld_sc1(rw, vl, OFF_O + L * ACT_BYTES + (tbg * KSD + kq * 8 + i) * 1024);
#pragma unroll
            for (int i = 0; i < 16; ++i) w1f[i] = ld_plain(rw, vl, OFF_W1 + (cbg1 * KSD + i) * 1024);
            SBAR();
            f32x16 aA = {}, aB = {};
#pragma unroll
            for (int i = 0; i < 8; ++i) { aA = mfma(woA[i], xa[i], aA); aB = mfma(woB[i], xa[i], aB); }
            float o[8];
            reduce4(lds, aA, aB, tb, kq, lane, o);
#pragma unroll
            for (int i = 0; i < 4; ++i) resid[i] = pk(bflo(resid[i]) + o[2 * i], bfhi(resid[i]) + o[2 * i + 1]);
            st_sc1(swap_order(resid), rw, vl, OFF_X1 + L * ACT_BYTES + (tbg * KSD + sown) * 1024);
            publish(cnt + cnt_word(L, CNT_X1, c));
        }
        u32x4 w2A[W2_REG], w2B[W2_REG];
        {
            PHASE_IDS(); W1_IDS();
            if (tid < 192) {
                const int col = min(32 * cb0 + tid, 1407);
                *(LAS float*)(lds + L_TAB + tid * 4) = *(const GAS float*)(p.ws + OFF_CS1 + col * 4);
                *(LAS float*)(lds + L_TAB + 768 + tid * 4) = *(const GAS float*)(p.ws + OFF_B1F + col * 4);
            }
            wait_cnt(cnt + cnt_word(L, CNT_X1, c), 8u, tmo);
            u32x4 stg[2][2][2];
#pragma unroll
            for (int ch = 0; ch < 2; ++ch)
#pragma unroll
                for (int t = 0; t < 2; ++t)
#pragma unroll
                    for (int i = 0; i < 2; ++i) stg[ch][t][i] = ld_sc1(rw, vl, OFF_X1 + L * ACT_BYTES + ((2 * c + t) * KSD + 16 * ch + 2 * w + i) * 1024);
#pragma unroll
            for (int i = 16; i < 32; ++i) w1f[i] = ld_plain(rw, vl, OFF_W1 + (cbg1 * KSD + i) * 1024);
            SBAR();
            float s1[2] = {0.f, 0.f}, s2[2] = {0.f, 0.f};
#pragma unroll
            for (int t = 0; t < 2; ++t)
#pragma unroll
                for (int i = 0; i < 2; ++i) {
                    lds_st(lds, L_ACT + (t * KSD + 2 * w + i) * 1024 + vl, stg[0][t][i]);
#pragma unroll
                    for (int d = 0; d < 4; ++d) { s1[t] = dot2(stg[0][t][i][d], 0x3f803f80u, s1[t]); s2[t] = dot2(stg[0][t][i][d], stg[0][t][i][d], s2[t]); }
                }
            LDS_BARRIER();
            f32x16 a0 = {}, a1 = {};
            const int tw = w < 4 ? 0 : ((w - 4) & 1);
            if (d_active) {
                if (w < 4) {
#pragma unroll
                    for (int s = 0; s < 16; ++s) { a0 = mfma(w1f[s], lds_ld(lds, L_ACT + s * 1024 + vl), a0); a1 = mfma(w1f[s], lds_ld(lds, L_ACT + (KSD + s) * 1024 + vl), a1); }
                } else {
#pragma unroll
                    for (int s = 0; s < 16; ++s) a0 = mfma(w1f[s], lds_ld(lds, L_ACT + (tw * KSD + s) * 1024 + vl), a0);
                }
            }
#pragma unroll
            for (int t = 0; t < 2; ++t)
#pragma unroll
                for (int i = 0; i < 2; ++i) {
                    lds_st(lds, L_ACT + (t * KSD + 16 + 2 * w + i) * 1024 + vl, stg[1][t][i]);
#pragma unroll
                    for (int d = 0; d < 4; ++d) { s1[t] = dot2(stg[1][t][i][d], 0x3f803f80u, s1[t]); s2[t] = dot2(stg[1][t][i][d], stg[1][t][i][d], s2[t]); }
                }
            {
                LAS f32x2* st = (LAS f32x2*)(lds + L_STAT);
#pragma unroll
                for (int t = 0; t < 2; ++t) { const float u1 = half_sum(s1[t]), u2 = half_sum(s2[t]); if (h == 0) st[(t * 8 + w) * 32 + r] = (f32x2){u1, u2}; }
            }
#pragma unroll
            for (int i = 0; i < W2_REG; ++i) { w2A[i] = ld_plain(rw, vw, OFF_W2 + (cbA * KSFP + kq * 22 + i) * 1024); }
            SBAR();
            LDS_BARRIER();
            if (d_active) {
                if (w < 4) {
#pragma unroll
                    for (int s = 16; s < 32; ++s) { a0 = mfma(w1f[s], lds_ld(lds, L_ACT + s * 1024 + vl), a0); a1 = mfma(w1f[s], lds_ld(lds, L_ACT + (KSD + s) * 1024 + vl), a1); }
                } else {
#pragma unroll
                    for (int s = 16; s < 32; ++s) a0 = mfma(w1f[s], lds_ld(lds, L_ACT + (tw * KSD + s) * 1024 + vl), a0);
                }
            }
#pragma unroll
            for (int i = 0; i < W2_REG; ++i) { w2B[i] = ld_plain(rw, vw, OFF_W2 + (cbB * KSFP + kq * 22 + i) * 1024); }
            SBAR();
            if (d_active) {
                auto finish = [&](const f32x16& acc, int t) {
                    LAS f32x2* st = (LAS f32x2*)(lds + L_STAT);
                    float t1 = 0.f, t2 = 0.f;
#pragma unroll
                    for (int i = 0; i < 8; ++i) { f32x2 v = st[(t * 8 + i) * 32 + r]; t1 += v[0]; t2 += v[1]; }
                    const float mu = t1 * (1.f / D), rstd = 1.f / sqrtf(fmaxf(t2 * (1.f / D) - mu * mu, 0.f) + LN_EPS);
                    const float nmr = -mu * rstd;
                    unsigned hp[8];
#pragma unroll
                    for (int g = 0; g < 4; ++g) {
                        const f32x4 cs = lds_ldf(lds, L_TAB + (32 * cb1 + 8 * g + 4 * h) * 4), bb = lds_ldf(lds, L_TAB + 768 + (32 * cb1 + 8 * g + 4 * h) * 4);
                        float y[4];
#pragma unroll
                        for (int i = 0; i < 4; ++i) {
                            const float x = fmaf(acc[4 * g + i], rstd, fmaf(cs[i], nmr, bb[i]));
                            const float t = x * fmaf(x * x, -2.f * 0.7978845608028654f * 0.044715f * 1.4426950408889634f, -2.f * 0.7978845608028654f * 1.4426950408889634f);
                            y[i] = x * __builtin_amdgcn_rcpf(1.f + __builtin_amdgcn_exp2f(t));
                        }
                        hp[2 * g] = pk(y[0], y[1]); hp[2 * g + 1] = pk(y[2], y[3]);
                    }
                    const int so = OFF_H + L * H_BYTES + ((2 * c + t) * KSF + 2 * cbg1) * 1024;
                    st_sc1(swap_order((u32x4){hp[0], hp[1], hp[2], hp[3]}), rw, vl, so);
                    st_sc1(swap_order((u32x4){hp[4], hp[5], hp[6], hp[7]}), rw, vl, so + 1024);
                };
                if (w < 4) { finish(a0, 0); finish(a1, 1); } else finish(a0, tw);
            }
            publish(cnt + cnt_word(L, CNT_H, c));
        }
        {
            PHASE_IDS();
            const f32x4 bb0 = *(const GAS f32x4*)(p.ws + OFF_B2F + (16 * sown + 4 * h) * 4), bb1 = *(const GAS f32x4*)(p.ws + OFF_B2F + (16 * sown + 8 + 4 * h) * 4);
            wait_cnt(cnt + cnt_word(L, CNT_H, c), 8u, tmo);
            u32x4 ha[22];
#pragma unroll
            for (int i = 0; i < 22; ++i) { const int s = min(kq * 22 + i, KSF - 1); ha[i] = ld_sc1(rw, vl, OFF_H + L * H_BYTES + (tbg * KSF + s) * 1024); }
            SBAR();
            f32x16 aA = {}, aB = {};
#pragma unroll
            for (int i = 0; i < W2_REG; ++i) { aA = mfma(w2A[i], ha[i], aA); aB = mfma(w2B[i], ha[i], aB); }
            {
                const int oa_ = L_W2 + (kq * 22 + (kq >> 1) * 11) * 1024 + vw, ob_ = L_W2 + (kq * 22 + ((kq >> 1) ^ 1) * 11) * 1024 + vw;
#pragma unroll
                for (int i = 0; i < 11; ++i) { aA = mfma(lds_ld(lds, oa_ + i * 1024), ha[W2_REG + i], aA); aB = mfma(lds_ld(lds, ob_ + i * 1024), ha[W2_REG + i], aB); }
            }
            float o[8];
            reduce4(lds, aA, aB, tb, kq, lane, o);
#pragma unroll
            for (int i = 0; i < 4; ++i) { o[i] += bb0[i]; o[4 + i] += bb1[i]; }
#pragma unroll
            for (int i = 0; i < 4; ++i) resid[i] = pk(bflo(resid[i]) + o[2 * i], bfhi(resid[i]) + o[2 * i + 1]);
            const u32x4 xo = swap_order(resid);
            if (L == 0) {
                st_sc1(xo, rw, vl, OFF_X2 + (tbg * KSD + sown) * 1024);
                publish(cnt + cnt_word(0, CNT_X2, c));
            } else {
                *(GAS u32x4*)(p.out + (size_t)(64 * c + 32 * tb + r) * D + 16 * sown + 8 * h) = xo;
            }
        }
    }
}

}

extern "C" void kernel_launch(void* const* d_in, const int* in_sizes, int n_in, void* d_out, int out_size, void* d_ws, size_t ws_size, hipStream_t stream) {
    Params p;
    p.x = (const uint16_t*)d_in[0]; p.wq = (const uint16_t*)d_in[1]; p.wk = (const uint16_t*)d_in[2]; p.wv = (const uint16_t*)d_in[3]; p.wo = (const uint16_t*)d_in[4];
    p.w1 = (const uint16_t*)d_in[6]; p.b1 = (const uint16_t*)d_in[7]; p.w2 = (const uint16_t*)d_in[8]; p.b2 = (const uint16_t*)d_in[9];
    p.out = (uint16_t*)d_out; p.ws = (unsigned char*)d_ws;
    (void)hipFuncSetAttribute((const void*)main_kernel, hipFuncAttributeMaxDynamicSharedMemorySize, LDS_BYTES);
    hipLaunchKernelGGL(prep_kernel, dim3(256), dim3(512), 0, stream, p);
    hipLaunchKernelGGL(main_kernel, dim3(256), dim3(512), LDS_BYTES, stream, p);
}
```

```cpp
#include <hip/hip_runtime.h>
#include <hip/hip_bf16.h>
#include <stdint.h>

#define GAS __attribute__((address_space(1)))
#define LAS __attribute__((address_space(3)))
typedef __bf16 bf16x8 __attribute__((ext_vector_type(8)));
typedef __bf16 bf16x2 __attribute__((ext_vector_type(2)));
typedef float f32x16 __attribute__((ext_vector_type(16)));
typedef float f32x4 __attribute__((ext_vector_type(4)));
typedef float f32x2 __attribute__((ext_vector_type(2)));
typedef unsigned u32x4 __attribute__((ext_vector_type(4)));
typedef GAS unsigned gu32;
#define RLX_AGENT __ATOMIC_RELAXED, __HIP_MEMORY_SCOPE_AGENT

namespace {

constexpr int S = 2048, D = 512, F = 1376;
constexpr int KSD = 32;
constexpr int KSF = 86;
constexpr int KSFP = 88;
constexpr float LN_EPS = 1e-5f;
constexpr float QSCALE = 0.25f * 1.4426950408889634f;
constexpr float SAFE_BOUND = 50.f;

constexpr int OFF_CNT = 0;
constexpr int CNT_BYTES = 65536;
constexpr int OFF_WQ = OFF_CNT + CNT_BYTES;
constexpr int OFF_WKV = OFF_WQ + 16 * KSD * 1024;
constexpr int OFF_WO = OFF_WKV + KSD * 1024;
constexpr int OFF_W1 = OFF_WO + 16 * KSD * 1024;
constexpr int OFF_W2 = OFF_W1 + 43 * KSD * 1024;
constexpr int OFF_CSQ = OFF_W2 + 16 * KSFP * 1024;
constexpr int OFF_CSKV = OFF_CSQ + 512 * 4;
constexpr int OFF_CS1 = OFF_CSKV + 128 * 4;
constexpr int OFF_B1F = OFF_CS1 + 1408 * 4;
constexpr int OFF_B2F = OFF_B1F + 1408 * 4;
constexpr int OFF_KMAX = OFF_B2F + 512 * 4;
constexpr int OFF_KF = (OFF_KMAX + 2 * 64 * 4 + 1023) / 1024 * 1024;
constexpr int OFF_VF = OFF_KF + 2 * 65536;
constexpr int OFF_O = OFF_VF + 2 * 65536;
constexpr int ACT_BYTES = 64 * KSD * 1024;
constexpr int OFF_X1 = OFF_O + 2 * ACT_BYTES;
constexpr int OFF_X2 = OFF_X1 + 2 * ACT_BYTES;
constexpr int H_BYTES = 64 * KSF * 1024;
constexpr int OFF_H = OFF_X2 + ACT_BYTES;
constexpr int WS_USED = OFF_H + 2 * H_BYTES;
constexpr int CW = 64;
constexpr int CNT_O = 0, CNT_X1 = 1, CNT_H = 2, CNT_X2 = 3;
__device__ __forceinline__ int cnt_word(int layer, int kind, int c) { return ((layer * 4 + kind) * 32 + c) * CW; }
constexpr int OFF_CNT2 = WS_USED;
constexpr int CNT2_BYTES = 33 * 256;
constexpr int WS_TOTAL = OFF_CNT2 + CNT2_BYTES;
constexpr unsigned SPIN_CAP = 1u << 15;

constexpr int L_K = 0, L_V = 65536, L_ONES = 131072, L_XCH = 135424;
constexpr int L_RED = 0, L_ACT = 0, L_W2 = 65536, L_REDA = 65536, L_KVRED = 0;
constexpr int L_STAT = 155648, L_TAB = 159744;
constexpr int LDS_BYTES = 163840;
constexpr int W2_REG = 11;

#define PROF 0
#define PROF_TID 0
constexpr int L_PROF = 161280;
constexpr int OFF_PROF = 64 << 20;
#define STAMP(i) do { } while (0)

struct Params {
    const uint16_t *x, *wq, *wk, *wv, *wo, *w1, *b1, *w2, *b2;
    uint16_t* out;
    unsigned char* ws;
};

__device__ __forceinline__ f32x16 mfma(u32x4 a, u32x4 b, f32x16 c) {
    return __builtin_amdgcn_mfma_f32_32x32x16_bf16(__builtin_bit_cast(bf16x8, a), __builtin_bit_cast(bf16x8, b), c, 0, 0, 0);
}
__device__ __forceinline__ unsigned pk(float a, float b) { f32x2 v = {a, b}; return __builtin_bit_cast(unsigned, __builtin_convertvector(v, bf16x2)); }
__device__ __forceinline__ float bflo(unsigned u) { return __uint_as_float(u << 16); }
__device__ __forceinline__ float bfhi(unsigned u) { return __uint_as_float(u & 0xffff0000u); }
__device__ __forceinline__ float bf2f(uint16_t v) { return __uint_as_float((unsigned)v << 16); }
__device__ __forceinline__ float dot2(unsigned a, unsigned b, float c) {
    return __builtin_amdgcn_fdot2_f32_bf16(__builtin_bit_cast(bf16x2, a), __builtin_bit_cast(bf16x2, b), c, false);
}
__device__ __forceinline__ float half_sum(float v) {
    auto r = __builtin_amdgcn_permlane32_swap(__float_as_uint(v), __float_as_uint(v), false, false);
    return __uint_as_float(r[0]) + __uint_as_float(r[1]);
}
__device__ __forceinline__ float half_max(float v) {
    auto r = __builtin_amdgcn_permlane32_swap(__float_as_uint(v), __float_as_uint(v), false, false);
    return fmaxf(__uint_as_float(r[0]), __uint_as_float(r[1]));
}
__device__ __forceinline__ u32x4 swap_order(u32x4 a) {
    auto r0 = __builtin_amdgcn_permlane32_swap(a[0], a[2], false, false);
    auto r1 = __builtin_amdgcn_permlane32_swap(a[1], a[3], false, false);
    return (u32x4){r0[0], r1[0], r0[1], r1[1]};
}
__device__ __forceinline__ float wave_max(float v) {
#pragma unroll
    for (int m = 1; m < 32; m <<= 1) v = fmaxf(v, __shfl_xor(v, m));
    return half_max(v);
}
#define SBAR() __builtin_amdgcn_sched_barrier(0)
#define PIN2(a, b) asm volatile("" : "+v"(a), "+v"(b))
#define LDS_BARRIER() asm volatile("s_waitcnt lgkmcnt(0)\n\ts_barrier" ::: "memory")
#define DRAIN() asm volatile("s_waitcnt vmcnt(0)" ::: "memory")
constexpr int AUX_SC1 = 16;
typedef __amdgpu_buffer_rsrc_t rsrc_t;
__device__ __forceinline__ u32x4 ld_sc1(rsrc_t r, int voff, int soff) { return __builtin_amdgcn_raw_buffer_load_b128(r, voff, soff, AUX_SC1); }
__device__ __forceinline__ u32x4 ld_plain(rsrc_t r, int voff, int soff) { return __builtin_amdgcn_raw_buffer_load_b128(r, voff, soff, 0); }
__device__ __forceinline__ void st_sc1(u32x4 v, rsrc_t r, int voff, int soff) { __builtin_amdgcn_raw_buffer_store_b128(v, r, voff, soff, AUX_SC1); }
__device__ __forceinline__ u32x4 lds_ld(LAS char* l, int off) { return *(LAS u32x4*)(l + off); }
__device__ __forceinline__ void lds_st(LAS char* l, int off, u32x4 v) { *(LAS u32x4*)(l + off) = v; }
__device__ __forceinline__ f32x4 lds_ldf(LAS char* l, int off) { return *(LAS f32x4*)(l + off); }
__device__ __forceinline__ void lds_stf(LAS char* l, int off, f32x4 v) { *(LAS f32x4*)(l + off) = v; }

__device__ __forceinline__ u32x4 gather_frag(const uint16_t* w, int ld, int k0, int col) {
    unsigned e[8];
#pragma unroll
    for (int j = 0; j < 8; ++j) e[j] = w[(size_t)(k0 + j) * ld + col];
    return (u32x4){e[0] | (e[1] << 16), e[2] | (e[3] << 16), e[4] | (e[5] << 16), e[6] | (e[7] << 16)};
}
__device__ __forceinline__ float frag_sum(u32x4 f) {
    return ((bflo(f[0]) + bfhi(f[0])) + (bflo(f[1]) + bfhi(f[1]))) + ((bflo(f[2]) + bfhi(f[2])) + (bflo(f[3]) + bfhi(f[3])));
}

__global__ void __launch_bounds__(512) prep_kernel(Params p) {
    __shared__ __attribute__((aligned(16))) char lds_[49152 + 4096];
    LAS char* lds = (LAS char*)lds_;
    const int b = blockIdx.x, tid = threadIdx.x, lane = tid & 63, r = lane & 31, h = lane >> 5;
    const int w = __builtin_amdgcn_readfirstlane(tid >> 6);
    unsigned char* ws = p.ws;
    if (b < 64) {
        u32x4 xf[4], wf[4];
#pragma unroll
        for (int i = 0; i < 4; ++i) xf[i] = *(const u32x4*)(p.x + (size_t)(32 * b + r) * D + 16 * (4 * w + i) + 8 * h);
        const uint16_t* wsrc = (r < 16) ? p.wk : p.wv;
#pragma unroll
        for (int i = 0; i < 4; ++i) wf[i] = gather_frag(wsrc, 16, 16 * (4 * w + i) + 8 * h, r & 15);
        float s1 = 0.f, s2 = 0.f;
#pragma unroll
        for (int i = 0; i < 4; ++i)
#pragma unroll
            for (int d = 0; d < 4; ++d) { s1 = dot2(xf[i][d], 0x3f803f80u, s1); s2 = dot2(xf[i][d], xf[i][d], s2); }
        s1 = half_sum(s1); s2 = half_sum(s2);
        LAS f32x2* st = (LAS f32x2*)(lds + 49152);
        if (h == 0) st[w * 32 + r] = (f32x2){s1, s2};
        __syncthreads();
        float t1 = 0.f, t2 = 0.f;
#pragma unroll
        for (int i = 0; i < 8; ++i) { f32x2 v = st[i * 32 + r]; t1 += v[0]; t2 += v[1]; }
        const float mu = t1 * (1.f / D), var = fmaxf(t2 * (1.f / D) - mu * mu, 0.f), rstd = 1.f / sqrtf(var + LN_EPS);
        f32x16 ak = {}, av = {};
#pragma unroll
        for (int i = 0; i < 4; ++i) {
            u32x4 xn;
#pragma unroll
            for (int d = 0; d < 4; ++d) xn[d] = pk((bflo(xf[i][d]) - mu) * rstd, (bfhi(xf[i][d]) - mu) * rstd);
            ak = mfma(wf[i], xn, ak);
            av = mfma(xn, wf[i], av);
        }
        lds_stf(lds, w * 2048 + lane * 16, (f32x4){ak[0], ak[1], ak[2], ak[3]});
        lds_stf(lds, w * 2048 + 1024 + lane * 16, (f32x4){ak[4], ak[5], ak[6], ak[7]});
#pragma unroll
        for (int g = 0; g < 4; ++g) lds_stf(lds, 16384 + w * 4096 + g * 1024 + lane * 16, (f32x4){av[4 * g], av[4 * g + 1], av[4 * g + 2], av[4 * g + 3]});
        __syncthreads();
        if (w < 3) {
            f32x4 a = {}, c = {};
            const int base = (w == 0) ? 0 : 16384 + (w - 1) * 2048, stride = (w == 0) ? 2048 : 4096;
#pragma unroll
            for (int i = 0; i < 8; ++i) { a += lds_ldf(lds, base + i * stride + lane * 16); c += lds_ldf(lds, base + i * stride + 1024 + lane * 16); }
            const u32x4 fr = {pk(a[0], a[1]), pk(a[2], a[3]), pk(c[0], c[1]), pk(c[2], c[3])};
            if (w == 0) {
                *(u32x4*)(ws + OFF_KF + b * 1024 + lane * 16) = fr;
                float n2 = (a[0] * a[0] + a[1] * a[1]) + (a[2] * a[2] + a[3] * a[3]) + (c[0] * c[0] + c[1] * c[1]) + (c[2] * c[2] + c[3] * c[3]);
                n2 = wave_max(half_sum(n2));
                if (lane == 0) *(float*)(ws + OFF_KMAX + b * 4) = n2;
            } else if (r >= 16) {
                *(u32x4*)(ws + OFF_VF + (2 * b + (w - 1)) * 512 + h * 256 + (r - 16) * 16) = fr;
            }
        }
    } else if (b < 124) {
        const int u = b - 64;
        const uint16_t* src; int ld, col, dst, cs;
        if (u < 16) { src = p.wq; ld = 512; col = 32 * u + r; dst = OFF_WQ + u * KSD * 1024; cs = OFF_CSQ + u * 128; }
        else if (u == 16) { src = (r < 16) ? p.wk : p.wv; ld = 16; col = r & 15; dst = OFF_WKV; cs = OFF_CSKV; }
        else { src = p.w1; ld = F; col = 32 * (u - 17) + r; dst = OFF_W1 + (u - 17) * KSD * 1024; cs = OFF_CS1 + (u - 17) * 128; }
        u32x4 f[4];
#pragma unroll
        for (int i = 0; i < 4; ++i) f[i] = gather_frag(src, ld, 16 * (4 * w + i) + 8 * h, col);
        float s = 0.f;
#pragma unroll
        for (int i = 0; i < 4; ++i) { *(u32x4*)(ws + dst + (4 * w + i) * 1024 + lane * 16) = f[i]; s += frag_sum(f[i]); }
        s = half_sum(s);
        LAS float* st = (LAS float*)lds;
        if (h == 0) st[w * 32 + r] = s;
        __syncthreads();
        if (tid < 32) { float t = 0.f;
#pragma unroll
            for (int i = 0; i < 8; ++i) t += st[i * 32 + tid];
            *(float*)(ws + cs + tid * 4) = t; }
    } else if (b < 156) {
        const int u = b - 124, cb = u >> 1;
#pragma unroll
        for (int i = 0; i < 2; ++i) { const int s = 16 * (u & 1) + 2 * w + i;
            *(u32x4*)(ws + OFF_WO + (cb * KSD + s) * 1024 + lane * 16) = gather_frag(p.wo, D, 16 * s + 8 * h, 32 * cb + r); }
    } else if (b < 244) {
        const int u = b - 156;
#pragma unroll
        for (int i = 0; i < 2; ++i) { const int f = 16 * u + 2 * w + i, cb = f / KSFP, s = f % KSFP;
            u32x4 v = {0u, 0u, 0u, 0u};
            if (s < KSF) v = gather_frag(p.w2, D, 16 * s + 8 * h, 32 * cb + r);
            *(u32x4*)(ws + OFF_W2 + f * 1024 + lane * 16) = v; }
    } else if (b < 252) {
        *(u32x4*)(ws + OFF_CNT + (b - 244) * 8192 + tid * 16) = (u32x4){0u, 0u, 0u, 0u};
    } else if (b == 252) {
        for (int i = tid; i < CNT2_BYTES / 4; i += 512) *(unsigned*)(ws + OFF_CNT2 + i * 4) = 0u;
    } else if (b == 253) {
        for (int i = tid; i < 1408; i += 512) *(float*)(ws + OFF_B1F + i * 4) = i < F ? bf2f(p.b1[i]) : 0.f;
        *(float*)(ws + OFF_B2F + tid * 4) = bf2f(p.b2[tid]);
    }
}

__device__ __forceinline__ void publish(gu32* cnt) {
    DRAIN();
    LDS_BARRIER();
    if (threadIdx.x == 0) __hip_atomic_fetch_add(cnt, 1u, RLX_AGENT);
}
__device__ __forceinline__ void wait_cnt(gu32* cnt, unsigned want, gu32* tmo) {
    if (threadIdx.x == 0) {
        unsigned spins = 0;
        while (__hip_atomic_load(cnt, RLX_AGENT) < want) {
            if (++spins > SPIN_CAP) { __hip_atomic_store(tmo, 1u, RLX_AGENT); break; }
        }
    }
    __builtin_amdgcn_fence(__ATOMIC_ACQUIRE, "wavefront");
    LDS_BARRIER();
}
__device__ __forceinline__ void glds16(const void* g, unsigned lds_dst) {
    unsigned keep;
    asm volatile("s_mov_b32 %0, m0\n\ts_mov_b32 m0, %2\n\ts_nop 0\n\tglobal_load_lds_dwordx4 %1, off\n\ts_mov_b32 m0, %0" : "=&s"(keep) : "v"(g), "s"(lds_dst) : "memory");
}
__device__ __forceinline__ void glds16_sc1(const void* g, unsigned lds_dst) {
    unsigned keep;
    asm volatile("s_mov_b32 %0, m0\n\ts_mov_b32 m0, %2\n\ts_nop 0\n\tglobal_load_lds_dwordx4 %1, off sc1\n\ts_mov_b32 m0, %0" : "=&s"(keep) : "v"(g), "s"(lds_dst) : "memory");
}
#define WAITVM(n) asm volatile("s_waitcnt vmcnt(" #n ")" ::: "memory")

__device__ __forceinline__ void reduce4(LAS char* lds, int region, const f32x16& aA, const f32x16& aB, int tb, int kq, int lane, float (&o)[8]) {
    const int base = region + tb * 32768 + lane * 16;
    { const int a = base + (((kq ^ 1) * 4 + kq) * 2048);
      lds_stf(lds, a, (f32x4){aA[8], aA[9], aA[10], aA[11]}); lds_stf(lds, a + 1024, (f32x4){aA[12], aA[13], aA[14], aA[15]}); }
    { const int a = base + (((kq ^ 2) * 4 + kq) * 2048);
      lds_stf(lds, a, (f32x4){aB[0], aB[1], aB[2], aB[3]}); lds_stf(lds, a + 1024, (f32x4){aB[4], aB[5], aB[6], aB[7]}); }
    { const int a = base + (((kq ^ 3) * 4 + kq) * 2048);
      lds_stf(lds, a, (f32x4){aB[8], aB[9], aB[10], aB[11]}); lds_stf(lds, a + 1024, (f32x4){aB[12], aB[13], aB[14], aB[15]}); }
    LDS_BARRIER();
    f32x4 lo = {aA[0], aA[1], aA[2], aA[3]}, hi = {aA[4], aA[5], aA[6], aA[7]};
#pragma unroll
    for (int x = 1; x < 4; ++x) { const int a = base + ((kq * 4 + (kq ^ x)) * 2048); lo += lds_ldf(lds, a); hi += lds_ldf(lds, a + 1024); }
#pragma unroll
    for (int i = 0; i < 4; ++i) { o[i] = lo[i]; o[4 + i] = hi[i]; }
}

#define A_E1(a) "v_exp_f32 v" #a ", v" #a "\n\t"
#define A_E4(a, b, c, d) A_E1(a) A_E1(b) A_E1(c) A_E1(d)
#define A_CVT(d, a, b) "v_cvt_pk_bf16_f32 v" #d ", v" #a ", v" #b "\n\t"
#define A_DSK(off) "ds_read_b128 v[100:103], v120 offset:" #off "\n\t"
#define A_DSV0(off) "ds_read_b128 v[104:107], v121 offset:" #off "\n\t"
#define A_DSV1(off) "ds_read_b128 v[108:111], v121 offset:" #off "\n\t"
#define A_WAIT(n) "s_waitcnt lgkmcnt(" #n ")\n\t"
#define A_MSB "v_mfma_f32_32x32x16_bf16 v[48:63], v[100:103], v[156:159], v[124:139]\n\t"
#define A_MSA "v_mfma_f32_32x32x16_bf16 v[32:47], v[100:103], v[96:99], v[64:79]\n\t"
#define A_MPV(O, V, P) "v_mfma_f32_32x32x16_bf16 " O ", " V ", " P ", " O "\n\t"
#define A_OA "v[80:95]"
#define A_OB "v[140:155]"
#define A_V0 "v[104:107]"
#define A_V1 "v[108:111]"
#define A_P0 "v[112:115]"
#define A_P1 "v[116:119]"
#define A_STEP(koff, v1off, v0off) \
    A_MSB A_DSK(koff) A_E4(32, 33, 34, 35) \
    A_MPV(A_OB, A_V1, A_P1) A_DSV1(v1off) A_E4(36, 37, 38, 39) \
    A_CVT(112, 32, 33) A_CVT(113, 34, 35) A_CVT(114, 36, 37) A_CVT(115, 38, 39) \
    A_E4(40, 41, 42, 43) \
    A_WAIT(2) A_MPV(A_OA, A_V0, A_P0) A_E4(44, 45, 46, 47) \
    A_CVT(116, 40, 41) A_CVT(117, 42, 43) A_CVT(118, 44, 45) A_CVT(119, 46, 47) \
    A_WAIT(1) A_MSA A_E4(48, 49, 50, 51) \
    A_WAIT(0) A_MPV(A_OA, A_V1, A_P1) A_E4(52, 53, 54, 55) \
    A_CVT(112, 48, 49) A_CVT(113, 50, 51) A_CVT(114, 52, 53) A_CVT(115, 54, 55) \
    A_E4(56, 57, 58, 59) \
    A_MPV(A_OB, A_V0, A_P0) A_DSV0(v0off) A_E4(60, 61, 62, 63) \
    A_CVT(116, 56, 57) A_CVT(117, 58, 59) A_CVT(118, 60, 61) A_CVT(119, 62, 63)

struct AttnState { f32x16 sa, sb, oa, ob, negma, negmb; u32x4 qa, qb, kf, v0, v1, p1; unsigned kaddr, vaddr, vstride; };
#define A_OPERANDS(st, cnt) \
        : "+{v[32:47]}"(st.sa), "+{v[48:63]}"(st.sb), "+{v[80:95]}"(st.oa), "+{v[140:155]}"(st.ob), "+{v[100:103]}"(st.kf), "+{v[104:107]}"(st.v0), "+{v[108:111]}"(st.v1), \
          "={v[112:115]}"(p0), "+{v[116:119]}"(st.p1), "+{v120}"(st.kaddr), "+{v121}"(st.vaddr), [n] "+s"(cnt) \
        : "{v[64:79]}"(st.negma), "{v[124:139]}"(st.negmb), "{v[96:99]}"(st.qa), "{v[156:159]}"(st.qb), "{v122}"(st.vstride) \
        : "memory", "scc"
#define A_ITER \
        A_STEP(1024, 512, 1024) \
        A_STEP(2048, 1536, 2048) \
        A_STEP(3072, 2560, 3072) \
        A_STEP(4096, 3584, 4096) \
        "v_add_u32 v120, 4096, v120\n\t" \
        "v_add_u32 v121, v122, v121\n\t"
__device__ __forceinline__ void attn_begin(AttnState& st, LAS char* lds, u32x4 qa, u32x4 qb, float ma, float mb, unsigned kaddr, unsigned vaddr, unsigned vstride) {
#pragma unroll
    for (int i = 0; i < 16; ++i) { st.negma[i] = -ma; st.negmb[i] = -mb; }
    st.qa = qa; st.qb = qb;
    st.kf = lds_ld(lds, kaddr); st.v0 = lds_ld(lds, vaddr); st.v1 = st.v0; st.p1 = (u32x4){0u, 0u, 0u, 0u};
    st.sa = mfma(st.kf, qa, st.negma); st.sb = st.sa;
    st.oa = (f32x16){}; st.ob = (f32x16){};
    const unsigned lbase = (unsigned)(uintptr_t)lds;
    st.kaddr = kaddr + lbase; st.vaddr = vaddr + lbase; st.vstride = vstride;
}
__device__ __forceinline__ void attn_first(AttnState& st) {
    unsigned cnt = 4; u32x4 p0;
    asm volatile(
        "s_nop 15\n\t"
        "ATT1_LOOP_%=:\n\t"
        A_ITER
        "s_cmp_lg_u32 %[n], 3\n\t"
        "s_cbranch_scc1 ATT1_GO_%=\n\t"
        "s_waitcnt vmcnt(0)\n\t"
        "s_barrier\n\t"
        "ATT1_GO_%=:\n\t"
        "s_sub_u32 %[n], %[n], 1\n\t"
        "s_cmp_lg_u32 %[n], 0\n\t"
        "s_cbranch_scc1 ATT1_LOOP_%=\n\t"
        A_OPERANDS(st, cnt));
}
__device__ __forceinline__ void attn_second(AttnState& st) {
    unsigned cnt = 4; u32x4 p0;
    asm volatile(
        "ATT2_LOOP_%=:\n\t"
        A_ITER
        "s_sub_u32 %[n], %[n], 1\n\t"
        "s_cmp_lg_u32 %[n], 0\n\t"
        "s_cbranch_scc1 ATT2_LOOP_%=\n\t"
        "s_waitcnt lgkmcnt(0)\n\t"
        A_MPV(A_OB, A_V1, A_P1)
        "s_nop 15\n\t"
        A_OPERANDS(st, cnt));
}

__device__ __forceinline__ void stats_acc(u32x4 f, float& s1, float& s2) {
#pragma unroll
    for (int d = 0; d < 4; ++d) { s1 = dot2(f[d], 0x3f803f80u, s1); s2 = dot2(f[d], f[d], s2); }
}
__device__ __forceinline__ void ln_from_sums(float t1, float t2, float& mu, float& rstd) {
    mu = t1 * (1.f / D);
    rstd = __builtin_amdgcn_rsqf(fmaxf(t2 * (1.f / D) - mu * mu, 0.f) + LN_EPS);
}

template <int TB>
__device__ __forceinline__ void run_layers(const Params& p, LAS char* lds) {
    const unsigned lbase = (unsigned)(uintptr_t)lds;
    const int tid0 = threadIdx.x, w0 = __builtin_amdgcn_readfirstlane(tid0 >> 6);
    const int kk0 = blockIdx.x >> 3, c0 = (blockIdx.x & 7) * 4 + (kk0 >> 3), j0 = kk0 & 7;
    const rsrc_t rw = __builtin_amdgcn_make_buffer_rsrc(p.ws, 0, WS_TOTAL, 0x00020000);
    const rsrc_t rx = __builtin_amdgcn_make_buffer_rsrc((void*)p.x, 0, S * D * 2, 0x00020000);
    gu32* cnt = (gu32*)(p.ws + OFF_CNT);
    gu32* cnt2 = (gu32*)(p.ws + OFF_CNT2);
    gu32* tmo = cnt2 + 32 * CW;
#define PHASE_IDS() \
    int tid = tid0, w = w0, c = c0, j = j0; \
    asm volatile("" : "+v"(tid)); asm volatile("" : "+s"(w), "+s"(c), "+s"(j)); \
    constexpr int tb = TB; const int lane = tid & 63, r = lane & 31, h = lane >> 5, kq = w & 3, tbg = 2 * c + tb; \
    const int vl = lane * 16;                                     \
    const int vw = (lane ^ ((kq & 1) << 4)) * 16;                 \
    const int cbA = 2 * j + (kq >> 1), cbB = 2 * j + ((kq >> 1) ^ 1); \
    const int sown = 4 * j + kq;                                  \
    (void)r; (void)h; (void)tbg; (void)vl; (void)vw; (void)cbA; (void)cbB; (void)sown; (void)tb;
#define W1_IDS() \
    const int ncb = j < 3 ? 6 : 5, cb0 = j < 3 ? 6 * j : 18 + 5 * (j - 3);     \
    const int cb1 = TB == 0 ? kq : 4 + (kq >> 1);     \
    const bool d_active = cb1 < ncb; \
    const int cbg1 = cb0 + (d_active ? cb1 : 0); (void)cbg1; \
    const int par = TB == 0 ? 0 : (kq & 1); (void)par;
#define LOAD_WQ() do { _Pragma("unroll") for (int i = 0; i < 8; ++i) { wqA[i] = ld_plain(rw, vw, OFF_WQ + (cbA * KSD + kq * 8 + i) * 1024); wqB[i] = ld_plain(rw, vw, OFF_WQ + (cbB * KSD + kq * 8 + i) * 1024); } SBAR(); } while (0)
#define LOAD_W1(lo) do { \
        _Pragma("unroll") for (int i = 0; i < 8; ++i) w1f[(lo) + i] = ld_plain(rw, vl, OFF_W1 + (cbg1 * KSD + (lo) + (TB == 0 ? i : 2 * i + par)) * 1024); \
        if (TB == 0) { _Pragma("unroll") for (int i = 8; i < 16; ++i) w1f[(lo) + i] = ld_plain(rw, vl, OFF_W1 + (cbg1 * KSD + (lo) + i) * 1024); } \
        SBAR(); } while (0)

    STAMP(0);
    u32x4 resid;
    { PHASE_IDS(); resid = swap_order(ld_plain(rx, r * 1024 + h * 16, (64 * c + 32 * tb) * 1024 + sown * 32)); }

#pragma unroll 1
    for (int L = 0; L < 2; ++L) {
        u32x4 qown; float q2own;
        {
            PHASE_IDS();
            u32x4 wqA[8], wqB[8];
            LOAD_WQ();
            if (L == 1) wait_cnt(cnt + cnt_word(0, CNT_X2, c), 8u, tmo);
            STAMP(1 + L * 32);
            const rsrc_t ra = L == 0 ? rx : rw;
            const int avo = L == 0 ? r * 1024 + h * 16 : vl;
            const int aso = L == 0 ? (64 * c + 32 * tb) * 1024 + kq * 8 * 32 : OFF_X2 + (tbg * KSD + kq * 8) * 1024;
            const int astr = L == 0 ? 32 : 1024;
            u32x4 xa[8];
#pragma unroll
            for (int i = 0; i < 8; ++i) xa[i] = ld_sc1(ra, avo, aso + i * astr);
            SBAR();
            const bool kvteam = (L == 1) && (j == tb);
            const f32x4 cs0 = *(const GAS f32x4*)(p.ws + OFF_CSQ + (16 * sown + 4 * h) * 4), cs1 = *(const GAS f32x4*)(p.ws + OFF_CSQ + (16 * sown + 8 + 4 * h) * 4);
            SBAR();
            STAMP(2 + L * 32);
            float s1 = 0.f, s2 = 0.f;
            f32x16 aA = {}, aB = {};
            if (kvteam) {
                u32x4 wk[8];
#pragma unroll
                for (int i = 0; i < 8; ++i) wk[i] = ld_plain(rw, vl, OFF_WKV + (kq * 8 + i) * 1024);
                f32x16 ak = {}, av = {};
#pragma unroll
                for (int i = 0; i < 8; ++i) { ak = mfma(wk[i], xa[i], ak); av = mfma(xa[i], wk[i], av); }
                const int a = L_KVRED + kq * 6144 + vl;
                lds_stf(lds, a, (f32x4){ak[0], ak[1], ak[2], ak[3]}); lds_stf(lds, a + 1024, (f32x4){ak[4], ak[5], ak[6], ak[7]});
#pragma unroll
                for (int g = 0; g < 4; ++g) lds_stf(lds, a + 2048 + g * 1024, (f32x4){av[4 * g], av[4 * g + 1], av[4 * g + 2], av[4 * g + 3]});
            }
#pragma unroll
            for (int i = 0; i < 8; ++i) {
                stats_acc(xa[i], s1, s2);
                aA = mfma(wqA[i], xa[i], aA); aB = mfma(wqB[i], xa[i], aB);
            }
            STAMP(3 + L * 32);
            s1 = half_sum(s1); s2 = half_sum(s2);
            LAS f32x2* st = (LAS f32x2*)(lds + L_STAT);
            if (h == 0) st[(tb * 8 + kq) * 32 + r] = (f32x2){s1, s2};
            float q[8];
            reduce4(lds, L_REDA, aA, aB, tb, kq, lane, q);
            STAMP(4 + L * 32);
            float t1 = 0.f, t2 = 0.f;
#pragma unroll
            for (int i = 0; i < 4; ++i) { f32x2 v = st[(tb * 8 + i) * 32 + r]; t1 += v[0]; t2 += v[1]; }
            float mu, rstd; ln_from_sums(t1, t2, mu, rstd);
            if (kvteam && kq < 3) {
                f32x4 a = {}, e = {};
                const int base = L_KVRED + vl + (kq == 0 ? 0 : 2048 + (kq - 1) * 2048);
#pragma unroll
                for (int i = 0; i < 4; ++i) { a += lds_ldf(lds, base + i * 6144); e += lds_ldf(lds, base + i * 6144 + 1024); }
                if (kq == 0) {
                    const f32x4 k0 = *(const GAS f32x4*)(p.ws + OFF_CSKV + (4 * h) * 4), k1 = *(const GAS f32x4*)(p.ws + OFF_CSKV + (8 + 4 * h) * 4);
#pragma unroll
                    for (int i = 0; i < 4; ++i) { a[i] = (a[i] - mu * k0[i]) * rstd; e[i] = (e[i] - mu * k1[i]) * rstd; }
                    st_sc1((u32x4){pk(a[0], a[1]), pk(a[2], a[3]), pk(e[0], e[1]), pk(e[2], e[3])}, rw, vl, OFF_KF + 65536 + tbg * 1024);
                    float n2 = (a[0] * a[0] + a[1] * a[1]) + (a[2] * a[2] + a[3] * a[3]) + (e[0] * e[0] + e[1] * e[1]) + (e[2] * e[2] + e[3] * e[3]);
                    n2 = wave_max(half_sum(n2));
                    if (lane == 0) __hip_atomic_store((gu32*)(p.ws + OFF_KMAX + (64 + tbg) * 4), __float_as_uint(n2), RLX_AGENT);
                } else {
                    const float csv = *(const GAS float*)(p.ws + OFF_CSKV + r * 4);
                    const int g = kq - 1;
#pragma unroll
                    for (int i = 0; i < 8; ++i) {
                        const int tok = 16 * g + (i & 3) + 8 * (i >> 2) + 4 * h;
                        float u1 = 0.f, u2 = 0.f;
#pragma unroll
                        for (int x = 0; x < 4; ++x) { f32x2 v = st[(tb * 8 + x) * 32 + tok]; u1 += v[0]; u2 += v[1]; }
                        float m2, r2; ln_from_sums(u1, u2, m2, r2);
                        if (i < 4) a[i] = (a[i] - m2 * csv) * r2; else e[i - 4] = (e[i - 4] - m2 * csv) * r2;
                    }
                    if (r >= 16) st_sc1((u32x4){pk(a[0], a[1]), pk(a[2], a[3]), pk(e[0], e[1]), pk(e[2], e[3])}, rw, h * 256 + (r - 16) * 16, OFF_VF + 65536 + (2 * tbg + g) * 512);
                }
            }
            STAMP(5 + L * 32);
            if (L == 1 && j < 2) {
                DRAIN();
                LDS_BARRIER();
                if (tid < 32) __hip_atomic_fetch_add(cnt2 + tid * CW, 1u, RLX_AGENT);
            }
            float n2 = 0.f;
#pragma unroll
            for (int i = 0; i < 4; ++i) { q[i] = (q[i] - mu * cs0[i]) * (rstd * QSCALE); q[4 + i] = (q[4 + i] - mu * cs1[i]) * (rstd * QSCALE); }
#pragma unroll
            for (int i = 0; i < 8; ++i) n2 += q[i] * q[i];
            q2own = half_sum(n2);
            qown = (u32x4){pk(q[0], q[1]), pk(q[2], q[3]), pk(q[4], q[5]), pk(q[6], q[7])};
        }
        u32x4 woA[8], woB[8];
        {
            PHASE_IDS();
            lds_st(lds, L_XCH + w * 2048 + vl, qown);
            *(LAS float*)(lds + L_XCH + w * 2048 + 1024 + lane * 4) = q2own;
            if (tid < 272) lds_st(lds, L_ONES + tid * 16, (u32x4){0x3f803f80u, 0x3f803f80u, 0x3f803f80u, 0x3f803f80u});
            STAMP(6 + L * 32);
            if (L == 1) wait_cnt(cnt2 + c * CW, 64u, tmo); else LDS_BARRIER();
            STAMP(7 + L * 32);
            const float kmx = __uint_as_float(__builtin_amdgcn_raw_buffer_load_b32(rw, lane * 4, OFF_KMAX + L * 256, AUX_SC1));
            SBAR();
            {
                const unsigned char* src = p.ws + (TB == 0 ? OFF_KF : OFF_VF) + L * 65536 + ((w >> 1) & 1) * 32768 + vl;
                const unsigned dst = lbase + (TB == 0 ? L_K : L_V) + ((w >> 1) & 1) * 32768;
                const int t0 = (w & 1) * 6, t1 = 12 + (w & 1) * 10;
#pragma unroll
                for (int i = 0; i < 6; ++i) glds16_sc1(src + (t0 + i) * 1024, dst + (t0 + i) * 1024);
#pragma unroll
                for (int i = 0; i < 10; ++i) glds16_sc1(src + (t1 + i) * 1024, dst + (t1 + i) * 1024);
            }
            const u32x4 qpar = lds_ld(lds, L_XCH + (w ^ 1) * 2048 + vl);
            const float q2par = *(LAS float*)(lds + L_XCH + (w ^ 1) * 2048 + 1024 + lane * 4);
            const float km2 = wave_max(kmx);
            float ma = __builtin_amdgcn_sqrtf(q2own * km2) * 1.02f, mb = __builtin_amdgcn_sqrtf(q2par * km2) * 1.02f;
            const bool unsafe = __any(!(ma <= SAFE_BOUND) || !(mb <= SAFE_BOUND));
            int* flag = nullptr; (void)flag;
            {
                LAS unsigned* fl = (LAS unsigned*)(lds + L_TAB);
                if (lane == 0) fl[w] = unsafe ? 1u : 0u;
            }
            STAMP(8 + L * 32);
            WAITVM(10);
            LDS_BARRIER();
            {
                LAS unsigned* fl = (LAS unsigned*)(lds + L_TAB);
                const u32x4 f0 = *(LAS u32x4*)fl, f1 = *(LAS u32x4*)(fl + 4);
                if (__builtin_amdgcn_readfirstlane(f0[0] | f0[1] | f0[2] | f0[3] | f1[0] | f1[1] | f1[2] | f1[3])) {
                    DRAIN();
                    LDS_BARRIER();
                    if (unsafe) {
                        float xa = -INFINITY, xb = -INFINITY;
#pragma unroll 1
                        for (int t = 0; t < 64; ++t) {
                            const u32x4 kf = lds_ld(lds, L_K + t * 1024 + vl);
                            const f32x16 sa = mfma(kf, qown, (f32x16){}), sb = mfma(kf, qpar, (f32x16){});
#pragma unroll
                            for (int i = 0; i < 16; ++i) { xa = fmaxf(xa, sa[i]); xb = fmaxf(xb, sb[i]); }
                        }
                        ma = half_max(xa); mb = half_max(xb);
                    }
                }
            }
            STAMP(9 + L * 32);
            const int kh = kq & 1;
            const bool real = r < 16;
            const unsigned kaddr = L_K + kh * 32768 + vl;
            const unsigned vaddr = real ? (unsigned)(L_V + kh * 32768 + h * 256 + r * 16) : (unsigned)(L_ONES + ((r >= 20 && r < 28) ? 64 : 0));
            AttnState st;
            attn_begin(st, lds, qown, qpar, ma, mb, kaddr, vaddr, real ? 4096u : 0u);
            attn_first(st);
            STAMP(10 + L * 32);
#pragma unroll
            for (int i = 0; i < 8; ++i) { woA[i] = ld_plain(rw, vw, OFF_WO + (cbA * KSD + kq * 8 + i) * 1024); woB[i] = ld_plain(rw, vw, OFF_WO + (cbB * KSD + kq * 8 + i) * 1024); }
            SBAR();
            STAMP(11 + L * 32);
            attn_second(st);
            STAMP(12 + L * 32);
            const f32x16 oa = st.oa, ob = st.ob;
            const int xo = L_XCH + w * 2304;
            lds_stf(lds, xo + vl, (f32x4){ob[0], ob[1], ob[2], ob[3]}); lds_stf(lds, xo + 1024 + vl, (f32x4){ob[4], ob[5], ob[6], ob[7]});
            *(LAS float*)(lds + xo + 2048 + lane * 4) = ob[8];
            LDS_BARRIER();
            const int xi = L_XCH + (w ^ 1) * 2304;
            const f32x4 e0 = lds_ldf(lds, xi + vl), e1 = lds_ldf(lds, xi + 1024 + vl);
            const float rl = __builtin_amdgcn_rcpf(oa[8] + *(LAS float*)(lds + xi + 2048 + lane * 4));
            const u32x4 of = {pk((oa[0] + e0[0]) * rl, (oa[1] + e0[1]) * rl), pk((oa[2] + e0[2]) * rl, (oa[3] + e0[3]) * rl),
                              pk((oa[4] + e1[0]) * rl, (oa[5] + e1[1]) * rl), pk((oa[6] + e1[2]) * rl, (oa[7] + e1[3]) * rl)};
            st_sc1(swap_order(of), rw, vl, OFF_O + L * ACT_BYTES + (tbg * KSD + sown) * 1024);
            publish(cnt + cnt_word(L, CNT_O, c));
            STAMP(13 + L * 32);
        }
        u32x4 w1f[32];
        {
            PHASE_IDS(); W1_IDS();
            LOAD_W1(0);
            STAMP(14 + L * 32);
            wait_cnt(cnt + cnt_word(L, CNT_O, c), 8u, tmo);
            STAMP(15 + L * 32);
            u32x4 xa[8];
#pragma unroll
            for (int i = 0; i < 8; ++i) xa[i] = ld_sc1(rw, vl, OFF_O + L * ACT_BYTES + (tbg * KSD + kq * 8 + i) * 1024);
            SBAR();
            f32x16 aA = {}, aB = {};
#pragma unroll
            for (int i = 0; i < 8; ++i) { aA = mfma(woA[i], xa[i], aA); aB = mfma(woB[i], xa[i], aB); }
            SBAR();
            STAMP(16 + L * 32);
            {
                const unsigned char* src = p.ws + OFF_W2 + ((2 * j + (w & 1)) * KSFP + (w >> 1) * 22 + W2_REG) * 1024 + vl;
#pragma unroll
                for (int i = 0; i < 5; ++i) glds16(src + i * 1024, lbase + L_W2 + (w * 11 + i) * 1024);
            }
            STAMP(17 + L * 32);
            float o[8];
            reduce4(lds, L_RED, aA, aB, tb, kq, lane, o);
#pragma unroll
            for (int i = 0; i < 4; ++i) resid[i] = pk(bflo(resid[i]) + o[2 * i], bfhi(resid[i]) + o[2 * i + 1]);
            st_sc1(swap_order(resid), rw, vl, OFF_X1 + L * ACT_BYTES + (tbg * KSD + sown) * 1024);
            STAMP(18 + L * 32);
            publish(cnt + cnt_word(L, CNT_X1, c));
            STAMP(19 + L * 32);
        }
        u32x4 w2A[W2_REG], w2B[W2_REG];
        {
            PHASE_IDS(); W1_IDS();
            LOAD_W1(16);
            if (w == 1 || w == 2 || w == 3) {
                const int col = min(32 * cb0 + tid - 64, 1407);
                *(LAS float*)(lds + L_TAB + (tid - 64) * 4) = *(const GAS float*)(p.ws + OFF_CS1 + col * 4);
                *(LAS float*)(lds + L_TAB + 768 + (tid - 64) * 4) = *(const GAS float*)(p.ws + OFF_B1F + col * 4);
            }
            STAMP(20 + L * 32);
            wait_cnt(cnt + cnt_word(L, CNT_X1, c), 8u, tmo);
            STAMP(21 + L * 32);
            {
                const unsigned char* src = p.ws + OFF_X1 + L * ACT_BYTES + (2 * c * KSD + 2 * w) * 1024 + vl;
#pragma unroll
                for (int ch = 0; ch < 2; ++ch)
#pragma unroll
                    for (int t = 0; t < 2; ++t)
#pragma unroll
                        for (int i = 0; i < 2; ++i) glds16_sc1(src + (t * KSD + 16 * ch + i) * 1024, lbase + L_ACT + (t * KSD + 16 * ch + 2 * w + i) * 1024);
            }
            WAITVM(4);
            LDS_BARRIER();
            STAMP(22 + L * 32);
            f32x16 a0 = {}, a1 = {};
            float s1[2] = {0.f, 0.f}, s2[2] = {0.f, 0.f};
            const unsigned char* w2l_src = p.ws + OFF_W2 + ((2 * j + (w & 1)) * KSFP + (w >> 1) * 22 + W2_REG) * 1024 + vl;
            auto w2_req = [&](int n) {
                if (n < 6) glds16(w2l_src + (5 + n) * 1024, lbase + L_W2 + (w * 11 + 5 + n) * 1024);
                else if (n < 6 + W2_REG) w2A[n - 6] = ld_plain(rw, vw, OFF_W2 + (cbA * KSFP + kq * 22 + n - 6) * 1024);
                else if (n < 6 + 2 * W2_REG) w2B[n - 6 - W2_REG] = ld_plain(rw, vw, OFF_W2 + (cbB * KSFP + kq * 22 + n - 6 - W2_REG) * 1024);
            };
            auto part = [&](int lo) {
                const int n0 = lo == 0 ? 0 : 14;
                if (TB == 0) {
                    u32x4 b0[3], b1[3];
#pragma unroll
                    for (int i = 0; i < 2; ++i) { b0[i] = lds_ld(lds, L_ACT + (lo + i) * 1024 + vl); b1[i] = lds_ld(lds, L_ACT + (KSD + lo + i) * 1024 + vl); }
#pragma unroll
                    for (int s = 0; s < 16; ++s) {
                        if (s + 2 < 16) { b0[(s + 2) % 3] = lds_ld(lds, L_ACT + (lo + s + 2) * 1024 + vl); b1[(s + 2) % 3] = lds_ld(lds, L_ACT + (KSD + lo + s + 2) * 1024 + vl); }
                        SBAR();
                        a0 = mfma(w1f[lo + s], b0[s % 3], a0); a1 = mfma(w1f[lo + s], b1[s % 3], a1);
                        PIN2(a0, a1);
                        if (s < 14) w2_req(n0 + s);
                        SBAR();
                    }
                } else {
                    u32x4 b0[3], b1[3];
                    const int ab = L_ACT + (lo + par) * 1024 + vl;
#pragma unroll
                    for (int i = 0; i < 2; ++i) { b0[i] = lds_ld(lds, ab + 2 * i * 1024); b1[i] = lds_ld(lds, ab + (KSD + 2 * i) * 1024); }
#pragma unroll
                    for (int s = 0; s < 8; ++s) {
                        if (s + 2 < 8) { b0[(s + 2) % 3] = lds_ld(lds, ab + 2 * (s + 2) * 1024); b1[(s + 2) % 3] = lds_ld(lds, ab + (KSD + 2 * (s + 2)) * 1024); }
                        SBAR();
                        a0 = mfma(w1f[lo + s], b0[s % 3], a0); a1 = mfma(w1f[lo + s], b1[s % 3], a1);
                        PIN2(a0, a1);
                        if (s < 7) { w2_req(n0 + 2 * s); w2_req(n0 + 2 * s + 1); }
                        SBAR();
                    }
                }
            };
            auto stats = [&](int lo) {
#pragma unroll
                for (int t = 0; t < 2; ++t)
#pragma unroll
                    for (int i = 0; i < 2; ++i) stats_acc(lds_ld(lds, L_ACT + (t * KSD + lo + 2 * w + i) * 1024 + vl), s1[t], s2[t]);
            };
            stats(0);
            SBAR();
            part(0);
            STAMP(23 + L * 32);
            WAITVM(14);
            LDS_BARRIER();
            stats(16);
            {
                LAS f32x2* st = (LAS f32x2*)(lds + L_STAT);
#pragma unroll
                for (int t = 0; t < 2; ++t) { const float u1 = half_sum(s1[t]), u2 = half_sum(s2[t]); if (h == 0) st[(t * 8 + w) * 32 + r] = (f32x2){u1, u2}; }
            }
            SBAR();
            part(16);
            STAMP(24 + L * 32);
            if (TB == 1) {
                const int xo = L_ACT + (w - 4) * 4096 + vl;
#pragma unroll
                for (int g = 0; g < 4; ++g) {
                    const f32x4 v = par ? (f32x4){a0[4 * g], a0[4 * g + 1], a0[4 * g + 2], a0[4 * g + 3]} : (f32x4){a1[4 * g], a1[4 * g + 1], a1[4 * g + 2], a1[4 * g + 3]};
                    lds_stf(lds, xo + g * 1024, v);
                }
            }
            LDS_BARRIER();
            if (TB == 1) {
                const int xi = L_ACT + ((w - 4) ^ 1) * 4096 + vl;
#pragma unroll
                for (int g = 0; g < 4; ++g) {
                    const f32x4 v = lds_ldf(lds, xi + g * 1024);
#pragma unroll
                    for (int i = 0; i < 4; ++i) a0[4 * g + i] = (par ? a1[4 * g + i] : a0[4 * g + i]) + v[i];
                }
            }
            STAMP(25 + L * 32);
            if (d_active) {
                auto finish = [&](const f32x16& acc, int t) {
                    LAS f32x2* st = (LAS f32x2*)(lds + L_STAT);
                    float t1 = 0.f, t2 = 0.f;
#pragma unroll
                    for (int i = 0; i < 8; ++i) { f32x2 v = st[(t * 8 + i) * 32 + r]; t1 += v[0]; t2 += v[1]; }
                    float mu, rstd; ln_from_sums(t1, t2, mu, rstd);
                    const float nmr = -mu * rstd;
                    unsigned hp[8];
#pragma unroll
                    for (int g = 0; g < 4; ++g) {
                        const f32x4 cs = lds_ldf(lds, L_TAB + (32 * cb1 + 8 * g + 4 * h) * 4), bb = lds_ldf(lds, L_TAB + 768 + (32 * cb1 + 8 * g + 4 * h) * 4);
                        float y[4];
#pragma unroll
                        for (int i = 0; i < 4; ++i) {
                            const float x = fmaf(acc[4 * g + i], rstd, fmaf(cs[i], nmr, bb[i]));
                            const float t = x * fmaf(x * x, -2.f * 0.7978845608028654f * 0.044715f * 1.4426950408889634f, -2.f * 0.7978845608028654f * 1.4426950408889634f);
                            y[i] = x * __builtin_amdgcn_rcpf(1.f + __builtin_amdgcn_exp2f(t));
                        }
                        hp[2 * g] = pk(y[0], y[1]); hp[2 * g + 1] = pk(y[2], y[3]);
                    }
                    const int so = OFF_H + L * H_BYTES + ((2 * c + t) * KSF + 2 * cbg1) * 1024;
                    st_sc1(swap_order((u32x4){hp[0], hp[1], hp[2], hp[3]}), rw, vl, so);
                    st_sc1(swap_order((u32x4){hp[4], hp[5], hp[6], hp[7]}), rw, vl, so + 1024);
                };
                if (TB == 0) { finish(a0, 0); finish(a1, 1); } else finish(a0, par);
            }
            STAMP(26 + L * 32);
            publish(cnt + cnt_word(L, CNT_H, c));
            STAMP(27 + L * 32);
        }
        {
            PHASE_IDS();
            const f32x4 bb0 = *(const GAS f32x4*)(p.ws + OFF_B2F + (16 * sown + 4 * h) * 4), bb1 = *(const GAS f32x4*)(p.ws + OFF_B2F + (16 * sown + 8 + 4 * h) * 4);
            wait_cnt(cnt + cnt_word(L, CNT_H, c), 8u, tmo);
            STAMP(28 + L * 32);
            u32x4 ha[22];
            const int hso = OFF_H + L * H_BYTES + tbg * KSF * 1024;
            constexpr int EWIN = 8;
#pragma unroll
            for (int i = 0; i < EWIN; ++i) ha[i] = ld_sc1(rw, vl, hso + min(kq * 22 + i, KSF - 1) * 1024);
            SBAR();
            f32x16 aA = {}, aB = {};
            const int oa_ = L_W2 + (kq * 22 + (kq >> 1) * 11) * 1024 + vw, ob_ = L_W2 + (kq * 22 + ((kq >> 1) ^ 1) * 11) * 1024 + vw;
            u32x4 la[3], lb[3];
#pragma unroll
            for (int i = 0; i < 22; ++i) {
                if (i + EWIN < 22) ha[i + EWIN] = ld_sc1(rw, vl, hso + min(kq * 22 + i + EWIN, KSF - 1) * 1024);
                if (i + 2 >= W2_REG && i + 2 < 22) { la[(i + 2 - W2_REG) % 3] = lds_ld(lds, oa_ + (i + 2 - W2_REG) * 1024); lb[(i + 2 - W2_REG) % 3] = lds_ld(lds, ob_ + (i + 2 - W2_REG) * 1024); }
                SBAR();
                if (i < W2_REG) { aA = mfma(w2A[i], ha[i], aA); aB = mfma(w2B[i], ha[i], aB); }
                else { aA = mfma(la[(i - W2_REG) % 3], ha[i], aA); aB = mfma(lb[(i - W2_REG) % 3], ha[i], aB); }
                PIN2(aA, aB);
                SBAR();
            }
            STAMP(29 + L * 32);
            float o[8];
            reduce4(lds, L_RED, aA, aB, tb, kq, lane, o);
#pragma unroll
            for (int i = 0; i < 4; ++i) { o[i] += bb0[i]; o[4 + i] += bb1[i]; }
#pragma unroll
            for (int i = 0; i < 4; ++i) resid[i] = pk(bflo(resid[i]) + o[2 * i], bfhi(resid[i]) + o[2 * i + 1]);
            STAMP(30 + L * 32);
            const u32x4 xo = swap_order(resid);
            if (L == 0) {
                st_sc1(xo, rw, vl, OFF_X2 + (tbg * KSD + sown) * 1024);
                publish(cnt + cnt_word(0, CNT_X2, c));
            } else {
                *(GAS u32x4*)(p.out + (size_t)(64 * c + 32 * tb + r) * D + 16 * sown + 8 * h) = xo;
            }
            STAMP(31 + L * 32);
        }
    }
}

__global__ void __launch_bounds__(512, 2) main_kernel(Params p) {
    extern __shared__ __attribute__((aligned(16))) char lds_dyn[];
    LAS char* lds = (LAS char*)lds_dyn;
    if (__builtin_amdgcn_readfirstlane(threadIdx.x >> 8) == 0) run_layers<0>(p, lds); else run_layers<1>(p, lds);
}

}

extern "C" void kernel_launch(void* const* d_in, const int* in_sizes, int n_in, void* d_out, int out_size, void* d_ws, size_t ws_size, hipStream_t stream) {
    Params p;
    p.x = (const uint16_t*)d_in[0]; p.wq = (const uint16_t*)d_in[1]; p.wk = (const uint16_t*)d_in[2]; p.wv = (const uint16_t*)d_in[3]; p.wo = (const uint16_t*)d_in[4];
    p.w1 = (const uint16_t*)d_in[6]; p.b1 = (const uint16_t*)d_in[7]; p.w2 = (const uint16_t*)d_in[8]; p.b2 = (const uint16_t*)d_in[9];
    p.out = (uint16_t*)d_out; p.ws = (unsigned char*)d_ws;
    (void)hipFuncSetAttribute((const void*)main_kernel, hipFuncAttributeMaxDynamicSharedMemorySize, LDS_BYTES);
    hipLaunchKernelGGL(prep_kernel, dim3(256), dim3(512), 0, stream, p);
    hipLaunchKernelGGL(main_kernel, dim3(256), dim3(512), LDS_BYTES, stream, p);
}
```

```cpp
#include <hip/hip_runtime.h>
#include <hip/hip_bf16.h>
#include <stdint.h>

#define GAS __attribute__((address_space(1)))
#define LAS __attribute__((address_space(3)))
typedef __bf16 bf16x8 __attribute__((ext_vector_type(8)));
typedef __bf16 bf16x2 __attribute__((ext_vector_type(2)));
typedef float f32x16 __attribute__((ext_vector_type(16)));
typedef float f32x4 __attribute__((ext_vector_type(4)));
typedef float f32x2 __attribute__((ext_vector_type(2)));
typedef unsigned u32x4 __attribute__((ext_vector_type(4)));
typedef GAS unsigned gu32;
#define RLX_AGENT __ATOMIC_RELAXED, __HIP_MEMORY_SCOPE_AGENT

namespace {

constexpr int S = 2048, D = 512, F = 1376;
constexpr int KSD = 32;
constexpr int KSF = 86;
constexpr int KSFP = 88;
constexpr float LN_EPS = 1e-5f;
constexpr float QSCALE = 0.25f * 1.4426950408889634f;
constexpr float SAFE_BOUND = 50.f;

constexpr int CW = 64;
constexpr int OFF_CNT = 0;
constexpr int CNT_O = 0, CNT_X1 = 1, CNT_H = 2, CNT_X2 = 3;
__device__ __forceinline__ int cnt_word(int layer, int kind, int c) { return ((layer * 4 + kind) * 32 + c) * CW; }
constexpr int OFF_SYNC = 65536;
constexpr int OFF_KVFL = OFF_SYNC + 1024;
constexpr int OFF_WO = OFF_KVFL + 2 * 32 * 256;
constexpr int OFF_W1 = OFF_WO + 16 * KSD * 1024;
constexpr int OFF_W2 = OFF_W1 + 43 * KSD * 1024;
constexpr int OFF_CS1 = OFF_W2 + 16 * KSFP * 1024;
constexpr int OFF_KMAX = OFF_CS1 + 1408 * 4;
constexpr int OFF_KF = (OFF_KMAX + 2 * 64 * 4 + 1023) / 1024 * 1024;
constexpr int OFF_VF = OFF_KF + 2 * 65536;
constexpr int OFF_O = OFF_VF + 2 * 65536;
constexpr int ACT_BYTES = 64 * KSD * 1024;
constexpr int OFF_X1 = OFF_O + 2 * ACT_BYTES;
constexpr int OFF_X2 = OFF_X1 + 2 * ACT_BYTES;
constexpr int H_BYTES = 64 * KSF * 1024;
constexpr int OFF_H = OFF_X2 + ACT_BYTES;
constexpr int WS_TOTAL = OFF_H + 2 * H_BYTES;
constexpr unsigned SPIN_CAP = 1u << 15;
constexpr unsigned PREP_WGS = 192;

constexpr int L_K = 0, L_V = 65536, L_ONES = 131072, L_XCHO = 135424;
constexpr int L_WQ = 0, L_WKV = 65536, L_KVRED = 98304, L_XCHQ = 139264;
constexpr int L_ACT = 0, L_W2 = 65536;
constexpr int L_STAT = 155648, L_TAB = 159744, L_CS = L_TAB, L_FLAG = 163584;
constexpr int LDS_BYTES = 163840;
constexpr int W2_REG = 11;

#define PROF 0
#define PROF_TID 0
constexpr int L_PROF = 161280;
constexpr int OFF_PROF = 64 << 20;
#define STAMP(i) do { } while (0)

struct Params {
    const uint16_t *x, *wq, *wk, *wv, *wo, *w1, *b1, *w2, *b2;
    uint16_t* out;
    unsigned char* ws;
};

__device__ __forceinline__ f32x16 mfma(u32x4 a, u32x4 b, f32x16 c) {
    return __builtin_amdgcn_mfma_f32_32x32x16_bf16(__builtin_bit_cast(bf16x8, a), __builtin_bit_cast(bf16x8, b), c, 0, 0, 0);
}
__device__ __forceinline__ unsigned pk(float a, float b) { f32x2 v = {a, b}; return __builtin_bit_cast(unsigned, __builtin_convertvector(v, bf16x2)); }
__device__ __forceinline__ float bflo(unsigned u) { return __uint_as_float(u << 16); }
__device__ __forceinline__ float bfhi(unsigned u) { return __uint_as_float(u & 0xffff0000u); }
__device__ __forceinline__ float bf2f(uint16_t v) { return __uint_as_float((unsigned)v << 16); }
__device__ __forceinline__ float dot2(unsigned a, unsigned b, float c) {
    return __builtin_amdgcn_fdot2_f32_bf16(__builtin_bit_cast(bf16x2, a), __builtin_bit_cast(bf16x2, b), c, false);
}
__device__ __forceinline__ float half_sum(float v) {
    auto r = __builtin_amdgcn_permlane32_swap(__float_as_uint(v), __float_as_uint(v), false, false);
    return __uint_as_float(r[0]) + __uint_as_float(r[1]);
}
__device__ __forceinline__ float half_max(float v) {
    auto r = __builtin_amdgcn_permlane32_swap(__float_as_uint(v), __float_as_uint(v), false, false);
    return fmaxf(__uint_as_float(r[0]), __uint_as_float(r[1]));
}
__device__ __forceinline__ u32x4 swap_order(u32x4 a) {
    auto r0 = __builtin_amdgcn_permlane32_swap(a[0], a[2], false, false);
    auto r1 = __builtin_amdgcn_permlane32_swap(a[1], a[3], false, false);
    return (u32x4){r0[0], r1[0], r0[1], r1[1]};
}
__device__ __forceinline__ float wave_max(float v) {
#pragma unroll
    for (int m = 1; m < 32; m <<= 1) v = fmaxf(v, __shfl_xor(v, m));
    return half_max(v);
}
#define SBAR() __builtin_amdgcn_sched_barrier(0)
#define PIN2(a, b) asm volatile("" : "+v"(a), "+v"(b))
#define LDS_BARRIER() asm volatile("s_waitcnt lgkmcnt(0)\n\ts_barrier" ::: "memory")
#define DRAIN() asm volatile("s_waitcnt vmcnt(0)" ::: "memory")
constexpr int AUX_SC1 = 16;
typedef __amdgpu_buffer_rsrc_t rsrc_t;
__device__ __forceinline__ u32x4 ld_sc1(rsrc_t r, int voff, int soff) { return __builtin_amdgcn_raw_buffer_load_b128(r, voff, soff, AUX_SC1); }
__device__ __forceinline__ u32x4 ld_plain(rsrc_t r, int voff, int soff) { return __builtin_amdgcn_raw_buffer_load_b128(r, voff, soff, 0); }
__device__ __forceinline__ void st_sc1(u32x4 v, rsrc_t r, int voff, int soff) { __builtin_amdgcn_raw_buffer_store_b128(v, r, voff, soff, AUX_SC1); }
__device__ __forceinline__ u32x4 lds_ld(LAS char* l, int off) { return *(LAS u32x4*)(l + off); }
__device__ __forceinline__ void lds_st(LAS char* l, int off, u32x4 v) { *(LAS u32x4*)(l + off) = v; }
__device__ __forceinline__ f32x4 lds_ldf(LAS char* l, int off) { return *(LAS f32x4*)(l + off); }
__device__ __forceinline__ void lds_stf(LAS char* l, int off, f32x4 v) { *(LAS f32x4*)(l + off) = v; }

__device__ __forceinline__ void publish(gu32* cnt) {
    DRAIN();
    LDS_BARRIER();
    if (threadIdx.x == 0) __hip_atomic_fetch_add(cnt, 1u, RLX_AGENT);
}
__device__ __forceinline__ void poll_ge(gu32* cnt, unsigned want, gu32* tmo) {
    unsigned spins = 0;
    while (__hip_atomic_load(cnt, RLX_AGENT) < want) {
        if (++spins > SPIN_CAP) { __hip_atomic_store(tmo, 1u, RLX_AGENT); break; }
    }
}
__device__ __forceinline__ void wait_cnt(gu32* cnt, unsigned want, gu32* tmo) {
    if (threadIdx.x == 0) poll_ge(cnt, want, tmo);
    __builtin_amdgcn_fence(__ATOMIC_ACQUIRE, "wavefront");
    LDS_BARRIER();
}
__device__ __forceinline__ void wait_flags(gu32* fl, unsigned tag, gu32* tmo) {
    if (threadIdx.x < 64) {
        unsigned spins = 0;
        while (!__all(__hip_atomic_load(fl + threadIdx.x, RLX_AGENT) == tag)) {
            if (++spins > SPIN_CAP) { if (threadIdx.x == 0) __hip_atomic_store(tmo, 1u, RLX_AGENT); break; }
        }
    }
    __builtin_amdgcn_fence(__ATOMIC_ACQUIRE, "wavefront");
    LDS_BARRIER();
}
__device__ __forceinline__ void glds16(const void* g, unsigned lds_dst) {
    unsigned keep;
    asm volatile("s_mov_b32 %0, m0\n\ts_mov_b32 m0, %2\n\ts_nop 0\n\tglobal_load_lds_dwordx4 %1, off\n\ts_mov_b32 m0, %0" : "=&s"(keep) : "v"(g), "s"(lds_dst) : "memory");
}
__device__ __forceinline__ void glds16_sc1(const void* g, unsigned lds_dst) {
    unsigned keep;
    asm volatile("s_mov_b32 %0, m0\n\ts_mov_b32 m0, %2\n\ts_nop 0\n\tglobal_load_lds_dwordx4 %1, off sc1\n\ts_mov_b32 m0, %0" : "=&s"(keep) : "v"(g), "s"(lds_dst) : "memory");
}
#define WAITVM(n) asm volatile("s_waitcnt vmcnt(" #n ")" ::: "memory")

__device__ __forceinline__ void reduce4(LAS char* lds, int region, const f32x16& aA, const f32x16& aB, int kq, int lane, float (&o)[8]) {
    const int base = region + lane * 16;
    { const int a = base + ((kq ^ 1) * 3 + 0) * 2048;
      lds_stf(lds, a, (f32x4){aA[8], aA[9], aA[10], aA[11]}); lds_stf(lds, a + 1024, (f32x4){aA[12], aA[13], aA[14], aA[15]}); }
    { const int a = base + ((kq ^ 2) * 3 + 1) * 2048;
      lds_stf(lds, a, (f32x4){aB[0], aB[1], aB[2], aB[3]}); lds_stf(lds, a + 1024, (f32x4){aB[4], aB[5], aB[6], aB[7]}); }
    { const int a = base + ((kq ^ 3) * 3 + 2) * 2048;
      lds_stf(lds, a, (f32x4){aB[8], aB[9], aB[10], aB[11]}); lds_stf(lds, a + 1024, (f32x4){aB[12], aB[13], aB[14], aB[15]}); }
    LDS_BARRIER();
    f32x4 lo = {aA[0], aA[1], aA[2], aA[3]}, hi = {aA[4], aA[5], aA[6], aA[7]};
#pragma unroll
    for (int x = 0; x < 3; ++x) { const int a = base + (kq * 3 + x) * 2048; lo += lds_ldf(lds, a); hi += lds_ldf(lds, a + 1024); }
#pragma unroll
    for (int i = 0; i < 4; ++i) { o[i] = lo[i]; o[4 + i] = hi[i]; }
}
typedef short v4i16 __attribute__((ext_vector_type(4)));
typedef unsigned u32x2 __attribute__((ext_vector_type(2)));
__device__ __forceinline__ u32x4 tr_frag(LAS char* lds, int a, int step) {
    const u32x2 lo = __builtin_bit_cast(u32x2, __builtin_amdgcn_ds_read_tr16_b64_v4i16((LAS v4i16*)(lds + a)));
    const u32x2 hi = __builtin_bit_cast(u32x2, __builtin_amdgcn_ds_read_tr16_b64_v4i16((LAS v4i16*)(lds + a + step)));
    return (u32x4){lo[0], lo[1], hi[0], hi[1]};
}
__device__ __forceinline__ float frag_sum(u32x4 f, float s) {
#pragma unroll
    for (int d = 0; d < 4; ++d) s = dot2(f[d], 0x3f803f80u, s);
    return s;
}

#define A_E1(a) "v_exp_f32 v" #a ", v" #a "\n\t"
#define A_E4(a, b, c, d) A_E1(a) A_E1(b) A_E1(c) A_E1(d)
#define A_CVT(d, a, b) "v_cvt_pk_bf16_f32 v" #d ", v" #a ", v" #b "\n\t"
#define A_DSK(off) "ds_read_b128 v[100:103], v120 offset:" #off "\n\t"
#define A_DSV0(off) "ds_read_b128 v[104:107], v121 offset:" #off "\n\t"
#define A_DSV1(off) "ds_read_b128 v[108:111], v121 offset:" #off "\n\t"
#define A_WAIT(n) "s_waitcnt lgkmcnt(" #n ")\n\t"
#define A_MSB "v_mfma_f32_32x32x16_bf16 v[48:63], v[100:103], v[156:159], v[124:139]\n\t"
#define A_MSA "v_mfma_f32_32x32x16_bf16 v[32:47], v[100:103], v[96:99], v[64:79]\n\t"
#define A_MPV(O, V, P) "v_mfma_f32_32x32x16_bf16 " O ", " V ", " P ", " O "\n\t"
#define A_OA "v[80:95]"
#define A_OB "v[140:155]"
#define A_V0 "v[104:107]"
#define A_V1 "v[108:111]"
#define A_P0 "v[112:115]"
#define A_P1 "v[116:119]"
#define A_STEP(koff, v1off, v0off) \
    A_MSB A_DSK(koff) A_E4(32, 33, 34, 35) \
    A_MPV(A_OB, A_V1, A_P1) A_DSV1(v1off) A_E4(36, 37, 38, 39) \
    A_CVT(112, 32, 33) A_CVT(113, 34, 35) A_CVT(114, 36, 37) A_CVT(115, 38, 39) \
    A_E4(40, 41, 42, 43) \
    A_WAIT(2) A_MPV(A_OA, A_V0, A_P0) A_E4(44, 45, 46, 47) \
    A_CVT(116, 40, 41) A_CVT(117, 42, 43) A_CVT(118, 44, 45) A_CVT(119, 46, 47) \
    A_WAIT(1) A_MSA A_E4(48, 49, 50, 51) \
    A_WAIT(0) A_MPV(A_OA, A_V1, A_P1) A_E4(52, 53, 54, 55) \
    A_CVT(112, 48, 49) A_CVT(113, 50, 51) A_CVT(114, 52, 53) A_CVT(115, 54, 55) \
    A_E4(56, 57, 58, 59) \
    A_MPV(A_OB, A_V0, A_P0) A_DSV0(v0off) A_E4(60, 61, 62, 63) \
    A_CVT(116, 56, 57) A_CVT(117, 58, 59) A_CVT(118, 60, 61) A_CVT(119, 62, 63)

struct AttnState { f32x16 sa, sb, oa, ob, negma, negmb; u32x4 qa, qb, kf, v0, v1, p1; unsigned kaddr, vaddr, vstride; };
#define A_OPERANDS(st, cnt) \
        : "+{v[32:47]}"(st.sa), "+{v[48:63]}"(st.sb), "+{v[80:95]}"(st.oa), "+{v[140:155]}"(st.ob), "+{v[100:103]}"(st.kf), "+{v[104:107]}"(st.v0), "+{v[108:111]}"(st.v1), \
          "={v[112:115]}"(p0), "+{v[116:119]}"(st.p1), "+{v120}"(st.kaddr), "+{v121}"(st.vaddr), [n] "+s"(cnt) \
        : "{v[64:79]}"(st.negma), "{v[124:139]}"(st.negmb), "{v[96:99]}"(st.qa), "{v[156:159]}"(st.qb), "{v122}"(st.vstride) \
        : "memory", "scc"
#define A_ITER \
        A_STEP(1024, 512, 1024) \
        A_STEP(2048, 1536, 2048) \
        A_STEP(3072, 2560, 3072) \
        A_STEP(4096, 3584, 4096) \
        "v_add_u32 v120, 4096, v120\n\t" \
        "v_add_u32 v121, v122, v121\n\t"
__device__ __forceinline__ void attn_begin(AttnState& st, LAS char* lds, u32x4 qa, u32x4 qb, float ma, float mb, unsigned kaddr, unsigned vaddr, unsigned vstride) {
#pragma unroll
    for (int i = 0; i < 16; ++i) { st.negma[i] = -ma; st.negmb[i] = -mb; }
    st.qa = qa; st.qb = qb;
    st.kf = lds_ld(lds, kaddr); st.v0 = lds_ld(lds, vaddr); st.v1 = st.v0; st.p1 = (u32x4){0u, 0u, 0u, 0u};
    st.sa = mfma(st.kf, qa, st.negma); st.sb = st.sa;
    st.oa = (f32x16){}; st.ob = (f32x16){};
    const unsigned lbase = (unsigned)(uintptr_t)lds;
    st.kaddr = kaddr + lbase; st.vaddr = vaddr + lbase; st.vstride = vstride;
}
__device__ __forceinline__ void attn_first(AttnState& st) {
    unsigned cnt = 4; u32x4 p0;
    asm volatile(
        "s_nop 15\n\t"
        "ATT1_LOOP_%=:\n\t"
        A_ITER
        "s_cmp_lg_u32 %[n], 3\n\t"
        "s_cbranch_scc1 ATT1_GO_%=\n\t"
        "s_waitcnt vmcnt(0)\n\t"
        "s_barrier\n\t"
        "ATT1_GO_%=:\n\t"
        "s_sub_u32 %[n], %[n], 1\n\t"
        "s_cmp_lg_u32 %[n], 0\n\t"
        "s_cbranch_scc1 ATT1_LOOP_%=\n\t"
        A_OPERANDS(st, cnt));
}
__device__ __forceinline__ void attn_second(AttnState& st) {
    unsigned cnt = 4; u32x4 p0;
    asm volatile(
        "ATT2_LOOP_%=:\n\t"
        A_ITER
        "s_sub_u32 %[n], %[n], 1\n\t"
        "s_cmp_lg_u32 %[n], 0\n\t"
        "s_cbranch_scc1 ATT2_LOOP_%=\n\t"
        "s_waitcnt lgkmcnt(0)\n\t"
        A_MPV(A_OB, A_V1, A_P1)
        "s_nop 15\n\t"
        A_OPERANDS(st, cnt));
}

__device__ __forceinline__ void stats_acc(u32x4 f, float& s1, float& s2) {
#pragma unroll
    for (int d = 0; d < 4; ++d) { s1 = dot2(f[d], 0x3f803f80u, s1); s2 = dot2(f[d], f[d], s2); }
}
__device__ __forceinline__ void ln_from_sums(float t1, float t2, float& mu, float& rstd) {
    mu = t1 * (1.f / D);
    rstd = __builtin_amdgcn_rsqf(fmaxf(t2 * (1.f / D) - mu * mu, 0.f) + LN_EPS);
}

template <int TB>
__device__ __forceinline__ void run_layers(const Params& p, LAS char* lds) {
    const unsigned lbase = (unsigned)(uintptr_t)lds;
    const int tid0 = threadIdx.x, w0 = __builtin_amdgcn_readfirstlane(tid0 >> 6);
    const int kk0 = blockIdx.x >> 3, c0 = (blockIdx.x & 7) * 4 + (kk0 >> 3), j0 = kk0 & 7;
    const rsrc_t rw = __builtin_amdgcn_make_buffer_rsrc(p.ws, 0, WS_TOTAL, 0x00020000);
    const rsrc_t rx = __builtin_amdgcn_make_buffer_rsrc((void*)p.x, 0, S * D * 2, 0x00020000);
    gu32* cnt = (gu32*)(p.ws + OFF_CNT);
    gu32* sync = (gu32*)(p.ws + OFF_SYNC);
    gu32* tmo = sync + 2 * CW;
#define PHASE_IDS() \
    int tid = tid0, w = w0, c = c0, j = j0; \
    asm volatile("" : "+v"(tid)); asm volatile("" : "+s"(w), "+s"(c), "+s"(j)); \
    constexpr int tb = TB; const int lane = tid & 63, r = lane & 31, h = lane >> 5, kq = w & 3, tbg = 2 * c + tb; \
    const int vl = lane * 16;                                     \
    const int vw = (lane ^ ((kq & 1) << 4)) * 16;                 \
    const int cbA = 2 * j + (kq >> 1), cbB = 2 * j + ((kq >> 1) ^ 1); \
    const int sown = 4 * j + kq;                                  \
    (void)r; (void)h; (void)tbg; (void)vl; (void)vw; (void)cbA; (void)cbB; (void)sown; (void)tb;
#define W1_IDS() \
    const int ncb = j < 3 ? 6 : 5, cb0 = j < 3 ? 6 * j : 18 + 5 * (j - 3);     \
    const int cb1 = TB == 0 ? kq : 4 + (kq >> 1);     \
    const bool d_active = cb1 < ncb; \
    const int cbg1 = cb0 + (d_active ? cb1 : 0); (void)cbg1; \
    const int par = TB == 0 ? 0 : (kq & 1); (void)par;
#define LOAD_W1(lo) do { \
        _Pragma("unroll") for (int i = 0; i < 8; ++i) w1f[(lo) + i] = ld_sc1(rw, vl, OFF_W1 + (cbg1 * KSD + (lo) + (TB == 0 ? i : 2 * i + par)) * 1024); \
        if (TB == 0) { _Pragma("unroll") for (int i = 8; i < 16; ++i) w1f[(lo) + i] = ld_sc1(rw, vl, OFF_W1 + (cbg1 * KSD + (lo) + i) * 1024); } \
        SBAR(); } while (0)

    STAMP(0);
    const unsigned epoch = __builtin_amdgcn_readfirstlane(__hip_atomic_load(sync, RLX_AGENT));
    u32x4 resid;
    {
        PHASE_IDS();
        resid = swap_order(ld_plain(rx, r * 1024 + h * 16, (64 * c + 32 * tb) * 1024 + sown * 32));
        if (j < 2 && tid < 4) __hip_atomic_store(cnt + cnt_word(j, tid, c), 0u, RLX_AGENT);
        if (c == 0 && j == 0 && tid >= 64 && tid < 66) __hip_atomic_store(sync + (tid - 63) * CW, 0u, RLX_AGENT);
    }

#pragma unroll 1
    for (int L = 0; L < 2; ++L) {
        u32x4 qown; float q2own;
        {
            PHASE_IDS();
            const bool kvm = j < 2;
            const bool kvteam = j == tb;
            if (kvm) {
                const uint16_t* src = ((lane & 2) ? p.wv : p.wk) + (64 * w + (lane >> 2)) * 16 + (lane & 1) * 8;
#pragma unroll
                for (int i = 0; i < 4; ++i) glds16(src + i * 256, lbase + L_WKV + (w * 4 + i) * 1024);
            }
            {
                const int row = lane >> 3, pc = lane & 7;
                const uint16_t* src = p.wq + (size_t)(64 * w + row) * D + 64 * j + (((pc >> 2) ^ ((row >> 1) & 1)) * 32 + (pc & 3) * 8);
#pragma unroll
                for (int i = 0; i < 8; ++i) glds16(src + i * 8 * D, lbase + L_WQ + (w * 8 + i) * 1024);
            }
            u32x4 praw[4]; int pdst[4];
            if (L == 0 && !kvm) {
                const int u = c * 6 + (j - 2);
#pragma unroll
                for (int i = 0; i < 4; ++i) {
                    const uint16_t* src; int ld, cb, s; pdst[i] = -1;
                    if (u < 43) { src = p.w1; ld = F; cb = u; s = 4 * w + i; pdst[i] = OFF_W1 + (cb * KSD + s) * 1024; }
                    else { const int f = ((u - 43) * 8 + w) + i * 1192;
                        if (i < 2 && f < 512) { src = p.wo; ld = D; cb = f >> 5; s = f & 31; pdst[i] = OFF_WO + f * 1024; }
                        else if (i < 2 && f < 1920) { const int g = f - 512; src = p.w2; ld = D; cb = g / KSFP; s = g % KSFP; pdst[i] = OFF_W2 + g * 1024; }
                        else { src = p.wo; ld = D; cb = 0; s = 0; } }
                    const bool zero = (src == p.w2) && s >= KSF;
                    praw[i] = (u32x4){0u, 0u, 0u, 0u};
                    if (pdst[i] >= 0 && !zero) praw[i] = *(const GAS u32x4*)(src + (size_t)(16 * s + (lane >> 2)) * ld + 32 * cb + (lane & 3) * 8);
                }
            }
            if (L == 1) wait_cnt(cnt + cnt_word(0, CNT_X2, c), 8u, tmo);
            STAMP(1 + L * 32);
            const rsrc_t ra = L == 0 ? rx : rw;
            const int avo = L == 0 ? r * 1024 + h * 16 : vl;
            const int aso = L == 0 ? (64 * c + 32 * tb) * 1024 + kq * 8 * 32 : OFF_X2 + (tbg * KSD + kq * 8) * 1024;
            const int astr = L == 0 ? 32 : 1024;
            u32x4 xa[8];
#pragma unroll
            for (int i = 0; i < 8; ++i) xa[i] = ld_sc1(ra, avo, aso + i * astr);
            SBAR();
            STAMP(2 + L * 32);
            WAITVM(8);
            LDS_BARRIER();
            float s1 = 0.f, s2 = 0.f;
#pragma unroll
            for (int i = 0; i < 8; ++i) stats_acc(xa[i], s1, s2);
            s1 = half_sum(s1); s2 = half_sum(s2);
            LAS f32x2* st = (LAS f32x2*)(lds + L_STAT);
            LAS float* cst = (LAS float*)(lds + L_CS);
            if (h == 0) st[(tb * 8 + kq) * 32 + r] = (f32x2){s1, s2};
            const int tq = (lane & 15) >> 2, tp = lane & 3, tg = (lane >> 4) & 1;
            if (kvm) {
                if (kvteam) {
                    f32x16 ak = {}, av = {};
                    float cs = 0.f;
                    const int a = L_WKV + (kq * 128 + 8 * h + tq) * 64 + tg * 32 + tp * 8;
#pragma unroll
                    for (int i = 0; i < 8; ++i) { const u32x4 wk = tr_frag(lds, a + i * 1024, 256); ak = mfma(wk, xa[i], ak); av = mfma(xa[i], wk, av); cs = frag_sum(wk, cs); }
                    cs = half_sum(cs);
                    if (h == 0) cst[kq * 96 + 64 + r] = cs;
                    const int o = L_KVRED + kq * 4096;
                    lds_stf(lds, o + vl, (f32x4){ak[0], ak[1], ak[2], ak[3]}); lds_stf(lds, o + 1024 + vl, (f32x4){ak[4], ak[5], ak[6], ak[7]});
                    if (r >= 16) {
#pragma unroll
                        for (int g = 0; g < 4; ++g) lds_stf(lds, o + 2048 + g * 512 + (h * 16 + r - 16) * 16, (f32x4){av[4 * g], av[4 * g + 1], av[4 * g + 2], av[4 * g + 3]});
                    }
                }
                LDS_BARRIER();
                if (kvteam && kq < 3) {
                    float t1 = 0.f, t2 = 0.f;
#pragma unroll
                    for (int i = 0; i < 4; ++i) { f32x2 v = st[(tb * 8 + i) * 32 + r]; t1 += v[0]; t2 += v[1]; }
                    float mu, rstd; ln_from_sums(t1, t2, mu, rstd);
                    f32x4 a = {}, e = {};
                    if (kq == 0) {
                        f32x4 k0 = {}, k1 = {};
#pragma unroll
                        for (int i = 0; i < 4; ++i) { a += lds_ldf(lds, L_KVRED + i * 4096 + vl); e += lds_ldf(lds, L_KVRED + i * 4096 + 1024 + vl);
                                                      k0 += lds_ldf(lds, L_CS + (i * 96 + 64 + 4 * h) * 4); k1 += lds_ldf(lds, L_CS + (i * 96 + 64 + 8 + 4 * h) * 4); }
#pragma unroll
                        for (int i = 0; i < 4; ++i) { a[i] = (a[i] - mu * k0[i]) * rstd; e[i] = (e[i] - mu * k1[i]) * rstd; }
                        st_sc1((u32x4){pk(a[0], a[1]), pk(a[2], a[3]), pk(e[0], e[1]), pk(e[2], e[3])}, rw, vl, OFF_KF + L * 65536 + tbg * 1024);
                        float n2 = (a[0] * a[0] + a[1] * a[1]) + (a[2] * a[2] + a[3] * a[3]) + (e[0] * e[0] + e[1] * e[1]) + (e[2] * e[2] + e[3] * e[3]);
                        n2 = wave_max(half_sum(n2));
                        if (lane == 0) __hip_atomic_store((gu32*)(p.ws + OFF_KMAX + (L * 64 + tbg) * 4), __float_as_uint(n2), RLX_AGENT);
                    } else {
                        const int g = kq - 1, lc = (h * 16 + (r & 15)) * 16;
                        float csv = 0.f;
#pragma unroll
                        for (int i = 0; i < 4; ++i) { a += lds_ldf(lds, L_KVRED + i * 4096 + 2048 + (2 * g) * 512 + lc); e += lds_ldf(lds, L_KVRED + i * 4096 + 2048 + (2 * g + 1) * 512 + lc);
                                                      csv += cst[i * 96 + 64 + 16 + (r & 15)]; }
#pragma unroll
                        for (int i = 0; i < 8; ++i) {
                            const int tok = 16 * g + (i & 3) + 8 * (i >> 2) + 4 * h;
                            const float m2 = __shfl(mu, tok), r2 = __shfl(rstd, tok);
                            if (i < 4) a[i] = (a[i] - m2 * csv) * r2; else e[i - 4] = (e[i - 4] - m2 * csv) * r2;
                        }
                        if (r >= 16) st_sc1((u32x4){pk(a[0], a[1]), pk(a[2], a[3]), pk(e[0], e[1]), pk(e[2], e[3])}, rw, h * 256 + (r - 16) * 16, OFF_VF + L * 65536 + (2 * tbg + g) * 512);
                    }
                }
                DRAIN();
                LDS_BARRIER();
                if (tid < 32) __hip_atomic_store((gu32*)(p.ws + OFF_KVFL) + (L * 32 + tid) * 64 + 2 * c + j, epoch + 1u + L, RLX_AGENT);
            }
            STAMP(3 + L * 32);
            f32x16 aA = {}, aB = {};
            {
                float csA = 0.f, csB = 0.f;
                const int sw = (tq >> 1) & 1, col = (tg ^ (kq & 1)) * 32 + tp * 8;
                const int a0 = L_WQ + (kq * 128 + 8 * h + tq) * 128 + col;
                const int aa = a0 + (((kq >> 1) ^ sw) * 64), ab = a0 + (((kq >> 1) ^ 1 ^ sw) * 64);
#pragma unroll
                for (int i = 0; i < 8; ++i) {
                    const u32x4 fa = tr_frag(lds, aa + i * 2048, 512), fb = tr_frag(lds, ab + i * 2048, 512);
                    aA = mfma(fa, xa[i], aA); aB = mfma(fb, xa[i], aB);
                    csA = frag_sum(fa, csA); csB = frag_sum(fb, csB);
                }
                csA = half_sum(csA); csB = half_sum(csB);
                if (TB == 0 && h == 0) { const int n = r ^ ((kq & 1) << 4); cst[kq * 96 + (kq >> 1) * 32 + n] = csA; cst[kq * 96 + ((kq >> 1) ^ 1) * 32 + n] = csB; }
            }
            STAMP(4 + L * 32);
            float q[8];
            reduce4(lds, (tb == (kvm ? j : 0)) ? 65536 : 114688, aA, aB, kq, lane, q);
            STAMP(5 + L * 32);
            float t1 = 0.f, t2 = 0.f;
            f32x4 cs0 = {}, cs1 = {};
#pragma unroll
            for (int i = 0; i < 4; ++i) { f32x2 v = st[(tb * 8 + i) * 32 + r]; t1 += v[0]; t2 += v[1];
                                          cs0 += lds_ldf(lds, L_CS + (i * 96 + 16 * kq + 4 * h) * 4); cs1 += lds_ldf(lds, L_CS + (i * 96 + 16 * kq + 8 + 4 * h) * 4); }
            float mu, rstd; ln_from_sums(t1, t2, mu, rstd);
            float n2 = 0.f;
#pragma unroll
            for (int i = 0; i < 4; ++i) { q[i] = (q[i] - mu * cs0[i]) * (rstd * QSCALE); q[4 + i] = (q[4 + i] - mu * cs1[i]) * (rstd * QSCALE); }
#pragma unroll
            for (int i = 0; i < 8; ++i) n2 += q[i] * q[i];
            q2own = half_sum(n2);
            qown = (u32x4){pk(q[0], q[1]), pk(q[2], q[3]), pk(q[4], q[5]), pk(q[6], q[7])};
            lds_st(lds, L_XCHQ + w * 1536 + vl, qown);
            *(LAS float*)(lds + L_XCHQ + w * 1536 + 1024 + lane * 4) = q2own;
            if (L == 0 && !kvm) {
                float cs = 0.f;
                const int a = L_WQ + w * 1024;
#pragma unroll
                for (int i = 0; i < 4; ++i) {
                    lds_st(lds, a + vl, praw[i]);
                    const u32x4 fr = tr_frag(lds, a + (8 * h + tq) * 64 + tg * 32 + tp * 8, 256);
                    if (pdst[i] >= 0) st_sc1(fr, rw, vl, pdst[i]);
                    cs = frag_sum(fr, cs);
                }
                const int u = c * 6 + (j - 2);
                if (u < 43) {
                    cs = half_sum(cs);
                    LAS float* t = (LAS float*)(lds + L_WQ + 8192);
                    if (h == 0) t[w * 32 + r] = cs;
                    LDS_BARRIER();
                    if (tid < 32) { float v = 0.f;
#pragma unroll
                        for (int i = 0; i < 8; ++i) v += t[i * 32 + tid];
                        __hip_atomic_store((gu32*)(p.ws + OFF_CS1 + (32 * u + tid) * 4), __float_as_uint(v), RLX_AGENT); }
                }
                DRAIN();
            }
            STAMP(6 + L * 32);
        }
        u32x4 woA[8], woB[8];
        {
            PHASE_IDS();
            wait_flags((gu32*)(p.ws + OFF_KVFL) + (L * 32 + c) * 64, epoch + 1u + L, tmo);
            if (L == 0 && j >= 2 && tid == 0) __hip_atomic_fetch_add(sync + CW, 1u, RLX_AGENT);
            STAMP(7 + L * 32);
            const float kmx = __uint_as_float(__builtin_amdgcn_raw_buffer_load_b32(rw, lane * 4, OFF_KMAX + L * 256, AUX_SC1));
            SBAR();
            {
                const unsigned char* src = p.ws + (TB == 0 ? OFF_KF : OFF_VF) + L * 65536 + ((w >> 1) & 1) * 32768 + vl;
                const unsigned dst = lbase + (TB == 0 ? L_K : L_V) + ((w >> 1) & 1) * 32768;
                const int t0 = (w & 1) * 6, t1 = 12 + (w & 1) * 10;
#pragma unroll
                for (int i = 0; i < 6; ++i) glds16_sc1(src + (t0 + i) * 1024, dst + (t0 + i) * 1024);
#pragma unroll
                for (int i = 0; i < 10; ++i) glds16_sc1(src + (t1 + i) * 1024, dst + (t1 + i) * 1024);
            }
            if (tid < 272) lds_st(lds, L_ONES + tid * 16, (u32x4){0x3f803f80u, 0x3f803f80u, 0x3f803f80u, 0x3f803f80u});
            const u32x4 qpar = lds_ld(lds, L_XCHQ + (w ^ 1) * 1536 + vl);
            const float q2par = *(LAS float*)(lds + L_XCHQ + (w ^ 1) * 1536 + 1024 + lane * 4);
            const float km2 = wave_max(kmx);
            float ma = __builtin_amdgcn_sqrtf(q2own * km2) * 1.02f, mb = __builtin_amdgcn_sqrtf(q2par * km2) * 1.02f;
            const bool unsafe = __any(!(ma <= SAFE_BOUND) || !(mb <= SAFE_BOUND));
            {
                LAS unsigned* fl = (LAS unsigned*)(lds + L_FLAG);
                if (lane == 0) fl[w] = unsafe ? 1u : 0u;
            }
            STAMP(8 + L * 32);
            WAITVM(10);
            LDS_BARRIER();
            {
                LAS unsigned* fl = (LAS unsigned*)(lds + L_FLAG);
                const u32x4 f0 = *(LAS u32x4*)fl, f1 = *(LAS u32x4*)(fl + 4);
                if (__builtin_amdgcn_readfirstlane(f0[0] | f0[1] | f0[2] | f0[3] | f1[0] | f1[1] | f1[2] | f1[3])) {
                    DRAIN();
                    LDS_BARRIER();
                    if (unsafe) {
                        float xa = -INFINITY, xb = -INFINITY;
#pragma unroll 1
                        for (int t = 0; t < 64; ++t) {
                            const u32x4 kf = lds_ld(lds, L_K + t * 1024 + vl);
                            const f32x16 sa = mfma(kf, qown, (f32x16){}), sb = mfma(kf, qpar, (f32x16){});
#pragma unroll
                            for (int i = 0; i < 16; ++i) { xa = fmaxf(xa, sa[i]); xb = fmaxf(xb, sb[i]); }
                        }
                        ma = half_max(xa); mb = half_max(xb);
                    }
                }
            }
            STAMP(9 + L * 32);
            const int kh = kq & 1;
            const bool real = r < 16;
            const unsigned kaddr = L_K + kh * 32768 + vl;
            const unsigned vaddr = real ? (unsigned)(L_V + kh * 32768 + h * 256 + r * 16) : (unsigned)(L_ONES + ((r >= 20 && r < 28) ? 64 : 0));
            AttnState st;
            attn_begin(st, lds, qown, qpar, ma, mb, kaddr, vaddr, real ? 4096u : 0u);
            attn_first(st);
            STAMP(10 + L * 32);
            if (L == 0) { if (lane == 0) poll_ge(sync + CW, PREP_WGS, tmo); __builtin_amdgcn_fence(__ATOMIC_ACQUIRE, "wavefront"); }
#pragma unroll
            for (int i = 0; i < 8; ++i) { woA[i] = ld_sc1(rw, vw, OFF_WO + (cbA * KSD + kq * 8 + i) * 1024); woB[i] = ld_sc1(rw, vw, OFF_WO + (cbB * KSD + kq * 8 + i) * 1024); }
            SBAR();
            STAMP(11 + L * 32);
            attn_second(st);
            STAMP(12 + L * 32);
            const f32x16 oa = st.oa, ob = st.ob;
            const int xo = L_XCHO + w * 2304;
            lds_stf(lds, xo + vl, (f32x4){ob[0], ob[1], ob[2], ob[3]}); lds_stf(lds, xo + 1024 + vl, (f32x4){ob[4], ob[5], ob[6], ob[7]});
            *(LAS float*)(lds + xo + 2048 + lane * 4) = ob[8];
            LDS_BARRIER();
            const int xi = L_XCHO + (w ^ 1) * 2304;
            const f32x4 e0 = lds_ldf(lds, xi + vl), e1 = lds_ldf(lds, xi + 1024 + vl);
            const float rl = __builtin_amdgcn_rcpf(oa[8] + *(LAS float*)(lds + xi + 2048 + lane * 4));
            const u32x4 of = {pk((oa[0] + e0[0]) * rl, (oa[1] + e0[1]) * rl), pk((oa[2] + e0[2]) * rl, (oa[3] + e0[3]) * rl),
                              pk((oa[4] + e1[0]) * rl, (oa[5] + e1[1]) * rl), pk((oa[6] + e1[2]) * rl, (oa[7] + e1[3]) * rl)};
            st_sc1(swap_order(of), rw, vl, OFF_O + L * ACT_BYTES + (tbg * KSD + sown) * 1024);
            publish(cnt + cnt_word(L, CNT_O, c));
            STAMP(13 + L * 32);
        }
        u32x4 w1f[32];
        {
            PHASE_IDS(); W1_IDS();
            LOAD_W1(0);
            STAMP(14 + L * 32);
            wait_cnt(cnt + cnt_word(L, CNT_O, c), 8u, tmo);
            STAMP(15 + L * 32);
            u32x4 xa[8];
#pragma unroll
            for (int i = 0; i < 8; ++i) xa[i] = ld_sc1(rw, vl, OFF_O + L * ACT_BYTES + (tbg * KSD + kq * 8 + i) * 1024);
            SBAR();
            f32x16 aA = {}, aB = {};
#pragma unroll
            for (int i = 0; i < 8; ++i) { aA = mfma(woA[i], xa[i], aA); aB = mfma(woB[i], xa[i], aB); }
            SBAR();
            STAMP(16 + L * 32);
            {
                const unsigned char* src = p.ws + OFF_W2 + ((2 * j + (w & 1)) * KSFP + (w >> 1) * 22 + W2_REG) * 1024 + vl;
#pragma unroll
                for (int i = 0; i < 5; ++i) glds16_sc1(src + i * 1024, lbase + L_W2 + (w * 11 + i) * 1024);
            }
            STAMP(17 + L * 32);
            float o[8];
            reduce4(lds, tb * 24576, aA, aB, kq, lane, o);
#pragma unroll
            for (int i = 0; i < 4; ++i) resid[i] = pk(bflo(resid[i]) + o[2 * i], bfhi(resid[i]) + o[2 * i + 1]);
            st_sc1(swap_order(resid), rw, vl, OFF_X1 + L * ACT_BYTES + (tbg * KSD + sown) * 1024);
            STAMP(18 + L * 32);
            publish(cnt + cnt_word(L, CNT_X1, c));
            STAMP(19 + L * 32);
        }
        u32x4 w2A[W2_REG], w2B[W2_REG];
        {
            PHASE_IDS(); W1_IDS();
            LOAD_W1(16);
            if (w == 1 || w == 2 || w == 3) {
                const int col = min(32 * cb0 + tid - 64, F - 1);
                *(LAS float*)(lds + L_TAB + (tid - 64) * 4) = __uint_as_float(__builtin_amdgcn_raw_buffer_load_b32(rw, col * 4, OFF_CS1, AUX_SC1));
                *(LAS float*)(lds + L_TAB + 768 + (tid - 64) * 4) = bf2f(p.b1[col]);
            }
            STAMP(20 + L * 32);
            wait_cnt(cnt + cnt_word(L, CNT_X1, c), 8u, tmo);
            STAMP(21 + L * 32);
            {
                const unsigned char* src = p.ws + OFF_X1 + L * ACT_BYTES + (2 * c * KSD + 2 * w) * 1024 + vl;
#pragma unroll
                for (int ch = 0; ch < 2; ++ch)
#pragma unroll
                    for (int t = 0; t < 2; ++t)
#pragma unroll
                        for (int i = 0; i < 2; ++i) glds16_sc1(src + (t * KSD + 16 * ch + i) * 1024, lbase + L_ACT + (t * KSD + 16 * ch + 2 * w + i) * 1024);
            }
            WAITVM(4);
            LDS_BARRIER();
            STAMP(22 + L * 32);
            f32x16 a0 = {}, a1 = {};
            float s1[2] = {0.f, 0.f}, s2[2] = {0.f, 0.f};
            const unsigned char* w2l_src = p.ws + OFF_W2 + ((2 * j + (w & 1)) * KSFP + (w >> 1) * 22 + W2_REG) * 1024 + vl;
            auto w2_req = [&](int n) {
                if (n < 6) glds16_sc1(w2l_src + (5 + n) * 1024, lbase + L_W2 + (w * 11 + 5 + n) * 1024);
                else if (n < 6 + W2_REG) w2A[n - 6] = ld_sc1(rw, vw, OFF_W2 + (cbA * KSFP + kq * 22 + n - 6) * 1024);
                else if (n < 6 + 2 * W2_REG) w2B[n - 6 - W2_REG] = ld_sc1(rw, vw, OFF_W2 + (cbB * KSFP + kq * 22 + n - 6 - W2_REG) * 1024);
            };
            auto part = [&](int lo) {
                const int n0 = lo == 0 ? 0 : 14;
                if (TB == 0) {
                    u32x4 b0[3], b1[3];
#pragma unroll
                    for (int i = 0; i < 2; ++i) { b0[i] = lds_ld(lds, L_ACT + (lo + i) * 1024 + vl); b1[i] = lds_ld(lds, L_ACT + (KSD + lo + i) * 1024 + vl); }
#pragma unroll
                    for (int s = 0; s < 16; ++s) {
                        if (s + 2 < 16) { b0[(s + 2) % 3] = lds_ld(lds, L_ACT + (lo + s + 2) * 1024 + vl); b1[(s + 2) % 3] = lds_ld(lds, L_ACT + (KSD + lo + s + 2) * 1024 + vl); }
                        SBAR();
                        a0 = mfma(w1f[lo + s], b0[s % 3], a0); a1 = mfma(w1f[lo + s], b1[s % 3], a1);
                        PIN2(a0, a1);
                        if (s < 14) w2_req(n0 + s);
                        SBAR();
                    }
                } else {
                    u32x4 b0[3], b1[3];
                    const int ab = L_ACT + (lo + par) * 1024 + vl;
#pragma unroll
                    for (int i = 0; i < 2; ++i) { b0[i] = lds_ld(lds, ab + 2 * i * 1024); b1[i] = lds_ld(lds, ab + (KSD + 2 * i) * 1024); }
#pragma unroll
                    for (int s = 0; s < 8; ++s) {
                        if (s + 2 < 8) { b0[(s + 2) % 3] = lds_ld(lds, ab + 2 * (s + 2) * 1024); b1[(s + 2) % 3] = lds_ld(lds, ab + (KSD + 2 * (s + 2)) * 1024); }
                        SBAR();
                        a0 = mfma(w1f[lo + s], b0[s % 3], a0); a1 = mfma(w1f[lo + s], b1[s % 3], a1);
                        PIN2(a0, a1);
                        if (s < 7) { w2_req(n0 + 2 * s); w2_req(n0 + 2 * s + 1); }
                        SBAR();
                    }
                }
            };
            auto stats = [&](int lo) {
#pragma unroll
                for (int t = 0; t < 2; ++t)
#pragma unroll
                    for (int i = 0; i < 2; ++i) stats_acc(lds_ld(lds, L_ACT + (t * KSD + lo + 2 * w + i) * 1024 + vl), s1[t], s2[t]);
            };
            stats(0);
            SBAR();
            part(0);
            STAMP(23 + L * 32);
            WAITVM(14);
            LDS_BARRIER();
            stats(16);
            {
                LAS f32x2* st = (LAS f32x2*)(lds + L_STAT);
#pragma unroll
                for (int t = 0; t < 2; ++t) { const float u1 = half_sum(s1[t]), u2 = half_sum(s2[t]); if (h == 0) st[(t * 8 + w) * 32 + r] = (f32x2){u1, u2}; }
            }
            SBAR();
            part(16);
            STAMP(24 + L * 32);
            if (TB == 1) {
                const int xo = L_ACT + (w - 4) * 4096 + vl;
#pragma unroll
                for (int g = 0; g < 4; ++g) {
                    const f32x4 v = par ? (f32x4){a0[4 * g], a0[4 * g + 1], a0[4 * g + 2], a0[4 * g + 3]} : (f32x4){a1[4 * g], a1[4 * g + 1], a1[4 * g + 2], a1[4 * g + 3]};
                    lds_stf(lds, xo + g * 1024, v);
                }
            }
            LDS_BARRIER();
            if (TB == 1) {
                const int xi = L_ACT + ((w - 4) ^ 1) * 4096 + vl;
#pragma unroll
                for (int g = 0; g < 4; ++g) {
                    const f32x4 v = lds_ldf(lds, xi + g * 1024);
#pragma unroll
                    for (int i = 0; i < 4; ++i) a0[4 * g + i] = (par ? a1[4 * g + i] : a0[4 * g + i]) + v[i];
                }
            }
            STAMP(25 + L * 32);
            if (d_active) {
                auto finish = [&](const f32x16& acc, int t) {
                    LAS f32x2* st = (LAS f32x2*)(lds + L_STAT);
                    float t1 = 0.f, t2 = 0.f;
#pragma unroll
                    for (int i = 0; i < 8; ++i) { f32x2 v = st[(t * 8 + i) * 32 + r]; t1 += v[0]; t2 += v[1]; }
                    float mu, rstd; ln_from_sums(t1, t2, mu, rstd);
                    const float nmr = -mu * rstd;
                    unsigned hp[8];
#pragma unroll
                    for (int g = 0; g < 4; ++g) {
                        const f32x4 cs = lds_ldf(lds, L_TAB + (32 * cb1 + 8 * g + 4 * h) * 4), bb = lds_ldf(lds, L_TAB + 768 + (32 * cb1 + 8 * g + 4 * h) * 4);
                        float y[4];
#pragma unroll
                        for (int i = 0; i < 4; ++i) {
                            const float x = fmaf(acc[4 * g + i], rstd, fmaf(cs[i], nmr, bb[i]));
                            const float t = x * fmaf(x * x, -2.f * 0.7978845608028654f * 0.044715f * 1.4426950408889634f, -2.f * 0.7978845608028654f * 1.4426950408889634f);
                            y[i] = x * __builtin_amdgcn_rcpf(1.f + __builtin_amdgcn_exp2f(t));
                        }
                        hp[2 * g] = pk(y[0], y[1]); hp[2 * g + 1] = pk(y[2], y[3]);
                    }
                    const int so = OFF_H + L * H_BYTES + ((2 * c + t) * KSF + 2 * cbg1) * 1024;
                    st_sc1(swap_order((u32x4){hp[0], hp[1], hp[2], hp[3]}), rw, vl, so);
                    st_sc1(swap_order((u32x4){hp[4], hp[5], hp[6], hp[7]}), rw, vl, so + 1024);
                };
                if (TB == 0) { finish(a0, 0); finish(a1, 1); } else finish(a0, par);
            }
            STAMP(26 + L * 32);
            publish(cnt + cnt_word(L, CNT_H, c));
            STAMP(27 + L * 32);
        }
        {
            PHASE_IDS();
            const u32x2 bb0 = *(const GAS u32x2*)(p.b2 + 16 * sown + 4 * h), bb1 = *(const GAS u32x2*)(p.b2 + 16 * sown + 8 + 4 * h);
            wait_cnt(cnt + cnt_word(L, CNT_H, c), 8u, tmo);
            STAMP(28 + L * 32);
            u32x4 ha[22];
            const int hso = OFF_H + L * H_BYTES + tbg * KSF * 1024;
            constexpr int EWIN = 8;
#pragma unroll
            for (int i = 0; i < EWIN; ++i) ha[i] = ld_sc1(rw, vl, hso + min(kq * 22 + i, KSF - 1) * 1024);
            SBAR();
            f32x16 aA = {}, aB = {};
            const int oa_ = L_W2 + (kq * 22 + (kq >> 1) * 11) * 1024 + vw, ob_ = L_W2 + (kq * 22 + ((kq >> 1) ^ 1) * 11) * 1024 + vw;
            u32x4 la[3], lb[3];
#pragma unroll
            for (int i = 0; i < 22; ++i) {
                if (i + EWIN < 22) ha[i + EWIN] = ld_sc1(rw, vl, hso + min(kq * 22 + i + EWIN, KSF - 1) * 1024);
                if (i + 2 >= W2_REG && i + 2 < 22) { la[(i + 2 - W2_REG) % 3] = lds_ld(lds, oa_ + (i + 2 - W2_REG) * 1024); lb[(i + 2 - W2_REG) % 3] = lds_ld(lds, ob_ + (i + 2 - W2_REG) * 1024); }
                SBAR();
                if (i < W2_REG) { aA = mfma(w2A[i], ha[i], aA); aB = mfma(w2B[i], ha[i], aB); }
                else { aA = mfma(la[(i - W2_REG) % 3], ha[i], aA); aB = mfma(lb[(i - W2_REG) % 3], ha[i], aB); }
                PIN2(aA, aB);
                SBAR();
            }
            STAMP(29 + L * 32);
            float o[8];
            reduce4(lds, tb * 24576, aA, aB, kq, lane, o);
#pragma unroll
            for (int i = 0; i < 2; ++i) { o[2 * i] += bflo(bb0[i]); o[2 * i + 1] += bfhi(bb0[i]); o[4 + 2 * i] += bflo(bb1[i]); o[4 + 2 * i + 1] += bfhi(bb1[i]); }
#pragma unroll
            for (int i = 0; i < 4; ++i) resid[i] = pk(bflo(resid[i]) + o[2 * i], bfhi(resid[i]) + o[2 * i + 1]);
            STAMP(30 + L * 32);
            const u32x4 xo = swap_order(resid);
            if (L == 0) {
                st_sc1(xo, rw, vl, OFF_X2 + (tbg * KSD + sown) * 1024);
                publish(cnt + cnt_word(0, CNT_X2, c));
            } else {
                *(GAS u32x4*)(p.out + (size_t)(64 * c + 32 * tb + r) * D + 16 * sown + 8 * h) = xo;
            }
            STAMP(31 + L * 32);
        }
    }
    if (blockIdx.x == 0 && threadIdx.x == 0) __hip_atomic_store(sync, epoch + 2u, RLX_AGENT);
}

__global__ void __launch_bounds__(512, 2) main_kernel(Params p) {
    extern __shared__ __attribute__((aligned(16))) char lds_dyn[];
    LAS char* lds = (LAS char*)lds_dyn;
    if (__builtin_amdgcn_readfirstlane(threadIdx.x >> 8) == 0) run_layers<0>(p, lds); else run_layers<1>(p, lds);
}

}

extern "C" void kernel_launch(void* const* d_in, const int* in_sizes, int n_in, void* d_out, int out_size, void* d_ws, size_t ws_size, hipStream_t stream) {
    Params p;
    p.x = (const uint16_t*)d_in[0]; p.wq = (const uint16_t*)d_in[1]; p.wk = (const uint16_t*)d_in[2]; p.wv = (const uint16_t*)d_in[3]; p.wo = (const uint16_t*)d_in[4];
    p.w1 = (const uint16_t*)d_in[6]; p.b1 = (const uint16_t*)d_in[7]; p.w2 = (const uint16_t*)d_in[8]; p.b2 = (const uint16_t*)d_in[9];
    p.out = (uint16_t*)d_out; p.ws = (unsigned char*)d_ws;
    (void)hipFuncSetAttribute((const void*)main_kernel, hipFuncAttributeMaxDynamicSharedMemorySize, LDS_BYTES);
    hipLaunchKernelGGL(main_kernel, dim3(256), dim3(512), LDS_BYTES, stream, p);
}
```

```cpp
#include <hip/hip_runtime.h>
#include <hip/hip_bf16.h>
#include <stdint.h>

#define GAS __attribute__((address_space(1)))
#define LAS __attribute__((address_space(3)))
typedef __bf16 bf16x8 __attribute__((ext_vector_type(8)));
typedef __bf16 bf16x2 __attribute__((ext_vector_type(2)));
typedef float f32x16 __attribute__((ext_vector_type(16)));
typedef float f32x4 __attribute__((ext_vector_type(4)));
typedef float f32x2 __attribute__((ext_vector_type(2)));
typedef unsigned u32x4 __attribute__((ext_vector_type(4)));
typedef GAS unsigned gu32;
#define RLX_AGENT __ATOMIC_RELAXED, __HIP_MEMORY_SCOPE_AGENT

namespace {

constexpr int S = 2048, D = 512, F = 1376;
constexpr int KSD = 32;
constexpr int KSF = 86;
constexpr int KSFP = 88;
constexpr float LN_EPS = 1e-5f;
constexpr float QSCALE = 0.25f * 1.4426950408889634f;
constexpr float SAFE_BOUND = 50.f;

constexpr int CW = 64;
constexpr int OFF_CNT = 0;
constexpr int CNT_O = 0, CNT_X1 = 1, CNT_H = 2, CNT_X2 = 3;
__device__ __forceinline__ int cnt_word(int layer, int kind, int c) { return ((layer * 4 + kind) * 32 + c) * CW; }
constexpr int OFF_SYNC = 65536;
constexpr int OFF_KVFL = OFF_SYNC + 1024;
constexpr int OFF_WO = OFF_KVFL + 2 * 32 * 512;
constexpr int OFF_W1 = OFF_WO + 16 * KSD * 1024;
constexpr int OFF_W2 = OFF_W1 + 43 * KSD * 1024;
constexpr int OFF_CS1 = OFF_W2 + 16 * KSFP * 1024;
constexpr int OFF_KF = (OFF_CS1 + 1408 * 4 + 1023) / 1024 * 1024;
constexpr int OFF_VF = OFF_KF + 2 * 65536;
constexpr int OFF_O = OFF_VF + 2 * 65536;
constexpr int ACT_BYTES = 64 * KSD * 1024;
constexpr int OFF_X1 = OFF_O + 2 * ACT_BYTES;
constexpr int OFF_X2 = OFF_X1 + 2 * ACT_BYTES;
constexpr int H_BYTES = 64 * KSF * 1024;
constexpr int OFF_H = OFF_X2 + ACT_BYTES;
constexpr int WS_TOTAL = OFF_H + 2 * H_BYTES;
constexpr unsigned SPIN_CAP = 1u << 15;
constexpr unsigned PREP_WGS = 192;

constexpr int L_K = 0, L_V = 65536, L_ONES = 131072, L_XCHO = 135424;
constexpr int L_WQ = 65536, L_WKV = 0, L_PREP = 0, L_KVRED = 32768, L_XCHQ = 57344;
constexpr int L_ACT = 0, L_W2A = 65536, L_W2B = 0;
constexpr int L_STAT = 155648, L_TAB = 159744, L_CS = L_TAB, L_FLAG = 163584;
constexpr int F_UNSAFE = L_FLAG, F_KM2 = L_FLAG + 32, F_N2 = L_FLAG + 36, F_KVCNT = L_FLAG + 40, F_OCNT = L_FLAG + 44, F_PAIR = L_FLAG + 64;
constexpr int LDS_BYTES = 163840;
constexpr int W2_REG = 3, W2_A = 11, W2_B = 8;
constexpr int W1_EARLY = 24;

#define PROF 0
#define PROF_TID 0
constexpr int L_PROF = 161280;
constexpr int OFF_PROF = 64 << 20;
#define STAMP(i) do { } while (0)

struct Params {
    const uint16_t *x, *wq, *wk, *wv, *wo, *w1, *b1, *w2, *b2;
    uint16_t* out;
    unsigned char* ws;
};

__device__ __forceinline__ f32x16 mfma(u32x4 a, u32x4 b, f32x16 c) {
    return __builtin_amdgcn_mfma_f32_32x32x16_bf16(__builtin_bit_cast(bf16x8, a), __builtin_bit_cast(bf16x8, b), c, 0, 0, 0);
}
__device__ __forceinline__ unsigned pk(float a, float b) { f32x2 v = {a, b}; return __builtin_bit_cast(unsigned, __builtin_convertvector(v, bf16x2)); }
__device__ __forceinline__ float bflo(unsigned u) { return __uint_as_float(u << 16); }
__device__ __forceinline__ float bfhi(unsigned u) { return __uint_as_float(u & 0xffff0000u); }
__device__ __forceinline__ float bf2f(uint16_t v) { return __uint_as_float((unsigned)v << 16); }
__device__ __forceinline__ float dot2(unsigned a, unsigned b, float c) {
    return __builtin_amdgcn_fdot2_f32_bf16(__builtin_bit_cast(bf16x2, a), __builtin_bit_cast(bf16x2, b), c, false);
}
__device__ __forceinline__ float half_sum(float v) {
    auto r = __builtin_amdgcn_permlane32_swap(__float_as_uint(v), __float_as_uint(v), false, false);
    return __uint_as_float(r[0]) + __uint_as_float(r[1]);
}
__device__ __forceinline__ float half_max(float v) {
    auto r = __builtin_amdgcn_permlane32_swap(__float_as_uint(v), __float_as_uint(v), false, false);
    return fmaxf(__uint_as_float(r[0]), __uint_as_float(r[1]));
}
__device__ __forceinline__ u32x4 swap_order(u32x4 a) {
    auto r0 = __builtin_amdgcn_permlane32_swap(a[0], a[2], false, false);
    auto r1 = __builtin_amdgcn_permlane32_swap(a[1], a[3], false, false);
    return (u32x4){r0[0], r1[0], r0[1], r1[1]};
}
template <int CTRL> __device__ __forceinline__ float dpp(float v) {
    return __uint_as_float(__builtin_amdgcn_update_dpp(0u, __float_as_uint(v), CTRL, 0xf, 0xf, true));
}
__device__ __forceinline__ float wave_max(float v) {
    v = fmaxf(v, dpp<0xB1>(v));
    v = fmaxf(v, dpp<0x4E>(v));
    v = fmaxf(v, dpp<0x141>(v));
    v = fmaxf(v, dpp<0x140>(v));
    auto r = __builtin_amdgcn_permlane16_swap(__float_as_uint(v), __float_as_uint(v), false, false);
    return half_max(fmaxf(__uint_as_float(r[0]), __uint_as_float(r[1])));
}
#define SBAR() __builtin_amdgcn_sched_barrier(0)
#define PIN2(a, b) asm volatile("" : "+v"(a), "+v"(b))
#define LDS_BARRIER() asm volatile("s_waitcnt lgkmcnt(0)\n\ts_barrier" ::: "memory")
#define DRAIN() asm volatile("s_waitcnt vmcnt(0)" ::: "memory")
constexpr int AUX_SC1 = 16;
typedef __amdgpu_buffer_rsrc_t rsrc_t;
__device__ __forceinline__ u32x4 ld_sc1(rsrc_t r, int voff, int soff) { return __builtin_amdgcn_raw_buffer_load_b128(r, voff, soff, AUX_SC1); }
__device__ __forceinline__ u32x4 ld_plain(rsrc_t r, int voff, int soff) { return __builtin_amdgcn_raw_buffer_load_b128(r, voff, soff, 0); }
__device__ __forceinline__ void st_sc1(u32x4 v, rsrc_t r, int voff, int soff) {
    __builtin_amdgcn_raw_buffer_store_b128(v, r, voff, soff, AUX_SC1);
    asm volatile("s_nop 1" :: "v"(v) : "memory");
}
__device__ __forceinline__ u32x4 lds_ld(LAS char* l, int off) { return *(LAS u32x4*)(l + off); }
__device__ __forceinline__ void lds_st(LAS char* l, int off, u32x4 v) { *(LAS u32x4*)(l + off) = v; }
__device__ __forceinline__ f32x4 lds_ldf(LAS char* l, int off) { return *(LAS f32x4*)(l + off); }
__device__ __forceinline__ void lds_stf(LAS char* l, int off, f32x4 v) { *(LAS f32x4*)(l + off) = v; }

__device__ __forceinline__ void publish(int tid, gu32* cnt) {
    DRAIN();
    LDS_BARRIER();
    if (tid == 0) __hip_atomic_fetch_add(cnt, 1u, RLX_AGENT);
}
__device__ __forceinline__ void poll_ge(gu32* cnt, unsigned want, gu32* tmo) {
    unsigned spins = 0;
    while (__hip_atomic_load(cnt, RLX_AGENT) < want) {
        if (++spins > SPIN_CAP) { __hip_atomic_store(tmo, 1u, RLX_AGENT); break; }
    }
}
__device__ __forceinline__ void wait_cnt(int tid, gu32* cnt, unsigned want, gu32* tmo) {
    if (tid == 0) poll_ge(cnt, want, tmo);
    __builtin_amdgcn_fence(__ATOMIC_ACQUIRE, "wavefront");
    LDS_BARRIER();
}
typedef GAS unsigned long long gu64;
__device__ __forceinline__ float wait_flags(int tid, LAS char* lds, gu64* fl, unsigned tag, gu32* tmo) {
    if (tid < 64) {
        unsigned spins = 0; unsigned long long v;
        while (v = __hip_atomic_load(fl + tid, RLX_AGENT), !__all((unsigned)(v >> 32) == tag)) {
            if (++spins > SPIN_CAP) { if (tid == 0) __hip_atomic_store(tmo, 1u, RLX_AGENT); break; }
        }
        const float m = wave_max(__uint_as_float((unsigned)v));
        if (tid == 0) *(LAS float*)(lds + F_KM2) = m;
    }
    __builtin_amdgcn_fence(__ATOMIC_ACQUIRE, "wavefront");
    LDS_BARRIER();
    return *(LAS float*)(lds + F_KM2);
}
__device__ __forceinline__ void glds16(const void* g, unsigned lds_dst) {
    unsigned keep;
    asm volatile("s_mov_b32 %0, m0\n\ts_mov_b32 m0, %2\n\ts_nop 0\n\tglobal_load_lds_dwordx4 %1, off\n\ts_mov_b32 m0, %0" : "=&s"(keep) : "v"(g), "s"(lds_dst) : "memory");
}
__device__ __forceinline__ void glds16_sc1(const void* g, unsigned lds_dst) {
    unsigned keep;
    asm volatile("s_mov_b32 %0, m0\n\ts_mov_b32 m0, %2\n\ts_nop 0\n\tglobal_load_lds_dwordx4 %1, off sc1\n\ts_mov_b32 m0, %0" : "=&s"(keep) : "v"(g), "s"(lds_dst) : "memory");
}
#define WAITVM(n) asm volatile("s_waitcnt vmcnt(" #n ")" ::: "memory")

__device__ __forceinline__ void reduce4(LAS char* lds, int region, const f32x16& aA, const f32x16& aB, int kq, int lane, float (&o)[8]) {
    const int base = region + lane * 16;
    { const int a = base + ((kq ^ 1) * 3 + 0) * 2048;
      lds_stf(lds, a, (f32x4){aA[8], aA[9], aA[10], aA[11]}); lds_stf(lds, a + 1024, (f32x4){aA[12], aA[13], aA[14], aA[15]}); }
    { const int a = base + ((kq ^ 2) * 3 + 1) * 2048;
      lds_stf(lds, a, (f32x4){aB[0], aB[1], aB[2], aB[3]}); lds_stf(lds, a + 1024, (f32x4){aB[4], aB[5], aB[6], aB[7]}); }
    { const int a = base + ((kq ^ 3) * 3 + 2) * 2048;
      lds_stf(lds, a, (f32x4){aB[8], aB[9], aB[10], aB[11]}); lds_stf(lds, a + 1024, (f32x4){aB[12], aB[13], aB[14], aB[15]}); }
    LDS_BARRIER();
    f32x4 lo = {aA[0], aA[1], aA[2], aA[3]}, hi = {aA[4], aA[5], aA[6], aA[7]}, t[6];
#pragma unroll
    for (int x = 0; x < 6; ++x) t[x] = lds_ldf(lds, base + kq * 3 * 2048 + x * 1024);
    SBAR();
#pragma unroll
    for (int x = 0; x < 3; ++x) { lo += t[2 * x]; hi += t[2 * x + 1]; }
#pragma unroll
    for (int i = 0; i < 4; ++i) { o[i] = lo[i]; o[4 + i] = hi[i]; }
}
typedef short v4i16 __attribute__((ext_vector_type(4)));
typedef unsigned u32x2 __attribute__((ext_vector_type(2)));
__device__ __forceinline__ u32x4 tr_frag(LAS char* lds, int a, int step) {
    const u32x2 lo = __builtin_bit_cast(u32x2, __builtin_amdgcn_ds_read_tr16_b64_v4i16((LAS v4i16*)(lds + a)));
    const u32x2 hi = __builtin_bit_cast(u32x2, __builtin_amdgcn_ds_read_tr16_b64_v4i16((LAS v4i16*)(lds + a + step)));
    return (u32x4){lo[0], lo[1], hi[0], hi[1]};
}
__device__ __forceinline__ float frag_sum(u32x4 f, float s) {
#pragma unroll
    for (int d = 0; d < 4; ++d) s = dot2(f[d], 0x3f803f80u, s);
    return s;
}

#define A_E1(a) "v_exp_f32 v" #a ", v" #a "\n\t"
#define A_E4(a, b, c, d) A_E1(a) A_E1(b) A_E1(c) A_E1(d)
#define A_CVT(d, a, b) "v_cvt_pk_bf16_f32 v" #d ", v" #a ", v" #b "\n\t"
#define A_DSK(off) "ds_read_b128 v[100:103], v120 offset:" #off "\n\t"
#define A_DSV0(off) "ds_read_b128 v[104:107], v121 offset:" #off "\n\t"
#define A_DSV1(off) "ds_read_b128 v[108:111], v121 offset:" #off "\n\t"
#define A_WAIT(n) "s_waitcnt lgkmcnt(" #n ")\n\t"
#define A_MSB "v_mfma_f32_32x32x16_bf16 v[48:63], v[100:103], v[156:159], v[124:139]\n\t"
#define A_MSA "v_mfma_f32_32x32x16_bf16 v[32:47], v[100:103], v[96:99], v[64:79]\n\t"
#define A_MPV(O, V, P) "v_mfma_f32_32x32x16_bf16 " O ", " V ", " P ", " O "\n\t"
#define A_OA "v[80:95]"
#define A_OB "v[140:155]"
#define A_V0 "v[104:107]"
#define A_V1 "v[108:111]"
#define A_P0 "v[112:115]"
#define A_P1 "v[116:119]"
#define A_STEP(koff, v1off, v0off) \
    A_MSB A_DSK(koff) A_E4(32, 33, 34, 35) \
    A_MPV(A_OB, A_V1, A_P1) A_DSV1(v1off) A_E4(36, 37, 38, 39) \
    A_CVT(112, 32, 33) A_CVT(113, 34, 35) A_CVT(114, 36, 37) A_CVT(115, 38, 39) \
    A_E4(40, 41, 42, 43) \
    A_WAIT(2) A_MPV(A_OA, A_V0, A_P0) A_E4(44, 45, 46, 47) \
    A_CVT(116, 40, 41) A_CVT(117, 42, 43) A_CVT(118, 44, 45) A_CVT(119, 46, 47) \
    A_WAIT(1) A_MSA A_E4(48, 49, 50, 51) \
    A_WAIT(0) A_MPV(A_OA, A_V1, A_P1) A_E4(52, 53, 54, 55) \
    A_CVT(112, 48, 49) A_CVT(113, 50, 51) A_CVT(114, 52, 53) A_CVT(115, 54, 55) \
    A_E4(56, 57, 58, 59) \
    A_MPV(A_OB, A_V0, A_P0) A_DSV0(v0off) A_E4(60, 61, 62, 63) \
    A_CVT(116, 56, 57) A_CVT(117, 58, 59) A_CVT(118, 60, 61) A_CVT(119, 62, 63)

struct AttnState { f32x16 sa, sb, oa, ob, negma, negmb; u32x4 qa, qb, kf, v0, v1, p1; unsigned kaddr, vaddr, vstride; };
#define A_OPERANDS(st, cnt) \
        : "+{v[32:47]}"(st.sa), "+{v[48:63]}"(st.sb), "+{v[80:95]}"(st.oa), "+{v[140:155]}"(st.ob), "+{v[100:103]}"(st.kf), "+{v[104:107]}"(st.v0), "+{v[108:111]}"(st.v1), \
          "={v[112:115]}"(p0), "+{v[116:119]}"(st.p1), "+{v120}"(st.kaddr), "+{v121}"(st.vaddr), [n] "+s"(cnt) \
        : "{v[64:79]}"(st.negma), "{v[124:139]}"(st.negmb), "{v[96:99]}"(st.qa), "{v[156:159]}"(st.qb), "{v122}"(st.vstride) \
        : "memory", "scc"
#define A_ITER \
        A_STEP(1024, 512, 1024) \
        A_STEP(2048, 1536, 2048) \
        A_STEP(3072, 2560, 3072) \
        A_STEP(4096, 3584, 4096) \
        "v_add_u32 v120, 4096, v120\n\t" \
        "v_add_u32 v121, v122, v121\n\t"
__device__ __forceinline__ void attn_begin(AttnState& st, LAS char* lds, u32x4 qa, u32x4 qb, float ma, float mb, unsigned kaddr, unsigned vaddr, unsigned vstride) {
#pragma unroll
    for (int i = 0; i < 16; ++i) { st.negma[i] = -ma; st.negmb[i] = -mb; }
    st.qa = qa; st.qb = qb;
    st.kf = lds_ld(lds, kaddr); st.v0 = lds_ld(lds, vaddr); st.v1 = st.v0; st.p1 = (u32x4){0u, 0u, 0u, 0u};
    st.sa = mfma(st.kf, qa, st.negma); st.sb = st.sa;
    st.oa = (f32x16){}; st.ob = (f32x16){};
    const unsigned lbase = (unsigned)(uintptr_t)lds;
    st.kaddr = kaddr + lbase; st.vaddr = vaddr + lbase; st.vstride = vstride;
}
#define A_DMA \
    "s_mov_b32 m0, s92\n\t" \
    "s_add_u32 s92, s92, 1024\n\t" \
    "global_load_lds_dwordx4 v123, s[90:91] sc1\n\t" \
    "s_add_u32 s90, s90, 1024\n\t" \
    "s_addc_u32 s91, s91, 0\n\t"
#define A_ITER_DMA \
        A_DMA A_DMA A_STEP(1024, 512, 1024) \
        A_DMA A_STEP(2048, 1536, 2048) \
        A_DMA A_STEP(3072, 2560, 3072) \
        A_DMA A_STEP(4096, 3584, 4096) \
        "v_add_u32 v120, 4096, v120\n\t" \
        "v_add_u32 v121, v122, v121\n\t"
__device__ __forceinline__ void attn_first(AttnState& st, unsigned long long src, unsigned dst, unsigned voff) {
    unsigned cnt = 2, keep; u32x4 p0;
    asm volatile(
        "s_mov_b32 %[keep], m0\n\t"
        "s_nop 15\n\t"
        A_ITER_DMA
        A_ITER_DMA
        "s_mov_b32 m0, %[keep]\n\t"
        "s_waitcnt vmcnt(0)\n\t"
        "s_barrier\n\t"
        "ATT1_LOOP_%=:\n\t"
        A_ITER
        "s_sub_u32 %[n], %[n], 1\n\t"
        "s_cmp_lg_u32 %[n], 0\n\t"
        "s_cbranch_scc1 ATT1_LOOP_%=\n\t"
        : "+{v[32:47]}"(st.sa), "+{v[48:63]}"(st.sb), "+{v[80:95]}"(st.oa), "+{v[140:155]}"(st.ob), "+{v[100:103]}"(st.kf), "+{v[104:107]}"(st.v0), "+{v[108:111]}"(st.v1),
          "={v[112:115]}"(p0), "+{v[116:119]}"(st.p1), "+{v120}"(st.kaddr), "+{v121}"(st.vaddr), [n] "+s"(cnt), [keep] "=&s"(keep), "+{s[90:91]}"(src), "+{s92}"(dst)
        : "{v[64:79]}"(st.negma), "{v[124:139]}"(st.negmb), "{v[96:99]}"(st.qa), "{v[156:159]}"(st.qb), "{v122}"(st.vstride), "{v123}"(voff)
        : "memory", "scc");
}
__device__ __forceinline__ void attn_second(AttnState& st) {
    unsigned cnt = 4; u32x4 p0;
    asm volatile(
        "ATT2_LOOP_%=:\n\t"
        A_ITER
        "s_sub_u32 %[n], %[n], 1\n\t"
        "s_cmp_lg_u32 %[n], 0\n\t"
        "s_cbranch_scc1 ATT2_LOOP_%=\n\t"
        "s_waitcnt lgkmcnt(0)\n\t"
        A_MPV(A_OB, A_V1, A_P1)
        "s_nop 15\n\t"
        A_OPERANDS(st, cnt));
}

__device__ __forceinline__ void stats_acc(u32x4 f, float& s1, float& s2) {
#pragma unroll
    for (int d = 0; d < 4; ++d) { s1 = dot2(f[d], 0x3f803f80u, s1); s2 = dot2(f[d], f[d], s2); }
}
template <int N>
__device__ __forceinline__ void stat_sums(LAS char* lds, int slot0, int tok, float& t1, float& t2) {
    f32x2 v[N];
#pragma unroll
    for (int i = 0; i < N; ++i) v[i] = *(LAS f32x2*)(lds + L_STAT + ((slot0 + i) * 32 + tok) * 8);
    SBAR();
    t1 = 0.f; t2 = 0.f;
#pragma unroll
    for (int i = 0; i < N; ++i) { t1 += v[i][0]; t2 += v[i][1]; }
}
__device__ __forceinline__ void ln_from_sums(float t1, float t2, float& mu, float& rstd) {
    mu = t1 * (1.f / D);
    rstd = __builtin_amdgcn_rsqf(fmaxf(t2 * (1.f / D) - mu * mu, 0.f) + LN_EPS);
}

template <int TB>
__device__ __forceinline__ void run_layers(const Params& p, LAS char* lds) {
    const unsigned lbase = (unsigned)(uintptr_t)lds;
    const int tid0 = threadIdx.x, w0 = __builtin_amdgcn_readfirstlane(tid0 >> 6);
    const int kk0 = blockIdx.x >> 3, c0 = (blockIdx.x & 7) * 4 + (kk0 >> 3), j0 = kk0 & 7;
    const rsrc_t rw = __builtin_amdgcn_make_buffer_rsrc(p.ws, 0, WS_TOTAL, 0x00020000);
    const rsrc_t rx = __builtin_amdgcn_make_buffer_rsrc((void*)p.x, 0, S * D * 2, 0x00020000);
    gu32* cnt = (gu32*)(p.ws + OFF_CNT);
    gu32* sync = (gu32*)(p.ws + OFF_SYNC);
    gu32* tmo = sync + 2 * CW;
#define PHASE_IDS() \
    int tid = tid0, w = w0, c = c0, j = j0; unsigned epoch = epoch0; \
    asm volatile("" : "+v"(tid)); asm volatile("" : "+s"(w), "+s"(c), "+s"(j), "+s"(epoch)); \
    constexpr int tb = TB; const int lane = tid & 63, r = lane & 31, h = lane >> 5, kq = w & 3, tbg = 2 * c + tb; \
    const int vl = lane * 16;                                     \
    const int vw = (lane ^ ((kq & 1) << 4)) * 16;                 \
    const int cbA = 2 * j + (kq >> 1), cbB = 2 * j + ((kq >> 1) ^ 1); \
    const int sown = 4 * j + kq;                                  \
    (void)epoch; (void)r; (void)h; (void)tbg; (void)vl; (void)vw; (void)cbA; (void)cbB; (void)sown; (void)tb;
#define W1_IDS() \
    const int ncb = j < 3 ? 6 : 5, cb0 = j < 3 ? 6 * j : 18 + 5 * (j - 3);     \
    const int cb1 = TB == 0 ? kq : 4 + (kq >> 1);     \
    const bool d_active = cb1 < ncb; \
    const int cbg1 = cb0 + (d_active ? cb1 : 0); (void)cbg1; \
    const int par = TB == 0 ? 0 : (kq & 1); (void)par;

    STAMP(0);
    unsigned epoch0;
    { gu32* e = sync; asm volatile("" : "+s"(e)); epoch0 = __builtin_amdgcn_readfirstlane(__hip_atomic_load(e, RLX_AGENT)); }
    asm volatile("" : "+s"(epoch0));
    u32x4 resid;
    {
        PHASE_IDS();
        resid = swap_order(ld_plain(rx, r * 1024 + h * 16, (64 * c + 32 * tb) * 1024 + sown * 32));
        if (tid < 16) *(LAS unsigned*)(lds + (tid < 8 ? F_PAIR + tid * 4 : F_OCNT)) = 0u;
        if (j == tb && kq == 0 && lane < 4) __hip_atomic_store(cnt + cnt_word(j, lane, c), 0u, RLX_AGENT);
        if (c == 0 && j == 0 && tid >= 4 && tid < 6) __hip_atomic_store(sync + (tid - 3) * CW, 0u, RLX_AGENT);
    }

#pragma unroll 1
    for (int L = 0; L < 2; ++L) {
        u32x4 qown; float q2own;
        {
            PHASE_IDS();
            const bool kvm = j < 2;
            const bool kvteam = j == tb;
            if (kvm && kvteam) {
                __builtin_amdgcn_s_setprio(2);
                const uint16_t* src = ((lane & 2) ? p.wv : p.wk) + (128 * kq + (lane >> 2)) * 16 + (lane & 1) * 8;
#pragma unroll
                for (int i = 0; i < 8; ++i) glds16(src + i * 256, lbase + L_WKV + (kq * 8 + i) * 1024);
            } else {
                const int row = lane >> 3, pc = lane & 7;
                const uint16_t* src = p.wq + (size_t)(64 * w + row) * D + 64 * j + (((pc >> 2) ^ ((row >> 1) & 1)) * 32 + (pc & 3) * 8);
                if (L == 0) {
#pragma unroll
                    for (int i = 0; i < 4; ++i) glds16(src + i * 8 * D, lbase + L_WQ + (w * 8 + i) * 1024);
                }
#pragma unroll
                for (int i = 4; i < 8; ++i) glds16(src + i * 8 * D, lbase + L_WQ + (w * 8 + i) * 1024);
                if (kvm) {
                    const int w2 = w ^ 4;
                    const uint16_t* src2 = p.wq + (size_t)(64 * w2 + row) * D + 64 * j + (((pc >> 2) ^ ((row >> 1) & 1)) * 32 + (pc & 3) * 8);
                    if (L == 0) {
#pragma unroll
                        for (int i = 0; i < 4; ++i) glds16(src2 + i * 8 * D, lbase + L_WQ + (w2 * 8 + i) * 1024);
                    }
#pragma unroll
                    for (int i = 4; i < 8; ++i) glds16(src2 + i * 8 * D, lbase + L_WQ + (w2 * 8 + i) * 1024);
                }
            }
            auto prep_dst = [&](int i) -> int {
                const int u = c * 6 + (j - 2);
                if (u < 43) return OFF_W1 + (u * KSD + 4 * w + i) * 1024;
                const int f = ((u - 43) * 8 + w) + i * 1192;
                if (i >= 2 || f >= 1920) return -1;
                return f < 512 ? OFF_WO + f * 1024 : OFF_W2 + (f - 512) * 1024;
            };
            if (L == 0 && !kvm) {
#pragma unroll
                for (int i = 0; i < 4; ++i) {
                    const int d = prep_dst(i);
                    if (d < 0) continue;
                    const uint16_t* src; int ld, cb, s;
                    if (d >= OFF_W2) { const int g = (d - OFF_W2) >> 10; src = p.w2; ld = D; cb = g / KSFP; s = min(g % KSFP, KSF - 1); }
                    else if (d >= OFF_W1) { const int g = (d - OFF_W1) >> 10; src = p.w1; ld = F; cb = g >> 5; s = g & 31; }
                    else { const int g = (d - OFF_WO) >> 10; src = p.wo; ld = D; cb = g >> 5; s = g & 31; }
                    glds16(src + (size_t)(16 * s + (lane >> 2)) * ld + 32 * cb + (lane & 3) * 8, lbase + L_PREP + (w * 4 + i) * 1024);
                }
            }
            u32x4 xa[8] = {};
            if (L == 0) {
#pragma unroll
                for (int i = 0; i < 8; ++i) xa[i] = ld_plain(rx, r * 1024 + h * 16, (64 * c + 32 * tb) * 1024 + (kq * 8 + i) * 32);
                SBAR();
                if (!kvm || kvteam) WAITVM(8);
            } else {
                if (tid == (kvm ? 256 * j : 0)) poll_ge(cnt + cnt_word(0, CNT_X2, c), 8u, tmo);
                __builtin_amdgcn_fence(__ATOMIC_ACQUIRE, "wavefront");
                if (!kvm || kvteam) WAITVM(0);
            }
            LDS_BARRIER();
            STAMP(1 + L * 32);
            if (L == 1) {
#pragma unroll
                for (int i = 0; i < 8; ++i) xa[i] = ld_sc1(rw, vl, OFF_X2 + (tbg * KSD + kq * 8 + i) * 1024);
                SBAR();
            }
            LAS f32x2* st = (LAS f32x2*)(lds + L_STAT);
            LAS float* cst = (LAS float*)(lds + L_CS);
            const int tq = (lane & 15) >> 2, tp = lane & 3, tg = (lane >> 4) & 1;
            f32x16 aA = {}, aB = {};
            auto store_stats = [&]() {
                float s1 = 0.f, s2 = 0.f;
#pragma unroll
                for (int i = 0; i < 8; ++i) stats_acc(xa[i], s1, s2);
                s1 = half_sum(s1); s2 = half_sum(s2);
                if (h == 0) st[(tb * 8 + kq) * 32 + r] = (f32x2){s1, s2};
            };
            auto do_q = [&]() {
                u32x4 fa[8], fb[8];
                float csA = 0.f, csB = 0.f;
                const int sw = (tq >> 1) & 1, col = (tg ^ (kq & 1)) * 32 + tp * 8;
                const int a0 = L_WQ + (kq * 128 + 8 * h + tq) * 128 + col;
                const int aa = a0 + (((kq >> 1) ^ sw) * 64), ab = a0 + (((kq >> 1) ^ 1 ^ sw) * 64);
#pragma unroll
                for (int i = 0; i < 8; ++i) { fa[i] = tr_frag(lds, aa + i * 2048, 512); fb[i] = tr_frag(lds, ab + i * 2048, 512); }
                SBAR();
#pragma unroll
                for (int i = 0; i < 8; ++i) { csA = frag_sum(fa[i], csA); csB = frag_sum(fb[i], csB); }
                csA = half_sum(csA); csB = half_sum(csB);
                if (TB == 0 && h == 0) { const int n = r ^ ((kq & 1) << 4); cst[kq * 96 + (kq >> 1) * 32 + n] = csA; cst[kq * 96 + ((kq >> 1) ^ 1) * 32 + n] = csB; }
                SBAR();
#pragma unroll
                for (int i = 0; i < 8; ++i) { aA = mfma(fa[i], xa[i], aA); aB = mfma(fb[i], xa[i], aB); }
                PIN2(aA, aB);
            };
            if (kvm) {
                if (kvteam) {
                    u32x4 wk[8];
                    const int a = L_WKV + (kq * 128 + 8 * h + tq) * 64 + tg * 32 + tp * 8;
#pragma unroll
                    for (int i = 0; i < 8; ++i) wk[i] = tr_frag(lds, a + i * 1024, 256);
                    SBAR();
                    float cs = 0.f;
#pragma unroll
                    for (int i = 0; i < 8; ++i) cs = frag_sum(wk[i], cs);
                    cs = half_sum(cs);
                    if (h == 0) cst[kq * 96 + 64 + r] = cs;
                    SBAR();
                    f32x16 ak = {}, av = {};
#pragma unroll
                    for (int i = 0; i < 8; ++i) { ak = mfma(wk[i], xa[i], ak); av = mfma(xa[i], wk[i], av); }
                    store_stats();
                    if (kq == 3 && lane == 0) *(LAS unsigned*)(lds + F_KVCNT) = 0u;
                    const int o = L_KVRED + kq * 4096;
                    lds_stf(lds, o + vl, (f32x4){ak[0], ak[1], ak[2], ak[3]}); lds_stf(lds, o + 1024 + vl, (f32x4){ak[4], ak[5], ak[6], ak[7]});
                    if (r >= 16) {
#pragma unroll
                        for (int g = 0; g < 4; ++g) lds_stf(lds, o + 2048 + g * 512 + (h * 16 + r - 16) * 16, (f32x4){av[4 * g], av[4 * g + 1], av[4 * g + 2], av[4 * g + 3]});
                    }
                } else WAITVM(8);
                LDS_BARRIER();
                if (kvteam && kq < 3) {
                    f32x4 a = {}, e = {}, ta[4], te[4];
                    float t1, t2, mu, rstd;
                    if (kq == 0) {
                        f32x4 k0 = {}, k1 = {}, u0[4], u1[4];
#pragma unroll
                        for (int i = 0; i < 4; ++i) { ta[i] = lds_ldf(lds, L_KVRED + i * 4096 + vl); te[i] = lds_ldf(lds, L_KVRED + i * 4096 + 1024 + vl);
                                                      u0[i] = lds_ldf(lds, L_CS + (i * 96 + 64 + 4 * h) * 4); u1[i] = lds_ldf(lds, L_CS + (i * 96 + 64 + 8 + 4 * h) * 4); }
                        stat_sums<4>(lds, tb * 8, r, t1, t2); ln_from_sums(t1, t2, mu, rstd);
#pragma unroll
                        for (int i = 0; i < 4; ++i) { a += ta[i]; e += te[i]; k0 += u0[i]; k1 += u1[i]; }
#pragma unroll
                        for (int i = 0; i < 4; ++i) { a[i] = (a[i] - mu * k0[i]) * rstd; e[i] = (e[i] - mu * k1[i]) * rstd; }
                        st_sc1((u32x4){pk(a[0], a[1]), pk(a[2], a[3]), pk(e[0], e[1]), pk(e[2], e[3])}, rw, vl, OFF_KF + L * 65536 + tbg * 1024);
                        float n2 = (a[0] * a[0] + a[1] * a[1]) + (a[2] * a[2] + a[3] * a[3]) + (e[0] * e[0] + e[1] * e[1]) + (e[2] * e[2] + e[3] * e[3]);
                        n2 = wave_max(half_sum(n2));
                        if (lane == 0) *(LAS float*)(lds + F_N2) = n2;
                    } else {
                        const int g = kq - 1, lc = (h * 16 + (r & 15)) * 16;
                        float csv = 0.f, uc[4];
#pragma unroll
                        for (int i = 0; i < 4; ++i) { ta[i] = lds_ldf(lds, L_KVRED + i * 4096 + 2048 + (2 * g) * 512 + lc); te[i] = lds_ldf(lds, L_KVRED + i * 4096 + 2048 + (2 * g + 1) * 512 + lc);
                                                      uc[i] = cst[i * 96 + 64 + 16 + (r & 15)]; }
                        stat_sums<4>(lds, tb * 8, r, t1, t2); ln_from_sums(t1, t2, mu, rstd);
#pragma unroll
                        for (int i = 0; i < 4; ++i) { a += ta[i]; e += te[i]; csv += uc[i]; }
#pragma unroll
                        for (int i = 0; i < 8; ++i) {
                            const int tok = 16 * g + (i & 3) + 8 * (i >> 2) + 4 * h;
                            const float m2 = __uint_as_float(__builtin_amdgcn_ds_bpermute(tok * 4, __float_as_uint(mu))), r2 = __uint_as_float(__builtin_amdgcn_ds_bpermute(tok * 4, __float_as_uint(rstd)));
                            if (i < 4) a[i] = (a[i] - m2 * csv) * r2; else e[i - 4] = (e[i - 4] - m2 * csv) * r2;
                        }
                        if (r >= 16) st_sc1((u32x4){pk(a[0], a[1]), pk(a[2], a[3]), pk(e[0], e[1]), pk(e[2], e[3])}, rw, h * 256 + (r - 16) * 16, OFF_VF + L * 65536 + (2 * tbg + g) * 512);
                    }
                }
                if (kvteam && kq < 3) {
                    DRAIN();
                    unsigned old = 0;
                    if (lane == 0) old = __hip_atomic_fetch_add((LAS unsigned*)(lds + F_KVCNT), 1u, __ATOMIC_RELAXED, __HIP_MEMORY_SCOPE_WORKGROUP);
                    if (__builtin_amdgcn_readfirstlane(old) == 2u && lane < 32)
                        __hip_atomic_store((gu64*)(p.ws + OFF_KVFL) + (L * 32 + lane) * 64 + 2 * c + j,
                                           ((unsigned long long)(epoch + 1u + L) << 32) | __float_as_uint(*(volatile LAS float*)(lds + F_N2)), RLX_AGENT);
                }
                __builtin_amdgcn_s_setprio(0);
                STAMP(3 + L * 32);
                do_q();
                if (!kvteam) store_stats();
            } else {
                do_q(); store_stats();
                STAMP(3 + L * 32);
            }
            STAMP(4 + L * 32);
            float q[8];
            reduce4(lds, kvm ? (kvteam ? 0 : 131072) : (TB == 0 ? 32768 : 131072), aA, aB, kq, lane, q);
            STAMP(5 + L * 32);
            float t1, t2;
            f32x4 cs0 = {}, cs1 = {}, u0[4], u1[4];
#pragma unroll
            for (int i = 0; i < 4; ++i) { u0[i] = lds_ldf(lds, L_CS + (i * 96 + 16 * kq + 4 * h) * 4); u1[i] = lds_ldf(lds, L_CS + (i * 96 + 16 * kq + 8 + 4 * h) * 4); }
            stat_sums<4>(lds, tb * 8, r, t1, t2);
#pragma unroll
            for (int i = 0; i < 4; ++i) { cs0 += u0[i]; cs1 += u1[i]; }
            float mu, rstd; ln_from_sums(t1, t2, mu, rstd);
            float n2 = 0.f;
#pragma unroll
            for (int i = 0; i < 4; ++i) { q[i] = (q[i] - mu * cs0[i]) * (rstd * QSCALE); q[4 + i] = (q[4 + i] - mu * cs1[i]) * (rstd * QSCALE); }
#pragma unroll
            for (int i = 0; i < 8; ++i) n2 += q[i] * q[i];
            q2own = half_sum(n2);
            qown = (u32x4){pk(q[0], q[1]), pk(q[2], q[3]), pk(q[4], q[5]), pk(q[6], q[7])};
            lds_st(lds, L_XCHQ + w * 1024 + vl, qown);
            *(LAS float*)(lds + L_STAT + (tb * 8 + 4 + kq) * 256 + lane * 4) = q2own;
            if (L == 0 && !kvm) {
                float cs = 0.f;
#pragma unroll
                for (int i = 0; i < 4; ++i) {
                    const int d = prep_dst(i);
                    if (d < 0) continue;
                    u32x4 fr = tr_frag(lds, L_PREP + (w * 4 + i) * 1024 + (8 * h + tq) * 64 + tg * 32 + tp * 8, 256);
                    if (d >= OFF_W2 && ((d - OFF_W2) >> 10) % KSFP >= KSF) fr = (u32x4){0u, 0u, 0u, 0u};
                    st_sc1(fr, rw, vl, d);
                    cs = frag_sum(fr, cs);
                }
                const int u = c * 6 + (j - 2);
                if (u < 43) {
                    cs = half_sum(cs);
                    LAS float* t = (LAS float*)(lds + L_WQ);
                    if (h == 0) t[w * 32 + r] = cs;
                    LDS_BARRIER();
                    if (tid < 32) { float v = 0.f, pv[8];
#pragma unroll
                        for (int i = 0; i < 8; ++i) pv[i] = t[i * 32 + tid];
                        SBAR();
#pragma unroll
                        for (int i = 0; i < 8; ++i) v += pv[i];
                        __hip_atomic_store((gu32*)(p.ws + OFF_CS1 + (32 * u + tid) * 4), __float_as_uint(v), RLX_AGENT); }
                }
                DRAIN();
            }
            STAMP(6 + L * 32);
        }
        u32x4 woA[8], woB[8];
        u32x4 w1f[32];
        {
            PHASE_IDS();
            const float km2 = wait_flags(tid, lds, (gu64*)(p.ws + OFF_KVFL) + (L * 32 + c) * 64, epoch + 1u + L, tmo);
            if (L == 0 && j >= 2 && tid == 0) __hip_atomic_fetch_add(sync + CW, 1u, RLX_AGENT);
            STAMP(7 + L * 32);
            {
                const unsigned char* src = p.ws + (TB == 0 ? OFF_KF : OFF_VF) + L * 65536 + ((w >> 1) & 1) * 32768 + vl;
                const unsigned dst = lbase + (TB == 0 ? L_K : L_V) + ((w >> 1) & 1) * 32768;
                const int t0 = (w & 1) * 6;
#pragma unroll
                for (int i = 0; i < 6; ++i) glds16_sc1(src + (t0 + i) * 1024, dst + (t0 + i) * 1024);
            }
            { unsigned one2 = 0x3f803f80u; asm volatile("" : "+v"(one2)); if (tid < 272) lds_st(lds, L_ONES + tid * 16, (u32x4){one2, one2, one2, one2}); }
            const u32x4 qpar = lds_ld(lds, L_XCHQ + (w ^ 1) * 1024 + vl);
            const float q2par = *(LAS float*)(lds + L_STAT + (tb * 8 + 4 + (kq ^ 1)) * 256 + lane * 4);
            float ma = __builtin_amdgcn_sqrtf(q2own * km2) * 1.02f, mb = __builtin_amdgcn_sqrtf(q2par * km2) * 1.02f;
            const bool unsafe = __any(!(ma <= SAFE_BOUND) || !(mb <= SAFE_BOUND));
            {
                LAS unsigned* fl = (LAS unsigned*)(lds + F_UNSAFE);
                if (lane == 0) fl[w] = unsafe ? 1u : 0u;
            }
            STAMP(8 + L * 32);
            WAITVM(0);
            LDS_BARRIER();
            {
                LAS unsigned* fl = (LAS unsigned*)(lds + F_UNSAFE);
                const u32x4 f0 = *(LAS u32x4*)fl, f1 = *(LAS u32x4*)(fl + 4);
                if (__builtin_amdgcn_readfirstlane(f0[0] | f0[1] | f0[2] | f0[3] | f1[0] | f1[1] | f1[2] | f1[3])) {
                    {
                        const unsigned char* src = p.ws + (TB == 0 ? OFF_KF : OFF_VF) + L * 65536 + ((w >> 1) & 1) * 32768 + (12 + (w & 1) * 10) * 1024 + vl;
                        const unsigned dst = lbase + (TB == 0 ? L_K : L_V) + ((w >> 1) & 1) * 32768 + (12 + (w & 1) * 10) * 1024;
#pragma unroll 1
                        for (int i = 0; i < 10; ++i) glds16_sc1(src + i * 1024, __builtin_amdgcn_readfirstlane(dst + i * 1024));
                    }
                    DRAIN();
                    LDS_BARRIER();
                    if (unsafe) {
                        float xa = -INFINITY, xb = -INFINITY;
#pragma unroll 1
                        for (int t = 0; t < 64; ++t) {
                            const u32x4 kf = lds_ld(lds, L_K + t * 1024 + vl);
                            const f32x16 sa = mfma(kf, qown, (f32x16){}), sb = mfma(kf, qpar, (f32x16){});
#pragma unroll
                            for (int i = 0; i < 16; ++i) { xa = fmaxf(xa, sa[i]); xb = fmaxf(xb, sb[i]); }
                        }
                        ma = half_max(xa); mb = half_max(xb);
                    }
                }
            }
            STAMP(9 + L * 32);
            const int kh = kq & 1;
            const bool real = r < 16;
            const unsigned kaddr = L_K + kh * 32768 + vl;
            const unsigned vaddr = real ? (unsigned)(L_V + kh * 32768 + h * 256 + r * 16) : (unsigned)(L_ONES + ((r >= 20 && r < 28) ? 64 : 0));
            AttnState st;
            attn_begin(st, lds, qown, qpar, ma, mb, kaddr, vaddr, real ? 4096u : 0u);
            attn_first(st, (unsigned long long)(uintptr_t)(p.ws + (TB == 0 ? OFF_KF : OFF_VF) + L * 65536 + ((w >> 1) & 1) * 32768 + (12 + (w & 1) * 10) * 1024),
                       lbase + (TB == 0 ? L_K : L_V) + ((w >> 1) & 1) * 32768 + (12 + (w & 1) * 10) * 1024, (unsigned)vl);
            STAMP(10 + L * 32);
            if (L == 0) { if (lane == 0) poll_ge(sync + CW, PREP_WGS, tmo); __builtin_amdgcn_fence(__ATOMIC_ACQUIRE, "wavefront"); }
#pragma unroll
            for (int i = 0; i < 8; ++i) { woA[i] = ld_sc1(rw, vw, OFF_WO + (cbA * KSD + kq * 8 + i) * 1024); woB[i] = ld_sc1(rw, vw, OFF_WO + (cbB * KSD + kq * 8 + i) * 1024); }
            if (TB == 1) { W1_IDS();
#pragma unroll
                for (int i = 0; i < 8; ++i) w1f[i] = ld_sc1(rw, vl, OFF_W1 + (cbg1 * KSD + 2 * i + par) * 1024); }
            SBAR();
            STAMP(11 + L * 32);
            attn_second(st);
            STAMP(12 + L * 32);
            const f32x16 oa = st.oa, ob = st.ob;
            const int xo = L_XCHO + w * 2304;
            lds_stf(lds, xo + vl, (f32x4){ob[0], ob[1], ob[2], ob[3]}); lds_stf(lds, xo + 1024 + vl, (f32x4){ob[4], ob[5], ob[6], ob[7]});
            *(LAS float*)(lds + xo + 2048 + lane * 4) = ob[8];
            asm volatile("s_waitcnt lgkmcnt(0)" ::: "memory");
            *(volatile LAS unsigned*)(lds + F_PAIR + w * 4) = L + 1u;
            while (*(volatile LAS unsigned*)(lds + F_PAIR + (w ^ 1) * 4) != L + 1u) { }
            asm volatile("" ::: "memory");
            const int xi = L_XCHO + (w ^ 1) * 2304;
            const f32x4 e0 = lds_ldf(lds, xi + vl), e1 = lds_ldf(lds, xi + 1024 + vl);
            const float lpar = *(LAS float*)(lds + xi + 2048 + lane * 4);
            SBAR();
            const float rl = __builtin_amdgcn_rcpf(oa[8] + lpar);
            const u32x4 of = {pk((oa[0] + e0[0]) * rl, (oa[1] + e0[1]) * rl), pk((oa[2] + e0[2]) * rl, (oa[3] + e0[3]) * rl),
                              pk((oa[4] + e1[0]) * rl, (oa[5] + e1[1]) * rl), pk((oa[6] + e1[2]) * rl, (oa[7] + e1[3]) * rl)};
            st_sc1(swap_order(of), rw, vl, OFF_O + L * ACT_BYTES + (tbg * KSD + sown) * 1024);
            DRAIN();
            unsigned old = 0;
            if (lane == 0) old = __hip_atomic_fetch_add((LAS unsigned*)(lds + F_OCNT), 1u, __ATOMIC_RELAXED, __HIP_MEMORY_SCOPE_WORKGROUP);
            if (__builtin_amdgcn_readfirstlane(old) == 8u * L + 7u && lane == 0) __hip_atomic_fetch_add(cnt + cnt_word(L, CNT_O, c), 1u, RLX_AGENT);
            STAMP(13 + L * 32);
            if (TB == 0) { W1_IDS();
#pragma unroll
                for (int i = 0; i < W1_EARLY; ++i) w1f[i] = ld_sc1(rw, vl, OFF_W1 + (cbg1 * KSD + i) * 1024);
                SBAR(); }
        }
        {
            PHASE_IDS();
            STAMP(14 + L * 32);
            wait_cnt(tid, cnt + cnt_word(L, CNT_O, c), 8u, tmo);
            STAMP(15 + L * 32);
            u32x4 xa[8];
#pragma unroll
            for (int i = 0; i < 8; ++i) xa[i] = ld_sc1(rw, vl, OFF_O + L * ACT_BYTES + (tbg * KSD + kq * 8 + i) * 1024);
            SBAR();
            f32x16 aA = {}, aB = {};
#pragma unroll
            for (int i = 0; i < 8; ++i) { aA = mfma(woA[i], xa[i], aA); aB = mfma(woB[i], xa[i], aB); }
            PIN2(aA, aB);
            SBAR();
            STAMP(16 + L * 32);
            {
                const unsigned char* src = p.ws + OFF_W2 + ((2 * j + (w & 1)) * KSFP + (w >> 1) * 22 + W2_REG) * 1024 + vl;
#pragma unroll
                for (int i = 0; i < 6; ++i) glds16_sc1(src + i * 1024, lbase + L_W2A + (w * W2_A + i) * 1024);
            }
            STAMP(17 + L * 32);
            float o[8];
            reduce4(lds, tb * 24576, aA, aB, kq, lane, o);
#pragma unroll
            for (int i = 0; i < 4; ++i) resid[i] = pk(bflo(resid[i]) + o[2 * i], bfhi(resid[i]) + o[2 * i + 1]);
            st_sc1(swap_order(resid), rw, vl, OFF_X1 + L * ACT_BYTES + (tbg * KSD + sown) * 1024);
            STAMP(18 + L * 32);
            publish(tid, cnt + cnt_word(L, CNT_X1, c));
            STAMP(19 + L * 32);
        }
        u32x4 w2A[W2_REG], w2B[W2_REG];
        {
            PHASE_IDS(); W1_IDS();
            if (w == 1 || w == 2 || w == 3) {
                const int col = min(32 * cb0 + tid - 64, F - 1);
                *(LAS float*)(lds + L_TAB + (tid - 64) * 4) = __uint_as_float(__builtin_amdgcn_raw_buffer_load_b32(rw, col * 4, OFF_CS1, AUX_SC1));
                *(LAS float*)(lds + L_TAB + 768 + (tid - 64) * 4) = bf2f(p.b1[col]);
            }
            STAMP(20 + L * 32);
            wait_cnt(tid, cnt + cnt_word(L, CNT_X1, c), 8u, tmo);
            STAMP(21 + L * 32);
            const unsigned char* xsrc = p.ws + OFF_X1 + L * ACT_BYTES + (2 * c * KSD + 2 * w) * 1024 + vl;
#pragma unroll
            for (int i = 0; i < 4; ++i) glds16_sc1(xsrc + ((i >> 1) * KSD + (i & 1)) * 1024, lbase + L_ACT + ((i >> 1) * KSD + 2 * w + (i & 1)) * 1024);
            WAITVM(0);
            LDS_BARRIER();
#pragma unroll
            for (int i = 0; i < 4; ++i) glds16_sc1(xsrc + ((i >> 1) * KSD + 16 + (i & 1)) * 1024, lbase + L_ACT + ((i >> 1) * KSD + 16 + 2 * w + (i & 1)) * 1024);
            STAMP(22 + L * 32);
            f32x16 a0 = {}, a1 = {};
            float s1[2] = {0.f, 0.f}, s2[2] = {0.f, 0.f};
            const unsigned char* w2a_src = p.ws + OFF_W2 + ((2 * j + (w & 1)) * KSFP + (w >> 1) * 22 + W2_REG) * 1024 + vl;
            auto req = [&](int lo, int n) {
                if (lo == 0) {
                    if (TB == 0) { if (n < 32 - W1_EARLY) w1f[W1_EARLY + n] = ld_sc1(rw, vl, OFF_W1 + (cbg1 * KSD + W1_EARLY + n) * 1024); }
                    else if (n < 8) w1f[16 + n] = ld_sc1(rw, vl, OFF_W1 + (cbg1 * KSD + 16 + 2 * n + par) * 1024);
                } else {
                    if (n < 5) glds16_sc1(w2a_src + (6 + n) * 1024, lbase + L_W2A + (w * W2_A + 6 + n) * 1024);
                    else if (n < 5 + W2_REG) w2A[n - 5] = ld_sc1(rw, vw, OFF_W2 + (cbA * KSFP + kq * 22 + n - 5) * 1024);
                    else if (n < 5 + 2 * W2_REG) w2B[n - 5 - W2_REG] = ld_sc1(rw, vw, OFF_W2 + (cbB * KSFP + kq * 22 + n - 5 - W2_REG) * 1024);
                }
            };
            auto part = [&](int lo) {
                u32x4 b0[3], b1[3];
                if (TB == 0) {
#pragma unroll
                    for (int i = 0; i < 2; ++i) { b0[i] = lds_ld(lds, L_ACT + (lo + i) * 1024 + vl); b1[i] = lds_ld(lds, L_ACT + (KSD + lo + i) * 1024 + vl); }
#pragma unroll
                    for (int s = 0; s < 16; ++s) {
                        if (s + 2 < 16) { b0[(s + 2) % 3] = lds_ld(lds, L_ACT + (lo + s + 2) * 1024 + vl); b1[(s + 2) % 3] = lds_ld(lds, L_ACT + (KSD + lo + s + 2) * 1024 + vl); }
                        SBAR();
                        a0 = mfma(w1f[lo + s], b0[s % 3], a0); a1 = mfma(w1f[lo + s], b1[s % 3], a1);
                        PIN2(a0, a1);
                        req(lo, s);
                        SBAR();
                    }
                } else {
                    const int ab = L_ACT + (lo + par) * 1024 + vl;
#pragma unroll
                    for (int i = 0; i < 2; ++i) { b0[i] = lds_ld(lds, ab + 2 * i * 1024); b1[i] = lds_ld(lds, ab + (KSD + 2 * i) * 1024); }
#pragma unroll
                    for (int s = 0; s < 8; ++s) {
                        if (s + 2 < 8) { b0[(s + 2) % 3] = lds_ld(lds, ab + 2 * (s + 2) * 1024); b1[(s + 2) % 3] = lds_ld(lds, ab + (KSD + 2 * (s + 2)) * 1024); }
                        SBAR();
                        a0 = mfma(w1f[lo + s], b0[s % 3], a0); a1 = mfma(w1f[lo + s], b1[s % 3], a1);
                        PIN2(a0, a1);
                        if (lo == 0) req(lo, s); else { req(lo, 2 * s); req(lo, 2 * s + 1); }
                        SBAR();
                    }
                }
            };
            auto stats = [&](int lo) {
                u32x4 v[4];
#pragma unroll
                for (int i = 0; i < 4; ++i) v[i] = lds_ld(lds, L_ACT + ((i >> 1) * KSD + lo + 2 * w + (i & 1)) * 1024 + vl);
                SBAR();
#pragma unroll
                for (int i = 0; i < 4; ++i) stats_acc(v[i], s1[i >> 1], s2[i >> 1]);
            };
            stats(0);
            SBAR();
            part(0);
            STAMP(23 + L * 32);
            WAITVM(8);
            static_assert(32 - W1_EARLY == 8, "count above");
            LDS_BARRIER();
            stats(16);
            {
                LAS f32x2* st = (LAS f32x2*)(lds + L_STAT);
#pragma unroll
                for (int t = 0; t < 2; ++t) { const float u1 = half_sum(s1[t]), u2 = half_sum(s2[t]); if (h == 0) st[(t * 8 + w) * 32 + r] = (f32x2){u1, u2}; }
            }
            SBAR();
            part(16);
            STAMP(24 + L * 32);
            if (TB == 1) {
                const int xo = L_ACT + kq * 4096 + vl;
#pragma unroll
                for (int g = 0; g < 4; ++g) {
                    const f32x4 v = par ? (f32x4){a0[4 * g], a0[4 * g + 1], a0[4 * g + 2], a0[4 * g + 3]} : (f32x4){a1[4 * g], a1[4 * g + 1], a1[4 * g + 2], a1[4 * g + 3]};
                    lds_stf(lds, xo + g * 1024, v);
                }
            }
            LDS_BARRIER();
            if (TB == 1) {
                const int xi = L_ACT + (kq ^ 1) * 4096 + vl;
                f32x4 v[4];
#pragma unroll
                for (int g = 0; g < 4; ++g) v[g] = lds_ldf(lds, xi + g * 1024);
                SBAR();
#pragma unroll
                for (int g = 0; g < 4; ++g)
#pragma unroll
                    for (int i = 0; i < 4; ++i) a0[4 * g + i] = (par ? a1[4 * g + i] : a0[4 * g + i]) + v[g][i];
                PIN2(a0, a1);
                asm volatile("s_waitcnt lgkmcnt(0)" ::: "memory");
            }
            {
                const int f0 = TB == 0 ? 16 + 8 * kq : 4 * (kq ^ 1), f1 = TB == 0 ? 20 + 8 * kq : 48 + 4 * kq;
#pragma unroll
                for (int i = 0; i < 8; ++i) { const int f = (i < 4 ? f0 : f1 - 4) + i;
                    glds16_sc1(p.ws + OFF_W2 + ((2 * j + ((f >> 3) & 1)) * KSFP + (f >> 4) * 22 + W2_REG + W2_A + (f & 7)) * 1024 + vl, lbase + L_W2B + f * 1024); }
            }
            STAMP(25 + L * 32);
            if (d_active) {
                auto finish = [&](const f32x16& acc, int t) {
                    f32x4 tcs[4], tbb[4];
#pragma unroll
                    for (int g = 0; g < 4; ++g) { tcs[g] = lds_ldf(lds, L_TAB + (32 * cb1 + 8 * g + 4 * h) * 4); tbb[g] = lds_ldf(lds, L_TAB + 768 + (32 * cb1 + 8 * g + 4 * h) * 4); }
                    float t1, t2; stat_sums<8>(lds, t * 8, r, t1, t2);
                    float mu, rstd; ln_from_sums(t1, t2, mu, rstd);
                    const float nmr = -mu * rstd;
                    unsigned hp[8];
#pragma unroll
                    for (int g = 0; g < 4; ++g) {
                        const f32x4 cs = tcs[g], bb = tbb[g];
                        float y[4];
#pragma unroll
                        for (int i = 0; i < 4; ++i) {
                            const float x = fmaf(acc[4 * g + i], rstd, fmaf(cs[i], nmr, bb[i]));
                            const float t = x * fmaf(x * x, -2.f * 0.7978845608028654f * 0.044715f * 1.4426950408889634f, -2.f * 0.7978845608028654f * 1.4426950408889634f);
                            y[i] = x * __builtin_amdgcn_rcpf(1.f + __builtin_amdgcn_exp2f(t));
                        }
                        hp[2 * g] = pk(y[0], y[1]); hp[2 * g + 1] = pk(y[2], y[3]);
                    }
                    const int so = OFF_H + L * H_BYTES + ((2 * c + t) * KSF + 2 * cbg1) * 1024;
                    st_sc1(swap_order((u32x4){hp[0], hp[1], hp[2], hp[3]}), rw, vl, so);
                    st_sc1(swap_order((u32x4){hp[4], hp[5], hp[6], hp[7]}), rw, vl, so + 1024);
                };
                if (TB == 0) { finish(a0, 0); finish(a1, 1); } else finish(a0, par);
            }
            STAMP(26 + L * 32);
            publish(tid, cnt + cnt_word(L, CNT_H, c));
            STAMP(27 + L * 32);
        }
        {
            PHASE_IDS();
            const u32x2 bb0 = *(const GAS u32x2*)(p.b2 + 16 * sown + 4 * h), bb1 = *(const GAS u32x2*)(p.b2 + 16 * sown + 8 + 4 * h);
            wait_cnt(tid, cnt + cnt_word(L, CNT_H, c), 8u, tmo);
            STAMP(28 + L * 32);
            u32x4 ha[22];
            const int hso = OFF_H + L * H_BYTES + tbg * KSF * 1024;
            constexpr int EWIN = 10;
#pragma unroll
            for (int i = 0; i < EWIN; ++i) ha[i] = ld_sc1(rw, vl, hso + min(kq * 22 + i, KSF - 1) * 1024);
            SBAR();
            f32x16 aA = {}, aB = {};
            const int aA0 = L_W2A + ((kq * 2 + (kq >> 1)) * W2_A) * 1024 + vw, aB0 = L_W2A + ((kq * 2 + ((kq >> 1) ^ 1)) * W2_A) * 1024 + vw;
            const int bA0 = L_W2B + ((kq * 2 + (kq >> 1)) * W2_B) * 1024 + vw, bB0 = L_W2B + ((kq * 2 + ((kq >> 1) ^ 1)) * W2_B) * 1024 + vw;
            u32x4 la[3], lb[3];
#pragma unroll
            for (int i = 0; i < 22; ++i) {
                if (i + EWIN < 22) ha[i + EWIN] = ld_sc1(rw, vl, hso + min(kq * 22 + i + EWIN, KSF - 1) * 1024);
                const int n = i + 2;
                if (n >= W2_REG && n < W2_REG + W2_A) { la[n % 3] = lds_ld(lds, aA0 + (n - W2_REG) * 1024); lb[n % 3] = lds_ld(lds, aB0 + (n - W2_REG) * 1024); }
                else if (n >= W2_REG + W2_A && n < 22) { la[n % 3] = lds_ld(lds, bA0 + (n - W2_REG - W2_A) * 1024); lb[n % 3] = lds_ld(lds, bB0 + (n - W2_REG - W2_A) * 1024); }
                SBAR();
                if (i < W2_REG) { aA = mfma(w2A[i], ha[i], aA); aB = mfma(w2B[i], ha[i], aB); }
                else { aA = mfma(la[i % 3], ha[i], aA); aB = mfma(lb[i % 3], ha[i], aB); }
                PIN2(aA, aB);
                SBAR();
            }
            STAMP(29 + L * 32);
            LDS_BARRIER();
            if (L == 0) {
                const int row = lane >> 3, pc = lane & 7;
                const uint16_t* src = p.wq + (size_t)(64 * w + row) * D + 64 * j + (((pc >> 2) ^ ((row >> 1) & 1)) * 32 + (pc & 3) * 8);
                const bool mine = !(j == tb);
                const uint16_t* src2 = p.wq + (size_t)(64 * (w ^ 4) + row) * D + 64 * j + (((pc >> 2) ^ ((row >> 1) & 1)) * 32 + (pc & 3) * 8);
                if (mine) {
#pragma unroll
                    for (int i = 0; i < 4; ++i) glds16(src + i * 8 * D, lbase + L_WQ + (w * 8 + i) * 1024);
                    if (j < 2) {
#pragma unroll
                        for (int i = 0; i < 4; ++i) glds16(src2 + i * 8 * D, lbase + L_WQ + ((w ^ 4) * 8 + i) * 1024);
                    }
                }
            }
            float o[8];
            reduce4(lds, tb * 24576, aA, aB, kq, lane, o);
#pragma unroll
            for (int i = 0; i < 2; ++i) { o[2 * i] += bflo(bb0[i]); o[2 * i + 1] += bfhi(bb0[i]); o[4 + 2 * i] += bflo(bb1[i]); o[4 + 2 * i + 1] += bfhi(bb1[i]); }
#pragma unroll
            for (int i = 0; i < 4; ++i) resid[i] = pk(bflo(resid[i]) + o[2 * i], bfhi(resid[i]) + o[2 * i + 1]);
            STAMP(30 + L * 32);
            const u32x4 xo = swap_order(resid);
            if (L == 0) {
                st_sc1(xo, rw, vl, OFF_X2 + (tbg * KSD + sown) * 1024);
                publish(tid, cnt + cnt_word(0, CNT_X2, c));
            } else {
                *(GAS u32x4*)(p.out + (size_t)(64 * c + 32 * tb + r) * D + 16 * sown + 8 * h) = xo;
            }
            STAMP(31 + L * 32);
        }
    }
    { unsigned e = epoch0; asm volatile("" : "+s"(e)); if (blockIdx.x == 0 && threadIdx.x == 0) __hip_atomic_store(sync, e + 2u, RLX_AGENT); }
}

__global__ void __launch_bounds__(512, 2) main_kernel(Params p) {
    extern __shared__ __attribute__((aligned(16))) char lds_dyn[];
    LAS char* lds = (LAS char*)lds_dyn;
    if (__builtin_amdgcn_readfirstlane(threadIdx.x >> 8) == 0) run_layers<0>(p, lds); else run_layers<1>(p, lds);
}

}

extern "C" void kernel_launch(void* const* d_in, const int* in_sizes, int n_in, void* d_out, int out_size, void* d_ws, size_t ws_size, hipStream_t stream) {
    Params p;
    p.x = (const uint16_t*)d_in[0]; p.wq = (const uint16_t*)d_in[1]; p.wk = (const uint16_t*)d_in[2]; p.wv = (const uint16_t*)d_in[3]; p.wo = (const uint16_t*)d_in[4];
    p.w1 = (const uint16_t*)d_in[6]; p.b1 = (const uint16_t*)d_in[7]; p.w2 = (const uint16_t*)d_in[8]; p.b2 = (const uint16_t*)d_in[9];
    p.out = (uint16_t*)d_out; p.ws = (unsigned char*)d_ws;
    (void)hipFuncSetAttribute((const void*)main_kernel, hipFuncAttributeMaxDynamicSharedMemorySize, LDS_BYTES);
    hipLaunchKernelGGL(main_kernel, dim3(256), dim3(512), LDS_BYTES, stream, p);
}
```

```cpp
#include <hip/hip_runtime.h>
#include <hip/hip_bf16.h>
#include <stdint.h>

#define GAS __attribute__((address_space(1)))
#define LAS __attribute__((address_space(3)))
typedef __bf16 bf16x8 __attribute__((ext_vector_type(8)));
typedef __bf16 bf16x2 __attribute__((ext_vector_type(2)));
typedef float f32x16 __attribute__((ext_vector_type(16)));
typedef float f32x4 __attribute__((ext_vector_type(4)));
typedef float f32x2 __attribute__((ext_vector_type(2)));
typedef unsigned u32x4 __attribute__((ext_vector_type(4)));
typedef GAS unsigned gu32;
#define RLX_AGENT __ATOMIC_RELAXED, __HIP_MEMORY_SCOPE_AGENT

namespace {

constexpr int S = 2048, D = 512, F = 1376;
constexpr int KSD = 32;
constexpr int KSF = 86;
constexpr int KSFP = 88;
constexpr float LN_EPS = 1e-5f;
constexpr float QSCALE = 0.25f * 1.4426950408889634f;
constexpr float SAFE_BOUND = 50.f;

constexpr int CW = 64;
constexpr int OFF_CNT = 0;
constexpr int CNT_O = 0, CNT_X1 = 1, CNT_H = 2, CNT_X2 = 3;
__device__ __forceinline__ int cnt_word(int layer, int kind, int c) { return ((layer * 4 + kind) * 32 + c) * CW; }
constexpr int OFF_SYNC = 65536;
constexpr int OFF_KVFL = OFF_SYNC + 1024;
constexpr int OFF_WO = OFF_KVFL + 2 * 32 * 512;
constexpr int OFF_W1 = OFF_WO + 16 * KSD * 1024;
constexpr int OFF_W2 = OFF_W1 + 43 * KSD * 1024;
constexpr int OFF_CS1 = OFF_W2 + 16 * KSFP * 1024;
constexpr int OFF_KF = (OFF_CS1 + 1408 * 4 + 1023) / 1024 * 1024;
constexpr int OFF_VF = OFF_KF + 2 * 65536;
constexpr int OFF_O = OFF_VF + 2 * 65536;
constexpr int ACT_BYTES = 64 * KSD * 1024;
constexpr int OFF_X1 = OFF_O + 2 * ACT_BYTES;
constexpr int OFF_X2 = OFF_X1 + 2 * ACT_BYTES;
constexpr int H_BYTES = 64 * KSF * 1024;
constexpr int OFF_H = OFF_X2 + ACT_BYTES;
constexpr int WS_TOTAL = OFF_H + 2 * H_BYTES;
constexpr unsigned SPIN_CAP = 1u << 15;
constexpr unsigned PREP_WGS = 192;

constexpr int L_K = 0, L_V = 65536, L_ONES = 131072, L_XCHO = 135424;
constexpr int L_WQ = 0, L_WKV = 65536, L_PREP = 65536, L_KVRED = 98304, L_XCHQ = 147456;
constexpr int L_ACT = 0, L_W2 = 65536;
constexpr int L_STAT = 155648, L_TAB = 159744, L_CS = L_TAB, L_FLAG = 163584;
constexpr int LDS_BYTES = 163840;
constexpr int W2_REG = 11;

#define PROF 0
#define PROF_TID 0
constexpr int L_PROF = 161280;
constexpr int OFF_PROF = 64 << 20;
#define STAMP(i) do { } while (0)

struct Params {
    const uint16_t *x, *wq, *wk, *wv, *wo, *w1, *b1, *w2, *b2;
    uint16_t* out;
    unsigned char* ws;
};

__device__ __forceinline__ f32x16 mfma(u32x4 a, u32x4 b, f32x16 c) {
    return __builtin_amdgcn_mfma_f32_32x32x16_bf16(__builtin_bit_cast(bf16x8, a), __builtin_bit_cast(bf16x8, b), c, 0, 0, 0);
}
__device__ __forceinline__ unsigned pk(float a, float b) { f32x2 v = {a, b}; return __builtin_bit_cast(unsigned, __builtin_convertvector(v, bf16x2)); }
__device__ __forceinline__ float bflo(unsigned u) { return __uint_as_float(u << 16); }
__device__ __forceinline__ float bfhi(unsigned u) { return __uint_as_float(u & 0xffff0000u); }
__device__ __forceinline__ float bf2f(uint16_t v) { return __uint_as_float((unsigned)v << 16); }
__device__ __forceinline__ float dot2(unsigned a, unsigned b, float c) {
    return __builtin_amdgcn_fdot2_f32_bf16(__builtin_bit_cast(bf16x2, a), __builtin_bit_cast(bf16x2, b), c, false);
}
__device__ __forceinline__ float half_sum(float v) {
    auto r = __builtin_amdgcn_permlane32_swap(__float_as_uint(v), __float_as_uint(v), false, false);
    return __uint_as_float(r[0]) + __uint_as_float(r[1]);
}
__device__ __forceinline__ float half_max(float v) {
    auto r = __builtin_amdgcn_permlane32_swap(__float_as_uint(v), __float_as_uint(v), false, false);
    return fmaxf(__uint_as_float(r[0]), __uint_as_float(r[1]));
}
__device__ __forceinline__ u32x4 swap_order(u32x4 a) {
    auto r0 = __builtin_amdgcn_permlane32_swap(a[0], a[2], false, false);
    auto r1 = __builtin_amdgcn_permlane32_swap(a[1], a[3], false, false);
    return (u32x4){r0[0], r1[0], r0[1], r1[1]};
}
template <int CTRL> __device__ __forceinline__ float dpp(float v) {
    return __uint_as_float(__builtin_amdgcn_update_dpp(0u, __float_as_uint(v), CTRL, 0xf, 0xf, true));
}
__device__ __forceinline__ float wave_max(float v) {
    v = fmaxf(v, dpp<0xB1>(v));
    v = fmaxf(v, dpp<0x4E>(v));
    v = fmaxf(v, dpp<0x141>(v));
    v = fmaxf(v, dpp<0x140>(v));
    auto r = __builtin_amdgcn_permlane16_swap(__float_as_uint(v), __float_as_uint(v), false, false);
    return half_max(fmaxf(__uint_as_float(r[0]), __uint_as_float(r[1])));
}
#define SBAR() __builtin_amdgcn_sched_barrier(0)
#define PIN2(a, b) asm volatile("" : "+v"(a), "+v"(b))
#define LDS_BARRIER() asm volatile("s_waitcnt lgkmcnt(0)\n\ts_barrier" ::: "memory")
#define DRAIN() asm volatile("s_waitcnt vmcnt(0)" ::: "memory")
constexpr int AUX_SC1 = 16;
typedef __amdgpu_buffer_rsrc_t rsrc_t;
__device__ __forceinline__ u32x4 ld_sc1(rsrc_t r, int voff, int soff) { return __builtin_amdgcn_raw_buffer_load_b128(r, voff, soff, AUX_SC1); }
__device__ __forceinline__ u32x4 ld_plain(rsrc_t r, int voff, int soff) { return __builtin_amdgcn_raw_buffer_load_b128(r, voff, soff, 0); }
__device__ __forceinline__ void st_sc1(u32x4 v, rsrc_t r, int voff, int soff) {
    __builtin_amdgcn_raw_buffer_store_b128(v, r, voff, soff, AUX_SC1);
    asm volatile("s_nop 1" :: "v"(v) : "memory");
}
__device__ __forceinline__ u32x4 lds_ld(LAS char* l, int off) { return *(LAS u32x4*)(l + off); }
__device__ __forceinline__ void lds_st(LAS char* l, int off, u32x4 v) { *(LAS u32x4*)(l + off) = v; }
__device__ __forceinline__ f32x4 lds_ldf(LAS char* l, int off) { return *(LAS f32x4*)(l + off); }
__device__ __forceinline__ void lds_stf(LAS char* l, int off, f32x4 v) { *(LAS f32x4*)(l + off) = v; }

__device__ __forceinline__ void publish(int tid, gu32* cnt) {
    DRAIN();
    LDS_BARRIER();
    if (tid == 0) __hip_atomic_fetch_add(cnt, 1u, RLX_AGENT);
}
__device__ __forceinline__ void poll_ge(gu32* cnt, unsigned want, gu32* tmo) {
    unsigned spins = 0;
    while (__hip_atomic_load(cnt, RLX_AGENT) < want) {
        if (++spins > SPIN_CAP) { __hip_atomic_store(tmo, 1u, RLX_AGENT); break; }
    }
}
__device__ __forceinline__ void wait_cnt(int tid, gu32* cnt, unsigned want, gu32* tmo) {
    if (tid == 0) poll_ge(cnt, want, tmo);
    __builtin_amdgcn_fence(__ATOMIC_ACQUIRE, "wavefront");
    LDS_BARRIER();
}
typedef GAS unsigned long long gu64;
__device__ __forceinline__ float wait_flags(int tid, LAS char* lds, gu64* fl, unsigned tag, gu32* tmo) {
    if (tid < 64) {
        unsigned spins = 0; unsigned long long v;
        while (v = __hip_atomic_load(fl + tid, RLX_AGENT), !__all((unsigned)(v >> 32) == tag)) {
            if (++spins > SPIN_CAP) { if (tid == 0) __hip_atomic_store(tmo, 1u, RLX_AGENT); break; }
        }
        const float m = wave_max(__uint_as_float((unsigned)v));
        if (tid == 0) *(LAS float*)(lds + L_FLAG + 32) = m;
    }
    __builtin_amdgcn_fence(__ATOMIC_ACQUIRE, "wavefront");
    LDS_BARRIER();
    return *(LAS float*)(lds + L_FLAG + 32);
}
__device__ __forceinline__ void glds16(const void* g, unsigned lds_dst) {
    unsigned keep;
    asm volatile("s_mov_b32 %0, m0\n\ts_mov_b32 m0, %2\n\ts_nop 0\n\tglobal_load_lds_dwordx4 %1, off\n\ts_mov_b32 m0, %0" : "=&s"(keep) : "v"(g), "s"(lds_dst) : "memory");
}
__device__ __forceinline__ void glds16_sc1(const void* g, unsigned lds_dst) {
    unsigned keep;
    asm volatile("s_mov_b32 %0, m0\n\ts_mov_b32 m0, %2\n\ts_nop 0\n\tglobal_load_lds_dwordx4 %1, off sc1\n\ts_mov_b32 m0, %0" : "=&s"(keep) : "v"(g), "s"(lds_dst) : "memory");
}
#define WAITVM(n) asm volatile("s_waitcnt vmcnt(" #n ")" ::: "memory")

__device__ __forceinline__ void reduce4(LAS char* lds, int region, const f32x16& aA, const f32x16& aB, int kq, int lane, float (&o)[8]) {
    const int base = region + lane * 16;
    { const int a = base + ((kq ^ 1) * 3 + 0) * 2048;
      lds_stf(lds, a, (f32x4){aA[8], aA[9], aA[10], aA[11]}); lds_stf(lds, a + 1024, (f32x4){aA[12], aA[13], aA[14], aA[15]}); }
    { const int a = base + ((kq ^ 2) * 3 + 1) * 2048;
      lds_stf(lds, a, (f32x4){aB[0], aB[1], aB[2], aB[3]}); lds_stf(lds, a + 1024, (f32x4){aB[4], aB[5], aB[6], aB[7]}); }
    { const int a = base + ((kq ^ 3) * 3 + 2) * 2048;
      lds_stf(lds, a, (f32x4){aB[8], aB[9], aB[10], aB[11]}); lds_stf(lds, a + 1024, (f32x4){aB[12], aB[13], aB[14], aB[15]}); }
    LDS_BARRIER();
    f32x4 lo = {aA[0], aA[1], aA[2], aA[3]}, hi = {aA[4], aA[5], aA[6], aA[7]}, t[6];
#pragma unroll
    for (int x = 0; x < 6; ++x) t[x] = lds_ldf(lds, base + kq * 3 * 2048 + x * 1024);
    SBAR();
#pragma unroll
    for (int x = 0; x < 3; ++x) { lo += t[2 * x]; hi += t[2 * x + 1]; }
#pragma unroll
    for (int i = 0; i < 4; ++i) { o[i] = lo[i]; o[4 + i] = hi[i]; }
}
typedef short v4i16 __attribute__((ext_vector_type(4)));
typedef unsigned u32x2 __attribute__((ext_vector_type(2)));
__device__ __forceinline__ u32x4 tr_frag(LAS char* lds, int a, int step) {
    const u32x2 lo = __builtin_bit_cast(u32x2, __builtin_amdgcn_ds_read_tr16_b64_v4i16((LAS v4i16*)(lds + a)));
    const u32x2 hi = __builtin_bit_cast(u32x2, __builtin_amdgcn_ds_read_tr16_b64_v4i16((LAS v4i16*)(lds + a + step)));
    return (u32x4){lo[0], lo[1], hi[0], hi[1]};
}
__device__ __forceinline__ float frag_sum(u32x4 f, float s) {
#pragma unroll
    for (int d = 0; d < 4; ++d) s = dot2(f[d], 0x3f803f80u, s);
    return s;
}

#define A_E1(a) "v_exp_f32 v" #a ", v" #a "\n\t"
#define A_E4(a, b, c, d) A_E1(a) A_E1(b) A_E1(c) A_E1(d)
#define A_CVT(d, a, b) "v_cvt_pk_bf16_f32 v" #d ", v" #a ", v" #b "\n\t"
#define A_DSK(off) "ds_read_b128 v[100:103], v120 offset:" #off "\n\t"
#define A_DSV0(off) "ds_read_b128 v[104:107], v121 offset:" #off "\n\t"
#define A_DSV1(off) "ds_read_b128 v[108:111], v121 offset:" #off "\n\t"
#define A_WAIT(n) "s_waitcnt lgkmcnt(" #n ")\n\t"
#define A_MSB "v_mfma_f32_32x32x16_bf16 v[48:63], v[100:103], v[156:159], v[124:139]\n\t"
#define A_MSA "v_mfma_f32_32x32x16_bf16 v[32:47], v[100:103], v[96:99], v[64:79]\n\t"
#define A_MPV(O, V, P) "v_mfma_f32_32x32x16_bf16 " O ", " V ", " P ", " O "\n\t"
#define A_OA "v[80:95]"
#define A_OB "v[140:155]"
#define A_V0 "v[104:107]"
#define A_V1 "v[108:111]"
#define A_P0 "v[112:115]"
#define A_P1 "v[116:119]"
#define A_STEP(koff, v1off, v0off) \
    A_MSB A_DSK(koff) A_E4(32, 33, 34, 35) \
    A_MPV(A_OB, A_V1, A_P1) A_DSV1(v1off) A_E4(36, 37, 38, 39) \
    A_CVT(112, 32, 33) A_CVT(113, 34, 35) A_CVT(114, 36, 37) A_CVT(115, 38, 39) \
    A_E4(40, 41, 42, 43) \
    A_WAIT(2) A_MPV(A_OA, A_V0, A_P0) A_E4(44, 45, 46, 47) \
    A_CVT(116, 40, 41) A_CVT(117, 42, 43) A_CVT(118, 44, 45) A_CVT(119, 46, 47) \
    A_WAIT(1) A_MSA A_E4(48, 49, 50, 51) \
    A_WAIT(0) A_MPV(A_OA, A_V1, A_P1) A_E4(52, 53, 54, 55) \
    A_CVT(112, 48, 49) A_CVT(113, 50, 51) A_CVT(114, 52, 53) A_CVT(115, 54, 55) \
    A_E4(56, 57, 58, 59) \
    A_MPV(A_OB, A_V0, A_P0) A_DSV0(v0off) A_E4(60, 61, 62, 63) \
    A_CVT(116, 56, 57) A_CVT(117, 58, 59) A_CVT(118, 60, 61) A_CVT(119, 62, 63)

struct AttnState { f32x16 sa, sb, oa, ob, negma, negmb; u32x4 qa, qb, kf, v0, v1, p1; unsigned kaddr, vaddr, vstride; };
#define A_OPERANDS(st, cnt) \
        : "+{v[32:47]}"(st.sa), "+{v[48:63]}"(st.sb), "+{v[80:95]}"(st.oa), "+{v[140:155]}"(st.ob), "+{v[100:103]}"(st.kf), "+{v[104:107]}"(st.v0), "+{v[108:111]}"(st.v1), \
          "={v[112:115]}"(p0), "+{v[116:119]}"(st.p1), "+{v120}"(st.kaddr), "+{v121}"(st.vaddr), [n] "+s"(cnt) \
        : "{v[64:79]}"(st.negma), "{v[124:139]}"(st.negmb), "{v[96:99]}"(st.qa), "{v[156:159]}"(st.qb), "{v122}"(st.vstride) \
        : "memory", "scc"
#define A_ITER \
        A_STEP(1024, 512, 1024) \
        A_STEP(2048, 1536, 2048) \
        A_STEP(3072, 2560, 3072) \
        A_STEP(4096, 3584, 4096) \
        "v_add_u32 v120, 4096, v120\n\t" \
        "v_add_u32 v121, v122, v121\n\t"
__device__ __forceinline__ void attn_begin(AttnState& st, LAS char* lds, u32x4 qa, u32x4 qb, float ma, float mb, unsigned kaddr, unsigned vaddr, unsigned vstride) {
#pragma unroll
    for (int i = 0; i < 16; ++i) { st.negma[i] = -ma; st.negmb[i] = -mb; }
    st.qa = qa; st.qb = qb;
    st.kf = lds_ld(lds, kaddr); st.v0 = lds_ld(lds, vaddr); st.v1 = st.v0; st.p1 = (u32x4){0u, 0u, 0u, 0u};
    st.sa = mfma(st.kf, qa, st.negma); st.sb = st.sa;
    st.oa = (f32x16){}; st.ob = (f32x16){};
    const unsigned lbase = (unsigned)(uintptr_t)lds;
    st.kaddr = kaddr + lbase; st.vaddr = vaddr + lbase; st.vstride = vstride;
}
#define A_DMA \
    "s_mov_b32 m0, s92\n\t" \
    "s_add_u32 s92, s92, 1024\n\t" \
    "global_load_lds_dwordx4 v123, s[90:91] sc1\n\t" \
    "s_add_u32 s90, s90, 1024\n\t" \
    "s_addc_u32 s91, s91, 0\n\t"
#define A_ITER_DMA \
        A_DMA A_DMA A_STEP(1024, 512, 1024) \
        A_DMA A_STEP(2048, 1536, 2048) \
        A_DMA A_STEP(3072, 2560, 3072) \
        A_DMA A_STEP(4096, 3584, 4096) \
        "v_add_u32 v120, 4096, v120\n\t" \
        "v_add_u32 v121, v122, v121\n\t"
__device__ __forceinline__ void attn_first(AttnState& st, unsigned long long src, unsigned dst, unsigned voff) {
    unsigned cnt = 2, keep; u32x4 p0;
    asm volatile(
        "s_mov_b32 %[keep], m0\n\t"
        "s_nop 15\n\t"
        A_ITER_DMA
        A_ITER_DMA
        "s_mov_b32 m0, %[keep]\n\t"
        "s_waitcnt vmcnt(0)\n\t"
        "s_barrier\n\t"
        "ATT1_LOOP_%=:\n\t"
        A_ITER
        "s_sub_u32 %[n], %[n], 1\n\t"
        "s_cmp_lg_u32 %[n], 0\n\t"
        "s_cbranch_scc1 ATT1_LOOP_%=\n\t"
        : "+{v[32:47]}"(st.sa), "+{v[48:63]}"(st.sb), "+{v[80:95]}"(st.oa), "+{v[140:155]}"(st.ob), "+{v[100:103]}"(st.kf), "+{v[104:107]}"(st.v0), "+{v[108:111]}"(st.v1),
          "={v[112:115]}"(p0), "+{v[116:119]}"(st.p1), "+{v120}"(st.kaddr), "+{v121}"(st.vaddr), [n] "+s"(cnt), [keep] "=&s"(keep), "+{s[90:91]}"(src), "+{s92}"(dst)
        : "{v[64:79]}"(st.negma), "{v[124:139]}"(st.negmb), "{v[96:99]}"(st.qa), "{v[156:159]}"(st.qb), "{v122}"(st.vstride), "{v123}"(voff)
        : "memory", "scc");
}
__device__ __forceinline__ void attn_second(AttnState& st) {
    unsigned cnt = 4; u32x4 p0;
    asm volatile(
        "ATT2_LOOP_%=:\n\t"
        A_ITER
        "s_sub_u32 %[n], %[n], 1\n\t"
        "s_cmp_lg_u32 %[n], 0\n\t"
        "s_cbranch_scc1 ATT2_LOOP_%=\n\t"
        "s_waitcnt lgkmcnt(0)\n\t"
        A_MPV(A_OB, A_V1, A_P1)
        "s_nop 15\n\t"
        A_OPERANDS(st, cnt));
}

__device__ __forceinline__ void stats_acc(u32x4 f, float& s1, float& s2) {
#pragma unroll
    for (int d = 0; d < 4; ++d) { s1 = dot2(f[d], 0x3f803f80u, s1); s2 = dot2(f[d], f[d], s2); }
}
template <int N>
__device__ __forceinline__ void stat_sums(LAS char* lds, int slot0, int tok, float& t1, float& t2) {
    f32x2 v[N];
#pragma unroll
    for (int i = 0; i < N; ++i) v[i] = *(LAS f32x2*)(lds + L_STAT + ((slot0 + i) * 32 + tok) * 8);
    SBAR();
    t1 = 0.f; t2 = 0.f;
#pragma unroll
    for (int i = 0; i < N; ++i) { t1 += v[i][0]; t2 += v[i][1]; }
}
__device__ __forceinline__ void ln_from_sums(float t1, float t2, float& mu, float& rstd) {
    mu = t1 * (1.f / D);
    rstd = __builtin_amdgcn_rsqf(fmaxf(t2 * (1.f / D) - mu * mu, 0.f) + LN_EPS);
}

template <int TB>
__device__ __forceinline__ void run_layers(const Params& p, LAS char* lds) {
    const unsigned lbase = (unsigned)(uintptr_t)lds;
    const int tid0 = threadIdx.x, w0 = __builtin_amdgcn_readfirstlane(tid0 >> 6);
    const int kk0 = blockIdx.x >> 3, c0 = (blockIdx.x & 7) * 4 + (kk0 >> 3), j0 = kk0 & 7;
    const rsrc_t rw = __builtin_amdgcn_make_buffer_rsrc(p.ws, 0, WS_TOTAL, 0x00020000);
    const rsrc_t rx = __builtin_amdgcn_make_buffer_rsrc((void*)p.x, 0, S * D * 2, 0x00020000);
    gu32* cnt = (gu32*)(p.ws + OFF_CNT);
    gu32* sync = (gu32*)(p.ws + OFF_SYNC);
    gu32* tmo = sync + 2 * CW;
#define PHASE_IDS() \
    int tid = tid0, w = w0, c = c0, j = j0; unsigned epoch = epoch0; \
    asm volatile("" : "+v"(tid)); asm volatile("" : "+s"(w), "+s"(c), "+s"(j), "+s"(epoch)); \
    constexpr int tb = TB; const int lane = tid & 63, r = lane & 31, h = lane >> 5, kq = w & 3, tbg = 2 * c + tb; \
    const int vl = lane * 16;                                     \
    const int vw = (lane ^ ((kq & 1) << 4)) * 16;                 \
    const int cbA = 2 * j + (kq >> 1), cbB = 2 * j + ((kq >> 1) ^ 1); \
    const int sown = 4 * j + kq;                                  \
    (void)epoch; (void)r; (void)h; (void)tbg; (void)vl; (void)vw; (void)cbA; (void)cbB; (void)sown; (void)tb;
#define W1_IDS() \
    const int ncb = j < 3 ? 6 : 5, cb0 = j < 3 ? 6 * j : 18 + 5 * (j - 3);     \
    const int cb1 = TB == 0 ? kq : 4 + (kq >> 1);     \
    const bool d_active = cb1 < ncb; \
    const int cbg1 = cb0 + (d_active ? cb1 : 0); (void)cbg1; \
    const int par = TB == 0 ? 0 : (kq & 1); (void)par;
#define LOAD_W1(lo) do { \
        _Pragma("unroll") for (int i = 0; i < 8; ++i) w1f[(lo) + i] = ld_sc1(rw, vl, OFF_W1 + (cbg1 * KSD + (lo) + (TB == 0 ? i : 2 * i + par)) * 1024); \
        if (TB == 0) { _Pragma("unroll") for (int i = 8; i < 16; ++i) w1f[(lo) + i] = ld_sc1(rw, vl, OFF_W1 + (cbg1 * KSD + (lo) + i) * 1024); } \
        SBAR(); } while (0)

    STAMP(0);
    unsigned epoch0;
    { gu32* e = sync; asm volatile("" : "+s"(e)); epoch0 = __builtin_amdgcn_readfirstlane(__hip_atomic_load(e, RLX_AGENT)); }
    asm volatile("" : "+s"(epoch0));
    u32x4 resid;
    {
        PHASE_IDS();
        resid = swap_order(ld_plain(rx, r * 1024 + h * 16, (64 * c + 32 * tb) * 1024 + sown * 32));
        if (j == tb && kq == 0 && lane < 4) __hip_atomic_store(cnt + cnt_word(j, lane, c), 0u, RLX_AGENT);
        if (c == 0 && j == 0 && tid >= 4 && tid < 6) __hip_atomic_store(sync + (tid - 3) * CW, 0u, RLX_AGENT);
    }

#pragma unroll 1
    for (int L = 0; L < 2; ++L) {
        u32x4 qown; float q2own;
        {
            PHASE_IDS();
            const bool kvm = j < 2;
            const bool kvteam = j == tb;
            if (kvm && kvteam) {
                __builtin_amdgcn_s_setprio(2);
                const uint16_t* src = ((lane & 2) ? p.wv : p.wk) + (128 * kq + (lane >> 2)) * 16 + (lane & 1) * 8;
#pragma unroll
                for (int i = 0; i < 8; ++i) glds16(src + i * 256, lbase + L_WKV + (kq * 8 + i) * 1024);
            } else {
                const int row = lane >> 3, pc = lane & 7;
                const int first = kvm ? 128 * kq : 64 * w;
                const uint16_t* src = p.wq + (size_t)(first + row) * D + 64 * j + (((pc >> 2) ^ ((row >> 1) & 1)) * 32 + (pc & 3) * 8);
#pragma unroll
                for (int i = 0; i < 8; ++i) glds16(src + i * 8 * D, lbase + L_WQ + (first / 8 + i) * 1024);
                if (kvm) {
#pragma unroll
                    for (int i = 8; i < 16; ++i) glds16(src + i * 8 * D, lbase + L_WQ + (first / 8 + i) * 1024);
                }
            }
            auto prep_dst = [&](int i) -> int {
                const int u = c * 6 + (j - 2);
                if (u < 43) return OFF_W1 + (u * KSD + 4 * w + i) * 1024;
                const int f = ((u - 43) * 8 + w) + i * 1192;
                if (i >= 2 || f >= 1920) return -1;
                return f < 512 ? OFF_WO + f * 1024 : OFF_W2 + (f - 512) * 1024;
            };
            if (L == 0 && !kvm) {
#pragma unroll
                for (int i = 0; i < 4; ++i) {
                    const int d = prep_dst(i);
                    if (d < 0) continue;
                    const uint16_t* src; int ld, cb, s;
                    if (d >= OFF_W2) { const int g = (d - OFF_W2) >> 10; src = p.w2; ld = D; cb = g / KSFP; s = min(g % KSFP, KSF - 1); }
                    else if (d >= OFF_W1) { const int g = (d - OFF_W1) >> 10; src = p.w1; ld = F; cb = g >> 5; s = g & 31; }
                    else { const int g = (d - OFF_WO) >> 10; src = p.wo; ld = D; cb = g >> 5; s = g & 31; }
                    glds16(src + (size_t)(16 * s + (lane >> 2)) * ld + 32 * cb + (lane & 3) * 8, lbase + L_PREP + (w * 4 + i) * 1024);
                }
            }
            u32x4 xa[8];
            if (L == 0) {
#pragma unroll
                for (int i = 0; i < 8; ++i) xa[i] = ld_plain(rx, r * 1024 + h * 16, (64 * c + 32 * tb) * 1024 + (kq * 8 + i) * 32);
                SBAR();
                if (!kvm || kvteam) WAITVM(8);
            } else {
                if (tid == (kvm ? 256 * j : 0)) poll_ge(cnt + cnt_word(0, CNT_X2, c), 8u, tmo);
                __builtin_amdgcn_fence(__ATOMIC_ACQUIRE, "wavefront");
                if (!kvm || kvteam) WAITVM(0);
            }
            LDS_BARRIER();
            STAMP(1 + L * 32);
            if (L == 1) {
#pragma unroll
                for (int i = 0; i < 8; ++i) xa[i] = ld_sc1(rw, vl, OFF_X2 + (tbg * KSD + kq * 8 + i) * 1024);
                SBAR();
            }
            LAS f32x2* st = (LAS f32x2*)(lds + L_STAT);
            LAS float* cst = (LAS float*)(lds + L_CS);
            const int tq = (lane & 15) >> 2, tp = lane & 3, tg = (lane >> 4) & 1;
            f32x16 aA = {}, aB = {};
            auto store_stats = [&]() {
                float s1 = 0.f, s2 = 0.f;
#pragma unroll
                for (int i = 0; i < 8; ++i) stats_acc(xa[i], s1, s2);
                s1 = half_sum(s1); s2 = half_sum(s2);
                if (h == 0) st[(tb * 8 + kq) * 32 + r] = (f32x2){s1, s2};
            };
            auto do_q = [&]() {
                u32x4 fa[8], fb[8];
                float csA = 0.f, csB = 0.f;
                const int sw = (tq >> 1) & 1, col = (tg ^ (kq & 1)) * 32 + tp * 8;
                const int a0 = L_WQ + (kq * 128 + 8 * h + tq) * 128 + col;
                const int aa = a0 + (((kq >> 1) ^ sw) * 64), ab = a0 + (((kq >> 1) ^ 1 ^ sw) * 64);
#pragma unroll
                for (int i = 0; i < 8; ++i) { fa[i] = tr_frag(lds, aa + i * 2048, 512); fb[i] = tr_frag(lds, ab + i * 2048, 512); }
                SBAR();
#pragma unroll
                for (int i = 0; i < 8; ++i) { csA = frag_sum(fa[i], csA); csB = frag_sum(fb[i], csB); }
                csA = half_sum(csA); csB = half_sum(csB);
                if (TB == 0 && h == 0) { const int n = r ^ ((kq & 1) << 4); cst[kq * 96 + (kq >> 1) * 32 + n] = csA; cst[kq * 96 + ((kq >> 1) ^ 1) * 32 + n] = csB; }
                SBAR();
#pragma unroll
                for (int i = 0; i < 8; ++i) { aA = mfma(fa[i], xa[i], aA); aB = mfma(fb[i], xa[i], aB); }
                PIN2(aA, aB);
            };
            if (kvm) {
                if (kvteam) {
                    u32x4 wk[8];
                    const int a = L_WKV + (kq * 128 + 8 * h + tq) * 64 + tg * 32 + tp * 8;
#pragma unroll
                    for (int i = 0; i < 8; ++i) wk[i] = tr_frag(lds, a + i * 1024, 256);
                    SBAR();
                    float cs = 0.f;
#pragma unroll
                    for (int i = 0; i < 8; ++i) cs = frag_sum(wk[i], cs);
                    cs = half_sum(cs);
                    if (h == 0) cst[kq * 96 + 64 + r] = cs;
                    SBAR();
                    f32x16 ak = {}, av = {};
#pragma unroll
                    for (int i = 0; i < 8; ++i) { ak = mfma(wk[i], xa[i], ak); av = mfma(xa[i], wk[i], av); }
                    store_stats();
                    if (kq == 3 && lane == 0) *(LAS unsigned*)(lds + L_FLAG + 40) = 0u;
                    const int o = L_KVRED + kq * 4096;
                    lds_stf(lds, o + vl, (f32x4){ak[0], ak[1], ak[2], ak[3]}); lds_stf(lds, o + 1024 + vl, (f32x4){ak[4], ak[5], ak[6], ak[7]});
                    if (r >= 16) {
#pragma unroll
                        for (int g = 0; g < 4; ++g) lds_stf(lds, o + 2048 + g * 512 + (h * 16 + r - 16) * 16, (f32x4){av[4 * g], av[4 * g + 1], av[4 * g + 2], av[4 * g + 3]});
                    }
                } else WAITVM(8);
                LDS_BARRIER();
                if (kvteam && kq < 3) {
                    f32x4 a = {}, e = {}, ta[4], te[4];
                    float t1, t2, mu, rstd;
                    if (kq == 0) {
                        f32x4 k0 = {}, k1 = {}, u0[4], u1[4];
#pragma unroll
                        for (int i = 0; i < 4; ++i) { ta[i] = lds_ldf(lds, L_KVRED + i * 4096 + vl); te[i] = lds_ldf(lds, L_KVRED + i * 4096 + 1024 + vl);
                                                      u0[i] = lds_ldf(lds, L_CS + (i * 96 + 64 + 4 * h) * 4); u1[i] = lds_ldf(lds, L_CS + (i * 96 + 64 + 8 + 4 * h) * 4); }
                        stat_sums<4>(lds, tb * 8, r, t1, t2); ln_from_sums(t1, t2, mu, rstd);
#pragma unroll
                        for (int i = 0; i < 4; ++i) { a += ta[i]; e += te[i]; k0 += u0[i]; k1 += u1[i]; }
#pragma unroll
                        for (int i = 0; i < 4; ++i) { a[i] = (a[i] - mu * k0[i]) * rstd; e[i] = (e[i] - mu * k1[i]) * rstd; }
                        st_sc1((u32x4){pk(a[0], a[1]), pk(a[2], a[3]), pk(e[0], e[1]), pk(e[2], e[3])}, rw, vl, OFF_KF + L * 65536 + tbg * 1024);
                        float n2 = (a[0] * a[0] + a[1] * a[1]) + (a[2] * a[2] + a[3] * a[3]) + (e[0] * e[0] + e[1] * e[1]) + (e[2] * e[2] + e[3] * e[3]);
                        n2 = wave_max(half_sum(n2));
                        if (lane == 0) *(LAS float*)(lds + L_FLAG + 36) = n2;
                    } else {
                        const int g = kq - 1, lc = (h * 16 + (r & 15)) * 16;
                        float csv = 0.f, uc[4];
#pragma unroll
                        for (int i = 0; i < 4; ++i) { ta[i] = lds_ldf(lds, L_KVRED + i * 4096 + 2048 + (2 * g) * 512 + lc); te[i] = lds_ldf(lds, L_KVRED + i * 4096 + 2048 + (2 * g + 1) * 512 + lc);
                                                      uc[i] = cst[i * 96 + 64 + 16 + (r & 15)]; }
                        stat_sums<4>(lds, tb * 8, r, t1, t2); ln_from_sums(t1, t2, mu, rstd);
#pragma unroll
                        for (int i = 0; i < 4; ++i) { a += ta[i]; e += te[i]; csv += uc[i]; }
#pragma unroll
                        for (int i = 0; i < 8; ++i) {
                            const int tok = 16 * g + (i & 3) + 8 * (i >> 2) + 4 * h;
                            const float m2 = __uint_as_float(__builtin_amdgcn_ds_bpermute(tok * 4, __float_as_uint(mu))), r2 = __uint_as_float(__builtin_amdgcn_ds_bpermute(tok * 4, __float_as_uint(rstd)));
                            if (i < 4) a[i] = (a[i] - m2 * csv) * r2; else e[i - 4] = (e[i - 4] - m2 * csv) * r2;
                        }
                        if (r >= 16) st_sc1((u32x4){pk(a[0], a[1]), pk(a[2], a[3]), pk(e[0], e[1]), pk(e[2], e[3])}, rw, h * 256 + (r - 16) * 16, OFF_VF + L * 65536 + (2 * tbg + g) * 512);
                    }
                }
                if (kvteam && kq < 3) {
                    DRAIN();
                    unsigned old = 0;
                    if (lane == 0) old = __hip_atomic_fetch_add((LAS unsigned*)(lds + L_FLAG + 40), 1u, __ATOMIC_RELAXED, __HIP_MEMORY_SCOPE_WORKGROUP);
                    if (__builtin_amdgcn_readfirstlane(old) == 2u && lane < 32)
                        __hip_atomic_store((gu64*)(p.ws + OFF_KVFL) + (L * 32 + lane) * 64 + 2 * c + j,
                                           ((unsigned long long)(epoch + 1u + L) << 32) | __float_as_uint(*(volatile LAS float*)(lds + L_FLAG + 36)), RLX_AGENT);
                }
                __builtin_amdgcn_s_setprio(0);
                STAMP(3 + L * 32);
                do_q();
                if (!kvteam) store_stats();
            } else {
                do_q(); store_stats();
                STAMP(3 + L * 32);
            }
            STAMP(4 + L * 32);
            float q[8];
            reduce4(lds, kvm ? (kvteam ? 65536 : 122880) : (TB == 0 ? 98304 : 122880), aA, aB, kq, lane, q);
            STAMP(5 + L * 32);
            float t1, t2;
            f32x4 cs0 = {}, cs1 = {}, u0[4], u1[4];
#pragma unroll
            for (int i = 0; i < 4; ++i) { u0[i] = lds_ldf(lds, L_CS + (i * 96 + 16 * kq + 4 * h) * 4); u1[i] = lds_ldf(lds, L_CS + (i * 96 + 16 * kq + 8 + 4 * h) * 4); }
            stat_sums<4>(lds, tb * 8, r, t1, t2);
#pragma unroll
            for (int i = 0; i < 4; ++i) { cs0 += u0[i]; cs1 += u1[i]; }
            float mu, rstd; ln_from_sums(t1, t2, mu, rstd);
            float n2 = 0.f;
#pragma unroll
            for (int i = 0; i < 4; ++i) { q[i] = (q[i] - mu * cs0[i]) * (rstd * QSCALE); q[4 + i] = (q[4 + i] - mu * cs1[i]) * (rstd * QSCALE); }
#pragma unroll
            for (int i = 0; i < 8; ++i) n2 += q[i] * q[i];
            q2own = half_sum(n2);
            qown = (u32x4){pk(q[0], q[1]), pk(q[2], q[3]), pk(q[4], q[5]), pk(q[6], q[7])};
            lds_st(lds, L_XCHQ + w * 1024 + vl, qown);
            *(LAS float*)(lds + L_STAT + (tb * 8 + 4 + kq) * 256 + lane * 4) = q2own;
            if (L == 0 && !kvm) {
                float cs = 0.f;
#pragma unroll
                for (int i = 0; i < 4; ++i) {
                    const int d = prep_dst(i);
                    if (d < 0) continue;
                    u32x4 fr = tr_frag(lds, L_PREP + (w * 4 + i) * 1024 + (8 * h + tq) * 64 + tg * 32 + tp * 8, 256);
                    if (d >= OFF_W2 && ((d - OFF_W2) >> 10) % KSFP >= KSF) fr = (u32x4){0u, 0u, 0u, 0u};
                    st_sc1(fr, rw, vl, d);
                    cs = frag_sum(fr, cs);
                }
                const int u = c * 6 + (j - 2);
                if (u < 43) {
                    cs = half_sum(cs);
                    LAS float* t = (LAS float*)(lds + L_WQ);
                    if (h == 0) t[w * 32 + r] = cs;
                    LDS_BARRIER();
                    if (tid < 32) { float v = 0.f, pv[8];
#pragma unroll
                        for (int i = 0; i < 8; ++i) pv[i] = t[i * 32 + tid];
                        SBAR();
#pragma unroll
                        for (int i = 0; i < 8; ++i) v += pv[i];
                        __hip_atomic_store((gu32*)(p.ws + OFF_CS1 + (32 * u + tid) * 4), __float_as_uint(v), RLX_AGENT); }
                }
                DRAIN();
            }
            STAMP(6 + L * 32);
        }
        u32x4 woA[8], woB[8];
        {
            PHASE_IDS();
            const float km2 = wait_flags(tid, lds, (gu64*)(p.ws + OFF_KVFL) + (L * 32 + c) * 64, epoch + 1u + L, tmo);
            if (L == 0 && j >= 2 && tid == 0) __hip_atomic_fetch_add(sync + CW, 1u, RLX_AGENT);
            STAMP(7 + L * 32);
            {
                const unsigned char* src = p.ws + (TB == 0 ? OFF_KF : OFF_VF) + L * 65536 + ((w >> 1) & 1) * 32768 + vl;
                const unsigned dst = lbase + (TB == 0 ? L_K : L_V) + ((w >> 1) & 1) * 32768;
                const int t0 = (w & 1) * 6;
#pragma unroll
                for (int i = 0; i < 6; ++i) glds16_sc1(src + (t0 + i) * 1024, dst + (t0 + i) * 1024);
            }
            { unsigned one2 = 0x3f803f80u; asm volatile("" : "+v"(one2)); if (tid < 272) lds_st(lds, L_ONES + tid * 16, (u32x4){one2, one2, one2, one2}); }
            const u32x4 qpar = lds_ld(lds, L_XCHQ + (w ^ 1) * 1024 + vl);
            const float q2par = *(LAS float*)(lds + L_STAT + (tb * 8 + 4 + (kq ^ 1)) * 256 + lane * 4);
            float ma = __builtin_amdgcn_sqrtf(q2own * km2) * 1.02f, mb = __builtin_amdgcn_sqrtf(q2par * km2) * 1.02f;
            const bool unsafe = __any(!(ma <= SAFE_BOUND) || !(mb <= SAFE_BOUND));
            {
                LAS unsigned* fl = (LAS unsigned*)(lds + L_FLAG);
                if (lane == 0) fl[w] = unsafe ? 1u : 0u;
            }
            STAMP(8 + L * 32);
            WAITVM(0);
            LDS_BARRIER();
            {
                LAS unsigned* fl = (LAS unsigned*)(lds + L_FLAG);
                const u32x4 f0 = *(LAS u32x4*)fl, f1 = *(LAS u32x4*)(fl + 4);
                if (__builtin_amdgcn_readfirstlane(f0[0] | f0[1] | f0[2] | f0[3] | f1[0] | f1[1] | f1[2] | f1[3])) {
                    {
                        const unsigned char* src = p.ws + (TB == 0 ? OFF_KF : OFF_VF) + L * 65536 + ((w >> 1) & 1) * 32768 + (12 + (w & 1) * 10) * 1024 + vl;
                        const unsigned dst = lbase + (TB == 0 ? L_K : L_V) + ((w >> 1) & 1) * 32768 + (12 + (w & 1) * 10) * 1024;
#pragma unroll 1
                        for (int i = 0; i < 10; ++i) glds16_sc1(src + i * 1024, __builtin_amdgcn_readfirstlane(dst + i * 1024));
                    }
                    DRAIN();
                    LDS_BARRIER();
                    if (unsafe) {
                        float xa = -INFINITY, xb = -INFINITY;
#pragma unroll 1
                        for (int t = 0; t < 64; ++t) {
                            const u32x4 kf = lds_ld(lds, L_K + t * 1024 + vl);
                            const f32x16 sa = mfma(kf, qown, (f32x16){}), sb = mfma(kf, qpar, (f32x16){});
#pragma unroll
                            for (int i = 0; i < 16; ++i) { xa = fmaxf(xa, sa[i]); xb = fmaxf(xb, sb[i]); }
                        }
                        ma = half_max(xa); mb = half_max(xb);
                    }
                }
            }
            STAMP(9 + L * 32);
            const int kh = kq & 1;
            const bool real = r < 16;
            const unsigned kaddr = L_K + kh * 32768 + vl;
            const unsigned vaddr = real ? (unsigned)(L_V + kh * 32768 + h * 256 + r * 16) : (unsigned)(L_ONES + ((r >= 20 && r < 28) ? 64 : 0));
            AttnState st;
            attn_begin(st, lds, qown, qpar, ma, mb, kaddr, vaddr, real ? 4096u : 0u);
            attn_first(st, (unsigned long long)(uintptr_t)(p.ws + (TB == 0 ? OFF_KF : OFF_VF) + L * 65536 + ((w >> 1) & 1) * 32768 + (12 + (w & 1) * 10) * 1024),
                       lbase + (TB == 0 ? L_K : L_V) + ((w >> 1) & 1) * 32768 + (12 + (w & 1) * 10) * 1024, (unsigned)vl);
            STAMP(10 + L * 32);
            if (L == 0) { if (lane == 0) poll_ge(sync + CW, PREP_WGS, tmo); __builtin_amdgcn_fence(__ATOMIC_ACQUIRE, "wavefront"); }
#pragma unroll
            for (int i = 0; i < 8; ++i) { woA[i] = ld_sc1(rw, vw, OFF_WO + (cbA * KSD + kq * 8 + i) * 1024); woB[i] = ld_sc1(rw, vw, OFF_WO + (cbB * KSD + kq * 8 + i) * 1024); }
            SBAR();
            STAMP(11 + L * 32);
            attn_second(st);
            STAMP(12 + L * 32);
            const f32x16 oa = st.oa, ob = st.ob;
            const int xo = L_XCHO + w * 2304;
            lds_stf(lds, xo + vl, (f32x4){ob[0], ob[1], ob[2], ob[3]}); lds_stf(lds, xo + 1024 + vl, (f32x4){ob[4], ob[5], ob[6], ob[7]});
            *(LAS float*)(lds + xo + 2048 + lane * 4) = ob[8];
            LDS_BARRIER();
            const int xi = L_XCHO + (w ^ 1) * 2304;
            const f32x4 e0 = lds_ldf(lds, xi + vl), e1 = lds_ldf(lds, xi + 1024 + vl);
            const float lpar = *(LAS float*)(lds + xi + 2048 + lane * 4);
            SBAR();
            const float rl = __builtin_amdgcn_rcpf(oa[8] + lpar);
            const u32x4 of = {pk((oa[0] + e0[0]) * rl, (oa[1] + e0[1]) * rl), pk((oa[2] + e0[2]) * rl, (oa[3] + e0[3]) * rl),
                              pk((oa[4] + e1[0]) * rl, (oa[5] + e1[1]) * rl), pk((oa[6] + e1[2]) * rl, (oa[7] + e1[3]) * rl)};
            st_sc1(swap_order(of), rw, vl, OFF_O + L * ACT_BYTES + (tbg * KSD + sown) * 1024);
            publish(tid, cnt + cnt_word(L, CNT_O, c));
            STAMP(13 + L * 32);
        }
        u32x4 w1f[32];
        {
            PHASE_IDS(); W1_IDS();
            LOAD_W1(0);
            STAMP(14 + L * 32);
            wait_cnt(tid, cnt + cnt_word(L, CNT_O, c), 8u, tmo);
            STAMP(15 + L * 32);
            u32x4 xa[8];
#pragma unroll
            for (int i = 0; i < 8; ++i) xa[i] = ld_sc1(rw, vl, OFF_O + L * ACT_BYTES + (tbg * KSD + kq * 8 + i) * 1024);
            SBAR();
            f32x16 aA = {}, aB = {};
#pragma unroll
            for (int i = 0; i < 8; ++i) { aA = mfma(woA[i], xa[i], aA); aB = mfma(woB[i], xa[i], aB); }
            SBAR();
            STAMP(16 + L * 32);
            {
                const unsigned char* src = p.ws + OFF_W2 + ((2 * j + (w & 1)) * KSFP + (w >> 1) * 22 + W2_REG) * 1024 + vl;
#pragma unroll
                for (int i = 0; i < 5; ++i) glds16_sc1(src + i * 1024, lbase + L_W2 + (w * 11 + i) * 1024);
            }
            STAMP(17 + L * 32);
            float o[8];
            reduce4(lds, tb * 24576, aA, aB, kq, lane, o);
#pragma unroll
            for (int i = 0; i < 4; ++i) resid[i] = pk(bflo(resid[i]) + o[2 * i], bfhi(resid[i]) + o[2 * i + 1]);
            st_sc1(swap_order(resid), rw, vl, OFF_X1 + L * ACT_BYTES + (tbg * KSD + sown) * 1024);
            STAMP(18 + L * 32);
            publish(tid, cnt + cnt_word(L, CNT_X1, c));
            STAMP(19 + L * 32);
        }
        u32x4 w2A[W2_REG], w2B[W2_REG];
        {
            PHASE_IDS(); W1_IDS();
            LOAD_W1(16);
            if (w == 1 || w == 2 || w == 3) {
                const int col = min(32 * cb0 + tid - 64, F - 1);
                *(LAS float*)(lds + L_TAB + (tid - 64) * 4) = __uint_as_float(__builtin_amdgcn_raw_buffer_load_b32(rw, col * 4, OFF_CS1, AUX_SC1));
                *(LAS float*)(lds + L_TAB + 768 + (tid - 64) * 4) = bf2f(p.b1[col]);
            }
            STAMP(20 + L * 32);
            wait_cnt(tid, cnt + cnt_word(L, CNT_X1, c), 8u, tmo);
            STAMP(21 + L * 32);
            {
                const unsigned char* src = p.ws + OFF_X1 + L * ACT_BYTES + (2 * c * KSD + 2 * w) * 1024 + vl;
#pragma unroll
                for (int ch = 0; ch < 2; ++ch)
#pragma unroll
                    for (int t = 0; t < 2; ++t)
#pragma unroll
                        for (int i = 0; i < 2; ++i) glds16_sc1(src + (t * KSD + 16 * ch + i) * 1024, lbase + L_ACT + (t * KSD + 16 * ch + 2 * w + i) * 1024);
            }
            WAITVM(4);
            LDS_BARRIER();
            STAMP(22 + L * 32);
            f32x16 a0 = {}, a1 = {};
            float s1[2] = {0.f, 0.f}, s2[2] = {0.f, 0.f};
            const unsigned char* w2l_src = p.ws + OFF_W2 + ((2 * j + (w & 1)) * KSFP + (w >> 1) * 22 + W2_REG) * 1024 + vl;
            auto w2_req = [&](int n) {
                if (n < 6) glds16_sc1(w2l_src + (5 + n) * 1024, lbase + L_W2 + (w * 11 + 5 + n) * 1024);
                else if (n < 6 + W2_REG) w2A[n - 6] = ld_sc1(rw, vw, OFF_W2 + (cbA * KSFP + kq * 22 + n - 6) * 1024);
                else if (n < 6 + 2 * W2_REG) w2B[n - 6 - W2_REG] = ld_sc1(rw, vw, OFF_W2 + (cbB * KSFP + kq * 22 + n - 6 - W2_REG) * 1024);
            };
            auto part = [&](int lo) {
                const int n0 = lo == 0 ? 0 : 14;
                if (TB == 0) {
                    u32x4 b0[3], b1[3];
#pragma unroll
                    for (int i = 0; i < 2; ++i) { b0[i] = lds_ld(lds, L_ACT + (lo + i) * 1024 + vl); b1[i] = lds_ld(lds, L_ACT + (KSD + lo + i) * 1024 + vl); }
#pragma unroll
                    for (int s = 0; s < 16; ++s) {
                        if (s + 2 < 16) { b0[(s + 2) % 3] = lds_ld(lds, L_ACT + (lo + s + 2) * 1024 + vl); b1[(s + 2) % 3] = lds_ld(lds, L_ACT + (KSD + lo + s + 2) * 1024 + vl); }
                        SBAR();
                        a0 = mfma(w1f[lo + s], b0[s % 3], a0); a1 = mfma(w1f[lo + s], b1[s % 3], a1);
                        PIN2(a0, a1);
                        if (s < 14) w2_req(n0 + s);
                        SBAR();
                    }
                } else {
                    u32x4 b0[3], b1[3];
                    const int ab = L_ACT + (lo + par) * 1024 + vl;
#pragma unroll
                    for (int i = 0; i < 2; ++i) { b0[i] = lds_ld(lds, ab + 2 * i * 1024); b1[i] = lds_ld(lds, ab + (KSD + 2 * i) * 1024); }
#pragma unroll
                    for (int s = 0; s < 8; ++s) {
                        if (s + 2 < 8) { b0[(s + 2) % 3] = lds_ld(lds, ab + 2 * (s + 2) * 1024); b1[(s + 2) % 3] = lds_ld(lds, ab + (KSD + 2 * (s + 2)) * 1024); }
                        SBAR();
                        a0 = mfma(w1f[lo + s], b0[s % 3], a0); a1 = mfma(w1f[lo + s], b1[s % 3], a1);
                        PIN2(a0, a1);
                        if (s < 7) { w2_req(n0 + 2 * s); w2_req(n0 + 2 * s + 1); }
                        SBAR();
                    }
                }
            };
            auto stats = [&](int lo) {
                u32x4 v[4];
#pragma unroll
                for (int i = 0; i < 4; ++i) v[i] = lds_ld(lds, L_ACT + ((i >> 1) * KSD + lo + 2 * w + (i & 1)) * 1024 + vl);
                SBAR();
#pragma unroll
                for (int i = 0; i < 4; ++i) stats_acc(v[i], s1[i >> 1], s2[i >> 1]);
            };
            stats(0);
            SBAR();
            part(0);
            STAMP(23 + L * 32);
            WAITVM(14);
            LDS_BARRIER();
            stats(16);
            {
                LAS f32x2* st = (LAS f32x2*)(lds + L_STAT);
#pragma unroll
                for (int t = 0; t < 2; ++t) { const float u1 = half_sum(s1[t]), u2 = half_sum(s2[t]); if (h == 0) st[(t * 8 + w) * 32 + r] = (f32x2){u1, u2}; }
            }
            SBAR();
            part(16);
            STAMP(24 + L * 32);
            if (TB == 1) {
                const int xo = L_ACT + (w - 4) * 4096 + vl;
#pragma unroll
                for (int g = 0; g < 4; ++g) {
                    const f32x4 v = par ? (f32x4){a0[4 * g], a0[4 * g + 1], a0[4 * g + 2], a0[4 * g + 3]} : (f32x4){a1[4 * g], a1[4 * g + 1], a1[4 * g + 2], a1[4 * g + 3]};
                    lds_stf(lds, xo + g * 1024, v);
                }
            }
            LDS_BARRIER();
            if (TB == 1) {
                const int xi = L_ACT + ((w - 4) ^ 1) * 4096 + vl;
                f32x4 v[4];
#pragma unroll
                for (int g = 0; g < 4; ++g) v[g] = lds_ldf(lds, xi + g * 1024);
                SBAR();
#pragma unroll
                for (int g = 0; g < 4; ++g)
#pragma unroll
                    for (int i = 0; i < 4; ++i) a0[4 * g + i] = (par ? a1[4 * g + i] : a0[4 * g + i]) + v[g][i];
            }
            STAMP(25 + L * 32);
            if (d_active) {
                auto finish = [&](const f32x16& acc, int t) {
                    f32x4 tcs[4], tbb[4];
#pragma unroll
                    for (int g = 0; g < 4; ++g) { tcs[g] = lds_ldf(lds, L_TAB + (32 * cb1 + 8 * g + 4 * h) * 4); tbb[g] = lds_ldf(lds, L_TAB + 768 + (32 * cb1 + 8 * g + 4 * h) * 4); }
                    float t1, t2; stat_sums<8>(lds, t * 8, r, t1, t2);
                    float mu, rstd; ln_from_sums(t1, t2, mu, rstd);
                    const float nmr = -mu * rstd;
                    unsigned hp[8];
#pragma unroll
                    for (int g = 0; g < 4; ++g) {
                        const f32x4 cs = tcs[g], bb = tbb[g];
                        float y[4];
#pragma unroll
                        for (int i = 0; i < 4; ++i) {
                            const float x = fmaf(acc[4 * g + i], rstd, fmaf(cs[i], nmr, bb[i]));
                            const float t = x * fmaf(x * x, -2.f * 0.7978845608028654f * 0.044715f * 1.4426950408889634f, -2.f * 0.7978845608028654f * 1.4426950408889634f);
                            y[i] = x * __builtin_amdgcn_rcpf(1.f + __builtin_amdgcn_exp2f(t));
                        }
                        hp[2 * g] = pk(y[0], y[1]); hp[2 * g + 1] = pk(y[2], y[3]);
                    }
                    const int so = OFF_H + L * H_BYTES + ((2 * c + t) * KSF + 2 * cbg1) * 1024;
                    st_sc1(swap_order((u32x4){hp[0], hp[1], hp[2], hp[3]}), rw, vl, so);
                    st_sc1(swap_order((u32x4){hp[4], hp[5], hp[6], hp[7]}), rw, vl, so + 1024);
                };
                if (TB == 0) { finish(a0, 0); finish(a1, 1); } else finish(a0, par);
            }
            STAMP(26 + L * 32);
            publish(tid, cnt + cnt_word(L, CNT_H, c));
            STAMP(27 + L * 32);
        }
        {
            PHASE_IDS();
            const u32x2 bb0 = *(const GAS u32x2*)(p.b2 + 16 * sown + 4 * h), bb1 = *(const GAS u32x2*)(p.b2 + 16 * sown + 8 + 4 * h);
            wait_cnt(tid, cnt + cnt_word(L, CNT_H, c), 8u, tmo);
            STAMP(28 + L * 32);
            u32x4 ha[22];
            const int hso = OFF_H + L * H_BYTES + tbg * KSF * 1024;
            constexpr int EWIN = 8;
#pragma unroll
            for (int i = 0; i < EWIN; ++i) ha[i] = ld_sc1(rw, vl, hso + min(kq * 22 + i, KSF - 1) * 1024);
            SBAR();
            f32x16 aA = {}, aB = {};
            const int oa_ = L_W2 + (kq * 22 + (kq >> 1) * 11) * 1024 + vw, ob_ = L_W2 + (kq * 22 + ((kq >> 1) ^ 1) * 11) * 1024 + vw;
            u32x4 la[3], lb[3];
#pragma unroll
            for (int i = 0; i < 22; ++i) {
                if (i + EWIN < 22) ha[i + EWIN] = ld_sc1(rw, vl, hso + min(kq * 22 + i + EWIN, KSF - 1) * 1024);
                if (i + 2 >= W2_REG && i + 2 < 22) { la[(i + 2 - W2_REG) % 3] = lds_ld(lds, oa_ + (i + 2 - W2_REG) * 1024); lb[(i + 2 - W2_REG) % 3] = lds_ld(lds, ob_ + (i + 2 - W2_REG) * 1024); }
                SBAR();
                if (i < W2_REG) { aA = mfma(w2A[i], ha[i], aA); aB = mfma(w2B[i], ha[i], aB); }
                else { aA = mfma(la[(i - W2_REG) % 3], ha[i], aA); aB = mfma(lb[(i - W2_REG) % 3], ha[i], aB); }
                PIN2(aA, aB);
                SBAR();
            }
            STAMP(29 + L * 32);
            float o[8];
            reduce4(lds, tb * 24576, aA, aB, kq, lane, o);
#pragma unroll
            for (int i = 0; i < 2; ++i) { o[2 * i] += bflo(bb0[i]); o[2 * i + 1] += bfhi(bb0[i]); o[4 + 2 * i] += bflo(bb1[i]); o[4 + 2 * i + 1] += bfhi(bb1[i]); }
#pragma unroll
            for (int i = 0; i < 4; ++i) resid[i] = pk(bflo(resid[i]) + o[2 * i], bfhi(resid[i]) + o[2 * i + 1]);
            STAMP(30 + L * 32);
            const u32x4 xo = swap_order(resid);
            if (L == 0) {
                st_sc1(xo, rw, vl, OFF_X2 + (tbg * KSD + sown) * 1024);
                publish(tid, cnt + cnt_word(0, CNT_X2, c));
            } else {
                *(GAS u32x4*)(p.out + (size_t)(64 * c + 32 * tb + r) * D + 16 * sown + 8 * h) = xo;
            }
            STAMP(31 + L * 32);
        }
    }
    { unsigned e = epoch0; asm volatile("" : "+s"(e)); if (blockIdx.x == 0 && threadIdx.x == 0) __hip_atomic_store(sync, e + 2u, RLX_AGENT); }
}

__global__ void __launch_bounds__(512, 2) main_kernel(Params p) {
    extern __shared__ __attribute__((aligned(16))) char lds_dyn[];
    LAS char* lds = (LAS char*)lds_dyn;
    if (__builtin_amdgcn_readfirstlane(threadIdx.x >> 8) == 0) run_layers<0>(p, lds); else run_layers<1>(p, lds);
}

}

extern "C" void kernel_launch(void* const* d_in, const int* in_sizes, int n_in, void* d_out, int out_size, void* d_ws, size_t ws_size, hipStream_t stream) {
    Params p;
    p.x = (const uint16_t*)d_in[0]; p.wq = (const uint16_t*)d_in[1]; p.wk = (const uint16_t*)d_in[2]; p.wv = (const uint16_t*)d_in[3]; p.wo = (const uint16_t*)d_in[4];
    p.w1 = (const uint16_t*)d_in[6]; p.b1 = (const uint16_t*)d_in[7]; p.w2 = (const uint16_t*)d_in[8]; p.b2 = (const uint16_t*)d_in[9];
    p.out = (uint16_t*)d_out; p.ws = (unsigned char*)d_ws;
    (void)hipFuncSetAttribute((const void*)main_kernel, hipFuncAttributeMaxDynamicSharedMemorySize, LDS_BYTES);
    hipLaunchKernelGGL(main_kernel, dim3(256), dim3(512), LDS_BYTES, stream, p);
}
```

```cpp
#include <hip/hip_runtime.h>
#include <hip/hip_bf16.h>
#include <stdint.h>

#define GAS __attribute__((address_space(1)))
#define LAS __attribute__((address_space(3)))
typedef __bf16 bf16x8 __attribute__((ext_vector_type(8)));
typedef __bf16 bf16x2 __attribute__((ext_vector_type(2)));
typedef float f32x16 __attribute__((ext_vector_type(16)));
typedef float f32x4 __attribute__((ext_vector_type(4)));
typedef float f32x2 __attribute__((ext_vector_type(2)));
typedef unsigned u32x4 __attribute__((ext_vector_type(4)));
typedef GAS unsigned gu32;
#define RLX_AGENT __ATOMIC_RELAXED, __HIP_MEMORY_SCOPE_AGENT

namespace {

constexpr int S = 2048, D = 512, F = 1376;
constexpr int KSD = 32;
constexpr int KSF = 86;
constexpr int KSFP = 88;
constexpr float LN_EPS = 1e-5f;
constexpr float QSCALE = 0.25f * 1.4426950408889634f;
constexpr float SAFE_BOUND = 50.f;

constexpr int CW = 64;
constexpr int OFF_CNT = 0;
constexpr int CNT_O = 0, CNT_X1 = 1, CNT_H = 2, CNT_X2 = 3;
__device__ __forceinline__ int cnt_word(int layer, int kind, int c) { return ((layer * 4 + kind) * 32 + c) * CW; }
constexpr int OFF_SYNC = 65536;
constexpr int OFF_KVFL = OFF_SYNC + 1024;
constexpr int OFF_WO = OFF_KVFL + 2 * 32 * 512;
constexpr int OFF_W1 = OFF_WO + 16 * KSD * 1024;
constexpr int OFF_W2 = OFF_W1 + 43 * KSD * 1024;
constexpr int OFF_CS1 = OFF_W2 + 16 * KSFP * 1024;
constexpr int OFF_KF = (OFF_CS1 + 1408 * 4 + 1023) / 1024 * 1024;
constexpr int OFF_VF = OFF_KF + 2 * 65536;
constexpr int OFF_O = OFF_VF + 2 * 65536;
constexpr int ACT_BYTES = 64 * KSD * 1024;
constexpr int OFF_X1 = OFF_O + 2 * ACT_BYTES;
constexpr int OFF_X2 = OFF_X1 + 2 * ACT_BYTES;
constexpr int H_BYTES = 64 * KSF * 1024;
constexpr int OFF_H = OFF_X2 + ACT_BYTES;
constexpr int WS_TOTAL = OFF_H + 2 * H_BYTES;
constexpr unsigned SPIN_CAP = 1u << 15;
constexpr unsigned PREP_WGS = 192;

constexpr int L_K = 0, L_V = 65536, L_ONES = 131072, L_XCHO = 135424;
constexpr int L_WQ = 65536, L_WKV = 0, L_PREP = 0, L_KVRED = 32768, L_XCHQ = 57344;
constexpr int L_ACT = 0, L_W2A = 65536, L_W2B = 0;
constexpr int L_STAT = 155648, L_TAB = 159744, L_CS = L_TAB, L_FLAG = 163584;
constexpr int F_UNSAFE = L_FLAG, F_KM2 = L_FLAG + 32, F_N2 = L_FLAG + 36, F_KVCNT = L_FLAG + 40, F_OCNT = L_FLAG + 44, F_PAIR = L_FLAG + 64;
constexpr int LDS_BYTES = 163840;
constexpr int W2_REG = 3, W2_A = 11, W2_B = 8;
constexpr int W1_EARLY = 24;

#define PROF 0
#define PROF_TID 0
constexpr int L_PROF = 161280;
constexpr int OFF_PROF = 64 << 20;
#define STAMP(i) do { } while (0)

struct Params {
    const uint16_t *x, *wq, *wk, *wv, *wo, *w1, *b1, *w2, *b2;
    uint16_t* out;
    unsigned char* ws;
};

__device__ __forceinline__ f32x16 mfma(u32x4 a, u32x4 b, f32x16 c) {
    return __builtin_amdgcn_mfma_f32_32x32x16_bf16(__builtin_bit_cast(bf16x8, a), __builtin_bit_cast(bf16x8, b), c, 0, 0, 0);
}
__device__ __forceinline__ unsigned pk(float a, float b) { f32x2 v = {a, b}; return __builtin_bit_cast(unsigned, __builtin_convertvector(v, bf16x2)); }
__device__ __forceinline__ float bflo(unsigned u) { return __uint_as_float(u << 16); }
__device__ __forceinline__ float bfhi(unsigned u) { return __uint_as_float(u & 0xffff0000u); }
__device__ __forceinline__ float bf2f(uint16_t v) { return __uint_as_float((unsigned)v << 16); }
__device__ __forceinline__ float dot2(unsigned a, unsigned b, float c) {
    return __builtin_amdgcn_fdot2_f32_bf16(__builtin_bit_cast(bf16x2, a), __builtin_bit_cast(bf16x2, b), c, false);
}
__device__ __forceinline__ float half_sum(float v) {
    auto r = __builtin_amdgcn_permlane32_swap(__float_as_uint(v), __float_as_uint(v), false, false);
    return __uint_as_float(r[0]) + __uint_as_float(r[1]);
}
__device__ __forceinline__ float half_max(float v) {
    auto r = __builtin_amdgcn_permlane32_swap(__float_as_uint(v), __float_as_uint(v), false, false);
    return fmaxf(__uint_as_float(r[0]), __uint_as_float(r[1]));
}
__device__ __forceinline__ u32x4 swap_order(u32x4 a) {
    auto r0 = __builtin_amdgcn_permlane32_swap(a[0], a[2], false, false);
    auto r1 = __builtin_amdgcn_permlane32_swap(a[1], a[3], false, false);
    return (u32x4){r0[0], r1[0], r0[1], r1[1]};
}
template <int CTRL> __device__ __forceinline__ float dpp(float v) {
    return __uint_as_float(__builtin_amdgcn_update_dpp(0u, __float_as_uint(v), CTRL, 0xf, 0xf, true));
}
__device__ __forceinline__ float wave_max(float v) {
    v = fmaxf(v, dpp<0xB1>(v));
    v = fmaxf(v, dpp<0x4E>(v));
    v = fmaxf(v, dpp<0x141>(v));
    v = fmaxf(v, dpp<0x140>(v));
    auto r = __builtin_amdgcn_permlane16_swap(__float_as_uint(v), __float_as_uint(v), false, false);
    return half_max(fmaxf(__uint_as_float(r[0]), __uint_as_float(r[1])));
}
#define SBAR() __builtin_amdgcn_sched_barrier(0)
#define PIN2(a, b) asm volatile("" : "+v"(a), "+v"(b))
#define LDS_BARRIER() asm volatile("s_waitcnt lgkmcnt(0)\n\ts_barrier" ::: "memory")
#define DRAIN() asm volatile("s_waitcnt vmcnt(0)" ::: "memory")
constexpr int AUX_SC1 = 16;
typedef __amdgpu_buffer_rsrc_t rsrc_t;
__device__ __forceinline__ u32x4 ld_sc1(rsrc_t r, int voff, int soff) { return __builtin_amdgcn_raw_buffer_load_b128(r, voff, soff, AUX_SC1); }
__device__ __forceinline__ u32x4 ld_plain(rsrc_t r, int voff, int soff) { return __builtin_amdgcn_raw_buffer_load_b128(r, voff, soff, 0); }
__device__ __forceinline__ void st_sc1(u32x4 v, rsrc_t r, int voff, int soff) {
    __builtin_amdgcn_raw_buffer_store_b128(v, r, voff, soff, AUX_SC1);
    asm volatile("s_nop 1" :: "v"(v) : "memory");
}
__device__ __forceinline__ u32x4 lds_ld(LAS char* l, int off) { return *(LAS u32x4*)(l + off); }
__device__ __forceinline__ void lds_st(LAS char* l, int off, u32x4 v) { *(LAS u32x4*)(l + off) = v; }
__device__ __forceinline__ f32x4 lds_ldf(LAS char* l, int off) { return *(LAS f32x4*)(l + off); }
__device__ __forceinline__ void lds_stf(LAS char* l, int off, f32x4 v) { *(LAS f32x4*)(l + off) = v; }

__device__ __forceinline__ void publish(int tid, gu32* cnt) {
    DRAIN();
    LDS_BARRIER();
    if (tid == 0) __hip_atomic_fetch_add(cnt, 1u, RLX_AGENT);
}
__device__ __forceinline__ void poll_ge(gu32* cnt, unsigned want, gu32* tmo) {
    unsigned spins = 0;
    while (__hip_atomic_load(cnt, RLX_AGENT) < want) {
        if (++spins > SPIN_CAP) { __hip_atomic_store(tmo, 1u, RLX_AGENT); break; }
    }
}
__device__ __forceinline__ void wait_cnt(int tid, gu32* cnt, unsigned want, gu32* tmo) {
    if (tid == 0) poll_ge(cnt, want, tmo);
    __builtin_amdgcn_fence(__ATOMIC_ACQUIRE, "wavefront");
    LDS_BARRIER();
}
typedef GAS unsigned long long gu64;
__device__ __forceinline__ float wait_flags(int tid, LAS char* lds, gu64* fl, unsigned tag, gu32* tmo) {
    if (tid < 64) {
        unsigned spins = 0; unsigned long long v;
        while (v = __hip_atomic_load(fl + tid, RLX_AGENT), !__all((unsigned)(v >> 32) == tag)) {
            if (++spins > SPIN_CAP) { if (tid == 0) __hip_atomic_store(tmo, 1u, RLX_AGENT); break; }
        }
        const float m = wave_max(__uint_as_float((unsigned)v));
        if (tid == 0) *(LAS float*)(lds + F_KM2) = m;
    }
    __builtin_amdgcn_fence(__ATOMIC_ACQUIRE, "wavefront");
    LDS_BARRIER();
    return *(LAS float*)(lds + F_KM2);
}
__device__ __forceinline__ void glds16(const void* g, unsigned lds_dst) {
    unsigned keep;
    asm volatile("s_mov_b32 %0, m0\n\ts_mov_b32 m0, %2\n\ts_nop 0\n\tglobal_load_lds_dwordx4 %1, off\n\ts_mov_b32 m0, %0" : "=&s"(keep) : "v"(g), "s"(lds_dst) : "memory");
}
__device__ __forceinline__ void glds16_sc1(const void* g, unsigned lds_dst) {
    unsigned keep;
    asm volatile("s_mov_b32 %0, m0\n\ts_mov_b32 m0, %2\n\ts_nop 0\n\tglobal_load_lds_dwordx4 %1, off sc1\n\ts_mov_b32 m0, %0" : "=&s"(keep) : "v"(g), "s"(lds_dst) : "memory");
}
#define WAITVM(n) asm volatile("s_waitcnt vmcnt(" #n ")" ::: "memory")

__device__ __forceinline__ void reduce4(LAS char* lds, int region, const f32x16& aA, const f32x16& aB, int kq, int lane, float (&o)[8]) {
    const int base = region + lane * 16;
    { const int a = base + ((kq ^ 1) * 3 + 0) * 2048;
      lds_stf(lds, a, (f32x4){aA[8], aA[9], aA[10], aA[11]}); lds_stf(lds, a + 1024, (f32x4){aA[12], aA[13], aA[14], aA[15]}); }
    { const int a = base + ((kq ^ 2) * 3 + 1) * 2048;
      lds_stf(lds, a, (f32x4){aB[0], aB[1], aB[2], aB[3]}); lds_stf(lds, a + 1024, (f32x4){aB[4], aB[5], aB[6], aB[7]}); }
    { const int a = base + ((kq ^ 3) * 3 + 2) * 2048;
      lds_stf(lds, a, (f32x4){aB[8], aB[9], aB[10], aB[11]}); lds_stf(lds, a + 1024, (f32x4){aB[12], aB[13], aB[14], aB[15]}); }
    LDS_BARRIER();
    f32x4 lo = {aA[0], aA[1], aA[2], aA[3]}, hi = {aA[4], aA[5], aA[6], aA[7]}, t[6];
#pragma unroll
    for (int x = 0; x < 6; ++x) t[x] = lds_ldf(lds, base + kq * 3 * 2048 + x * 1024);
    SBAR();
#pragma unroll
    for (int x = 0; x < 3; ++x) { lo += t[2 * x]; hi += t[2 * x + 1]; }
#pragma unroll
    for (int i = 0; i < 4; ++i) { o[i] = lo[i]; o[4 + i] = hi[i]; }
}
typedef short v4i16 __attribute__((ext_vector_type(4)));
typedef unsigned u32x2 __attribute__((ext_vector_type(2)));
__device__ __forceinline__ u32x4 tr_frag(LAS char* lds, int a, int step) {
    const u32x2 lo = __builtin_bit_cast(u32x2, __builtin_amdgcn_ds_read_tr16_b64_v4i16((LAS v4i16*)(lds + a)));
    const u32x2 hi = __builtin_bit_cast(u32x2, __builtin_amdgcn_ds_read_tr16_b64_v4i16((LAS v4i16*)(lds + a + step)));
    return (u32x4){lo[0], lo[1], hi[0], hi[1]};
}
__device__ __forceinline__ float frag_sum(u32x4 f, float s) {
#pragma unroll
    for (int d = 0; d < 4; ++d) s = dot2(f[d], 0x3f803f80u, s);
    return s;
}

#define A_E1(a) "v_exp_f32 v" #a ", v" #a "\n\t"
#define A_E4(a, b, c, d) A_E1(a) A_E1(b) A_E1(c) A_E1(d)
#define A_CVT(d, a, b) "v_cvt_pk_bf16_f32 v" #d ", v" #a ", v" #b "\n\t"
#define A_DSK(off) "ds_read_b128 v[92:95], v112 offset:" #off "\n\t"
#define A_DSV0(off) "ds_read_b128 v[96:99], v113 offset:" #off "\n\t"
#define A_DSV1(off) "ds_read_b128 v[100:103], v113 offset:" #off "\n\t"
#define A_WAIT(n) "s_waitcnt lgkmcnt(" #n ")\n\t"
#define A_MSB "v_mfma_f32_32x32x16_bf16 v[40:55], v[92:95], v[148:151], v[116:131]\n\t"
#define A_MSA "v_mfma_f32_32x32x16_bf16 v[24:39], v[92:95], v[88:91], v[56:71]\n\t"
#define A_MPV(O, V, P) "v_mfma_f32_32x32x16_bf16 " O ", " V ", " P ", " O "\n\t"
#define A_OA "v[72:87]"
#define A_OB "v[132:147]"
#define A_V0 "v[96:99]"
#define A_V1 "v[100:103]"
#define A_P0 "v[104:107]"
#define A_P1 "v[108:111]"
#define A_STEP(koff, v1off, v0off) \
    A_MSB A_DSK(koff) A_E4(24, 25, 26, 27) \
    A_MPV(A_OB, A_V1, A_P1) A_DSV1(v1off) A_E4(28, 29, 30, 31) \
    A_CVT(104, 24, 25) A_CVT(105, 26, 27) A_CVT(106, 28, 29) A_CVT(107, 30, 31) \
    A_E4(32, 33, 34, 35) \
    A_WAIT(2) A_MPV(A_OA, A_V0, A_P0) A_E4(36, 37, 38, 39) \
    A_CVT(108, 32, 33) A_CVT(109, 34, 35) A_CVT(110, 36, 37) A_CVT(111, 38, 39) \
    A_WAIT(1) A_MSA A_E4(40, 41, 42, 43) \
    A_WAIT(0) A_MPV(A_OA, A_V1, A_P1) A_E4(44, 45, 46, 47) \
    A_CVT(104, 40, 41) A_CVT(105, 42, 43) A_CVT(106, 44, 45) A_CVT(107, 46, 47) \
    A_E4(48, 49, 50, 51) \
    A_MPV(A_OB, A_V0, A_P0) A_DSV0(v0off) A_E4(52, 53, 54, 55) \
    A_CVT(108, 48, 49) A_CVT(109, 50, 51) A_CVT(110, 52, 53) A_CVT(111, 54, 55)

struct AttnState { f32x16 sa, sb, oa, ob, negma, negmb; u32x4 qa, qb, kf, v0, v1, p1; unsigned kaddr, vaddr, vstride; };
#define A_OPERANDS(st, cnt) \
        : "+{v[24:39]}"(st.sa), "+{v[40:55]}"(st.sb), "+{v[72:87]}"(st.oa), "+{v[132:147]}"(st.ob), "+{v[92:95]}"(st.kf), "+{v[96:99]}"(st.v0), "+{v[100:103]}"(st.v1), \
          "={v[104:107]}"(p0), "+{v[108:111]}"(st.p1), "+{v112}"(st.kaddr), "+{v113}"(st.vaddr), [n] "+s"(cnt) \
        : "{v[56:71]}"(st.negma), "{v[116:131]}"(st.negmb), "{v[88:91]}"(st.qa), "{v[148:151]}"(st.qb), "{v114}"(st.vstride) \
        : "memory", "scc"
#define A_ITER \
        A_STEP(1024, 512, 1024) \
        A_STEP(2048, 1536, 2048) \
        A_STEP(3072, 2560, 3072) \
        A_STEP(4096, 3584, 4096) \
        "v_add_u32 v112, 4096, v112\n\t" \
        "v_add_u32 v113, v114, v113\n\t"
__device__ __forceinline__ void attn_begin(AttnState& st, LAS char* lds, u32x4 qa, u32x4 qb, float ma, float mb, unsigned kaddr, unsigned vaddr, unsigned vstride) {
#pragma unroll
    for (int i = 0; i < 16; ++i) { st.negma[i] = -ma; st.negmb[i] = -mb; }
    st.qa = qa; st.qb = qb;
    st.kf = lds_ld(lds, kaddr); st.v0 = lds_ld(lds, vaddr); st.v1 = st.v0; st.p1 = (u32x4){0u, 0u, 0u, 0u};
    st.sa = mfma(st.kf, qa, st.negma); st.sb = st.sa;
    st.oa = (f32x16){}; st.ob = (f32x16){};
    const unsigned lbase = (unsigned)(uintptr_t)lds;
    st.kaddr = kaddr + lbase; st.vaddr = vaddr + lbase; st.vstride = vstride;
}
#define A_DMA \
    "s_mov_b32 m0, s92\n\t" \
    "s_add_u32 s92, s92, 1024\n\t" \
    "global_load_lds_dwordx4 v115, s[90:91] sc1\n\t" \
    "s_add_u32 s90, s90, 1024\n\t" \
    "s_addc_u32 s91, s91, 0\n\t"
#define A_ITER_DMA \
        A_DMA A_DMA A_STEP(1024, 512, 1024) \
        A_DMA A_STEP(2048, 1536, 2048) \
        A_DMA A_STEP(3072, 2560, 3072) \
        A_DMA A_STEP(4096, 3584, 4096) \
        "v_add_u32 v112, 4096, v112\n\t" \
        "v_add_u32 v113, v114, v113\n\t"
__device__ __forceinline__ void attn_first(AttnState& st, unsigned long long src, unsigned dst, unsigned voff) {
    unsigned cnt = 2, keep; u32x4 p0;
    asm volatile(
        "s_mov_b32 %[keep], m0\n\t"
        "s_nop 15\n\t"
        A_ITER_DMA
        A_ITER_DMA
        "s_mov_b32 m0, %[keep]\n\t"
        "s_waitcnt vmcnt(0)\n\t"
        "s_barrier\n\t"
        "ATT1_LOOP_%=:\n\t"
        A_ITER
        "s_sub_u32 %[n], %[n], 1\n\t"
        "s_cmp_lg_u32 %[n], 0\n\t"
        "s_cbranch_scc1 ATT1_LOOP_%=\n\t"
        : "+{v[24:39]}"(st.sa), "+{v[40:55]}"(st.sb), "+{v[72:87]}"(st.oa), "+{v[132:147]}"(st.ob), "+{v[92:95]}"(st.kf), "+{v[96:99]}"(st.v0), "+{v[100:103]}"(st.v1),
          "={v[104:107]}"(p0), "+{v[108:111]}"(st.p1), "+{v112}"(st.kaddr), "+{v113}"(st.vaddr), [n] "+s"(cnt), [keep] "=&s"(keep), "+{s[90:91]}"(src), "+{s92}"(dst)
        : "{v[56:71]}"(st.negma), "{v[116:131]}"(st.negmb), "{v[88:91]}"(st.qa), "{v[148:151]}"(st.qb), "{v114}"(st.vstride), "{v115}"(voff)
        : "memory", "scc");
}
typedef unsigned u32x16 __attribute__((ext_vector_type(16)));
struct Prefetch { u32x16 a0, a1, b0, b1, w0, w1; };
template <int TB>
__device__ __forceinline__ void attn_second(AttnState& st, Prefetch& pf, u32x4 rs, unsigned voffw, unsigned voffl, unsigned soa, unsigned sob, unsigned sow) {
    u32x4 p0;
    if constexpr (TB == 0) {
    asm volatile(
        "buffer_load_dwordx4 v[152:155], v115, %[rs], s93 offen offset:0 sc1\n\t" A_STEP(1024, 512, 1024)
        "buffer_load_dwordx4 v[156:159], v115, %[rs], s93 offen offset:1024 sc1\n\t" A_STEP(2048, 1536, 2048)
        "buffer_load_dwordx4 v[160:163], v115, %[rs], s93 offen offset:2048 sc1\n\t" A_STEP(3072, 2560, 3072)
        "buffer_load_dwordx4 v[164:167], v115, %[rs], s93 offen offset:3072 sc1\n\t" "s_add_u32 s93, s93, 4096\n\t" A_STEP(4096, 3584, 4096)
        "v_add_u32 v112, 4096, v112\n\t" "v_add_u32 v113, v114, v113\n\t"
        "buffer_load_dwordx4 v[168:171], v115, %[rs], s93 offen offset:0 sc1\n\t" A_STEP(1024, 512, 1024)
        "buffer_load_dwordx4 v[172:175], v115, %[rs], s93 offen offset:1024 sc1\n\t" A_STEP(2048, 1536, 2048)
        "buffer_load_dwordx4 v[176:179], v115, %[rs], s93 offen offset:2048 sc1\n\t" A_STEP(3072, 2560, 3072)
        "buffer_load_dwordx4 v[180:183], v115, %[rs], s93 offen offset:3072 sc1\n\t" "s_add_u32 s93, s93, 4096\n\t" A_STEP(4096, 3584, 4096)
        "v_add_u32 v112, 4096, v112\n\t" "v_add_u32 v113, v114, v113\n\t"
        "buffer_load_dwordx4 v[184:187], v115, %[rs], s94 offen offset:0 sc1\n\t" A_STEP(1024, 512, 1024)
        "buffer_load_dwordx4 v[188:191], v115, %[rs], s94 offen offset:1024 sc1\n\t" A_STEP(2048, 1536, 2048)
        "buffer_load_dwordx4 v[192:195], v115, %[rs], s94 offen offset:2048 sc1\n\t" A_STEP(3072, 2560, 3072)
        "buffer_load_dwordx4 v[196:199], v115, %[rs], s94 offen offset:3072 sc1\n\t" "s_add_u32 s94, s94, 4096\n\t" A_STEP(4096, 3584, 4096)
        "v_add_u32 v112, 4096, v112\n\t" "v_add_u32 v113, v114, v113\n\t"
        "buffer_load_dwordx4 v[200:203], v115, %[rs], s94 offen offset:0 sc1\n\t" A_STEP(1024, 512, 1024)
        "buffer_load_dwordx4 v[204:207], v115, %[rs], s94 offen offset:1024 sc1\n\t" A_STEP(2048, 1536, 2048)
        "buffer_load_dwordx4 v[208:211], v115, %[rs], s94 offen offset:2048 sc1\n\t" A_STEP(3072, 2560, 3072)
        "buffer_load_dwordx4 v[212:215], v115, %[rs], s94 offen offset:3072 sc1\n\t" "s_add_u32 s94, s94, 4096\n\t" A_STEP(4096, 3584, 4096)
        "v_add_u32 v112, 4096, v112\n\t" "v_add_u32 v113, v114, v113\n\t"
        "s_waitcnt lgkmcnt(0)\n\t"
        A_MPV(A_OB, A_V1, A_P1)
        "s_nop 15\n\t"
        : "+{v[24:39]}"(st.sa), "+{v[40:55]}"(st.sb), "+{v[72:87]}"(st.oa), "+{v[132:147]}"(st.ob), "+{v[92:95]}"(st.kf), "+{v[96:99]}"(st.v0), "+{v[100:103]}"(st.v1),
          "={v[104:107]}"(p0), "+{v[108:111]}"(st.p1), "+{v112}"(st.kaddr), "+{v113}"(st.vaddr), "+{s93}"(soa), "+{s94}"(sob),
          "=&{v[152:167]}"(pf.a0), "=&{v[168:183]}"(pf.a1), "=&{v[184:199]}"(pf.b0), "=&{v[200:215]}"(pf.b1)
        : "{v[56:71]}"(st.negma), "{v[116:131]}"(st.negmb), "{v[88:91]}"(st.qa), "{v[148:151]}"(st.qb), "{v114}"(st.vstride), "{v115}"(voffw), [rs] "s"(rs)
        : "memory", "scc");
    } else {
    asm volatile(
        "buffer_load_dwordx4 v[152:155], v115, %[rs], s93 offen offset:0 sc1\n\t" "buffer_load_dwordx4 v[216:219], %[vl], %[rs], s95 offen offset:0 sc1\n\t" A_STEP(1024, 512, 1024)
        "buffer_load_dwordx4 v[156:159], v115, %[rs], s93 offen offset:1024 sc1\n\t" "buffer_load_dwordx4 v[220:223], %[vl], %[rs], s95 offen offset:2048 sc1\n\t" "s_add_u32 s95, s95, 4096\n\t" A_STEP(2048, 1536, 2048)
        "buffer_load_dwordx4 v[160:163], v115, %[rs], s93 offen offset:2048 sc1\n\t" "buffer_load_dwordx4 v[224:227], %[vl], %[rs], s95 offen offset:0 sc1\n\t" A_STEP(3072, 2560, 3072)
        "buffer_load_dwordx4 v[164:167], v115, %[rs], s93 offen offset:3072 sc1\n\t" "s_add_u32 s93, s93, 4096\n\t" "buffer_load_dwordx4 v[228:231], %[vl], %[rs], s95 offen offset:2048 sc1\n\t" "s_add_u32 s95, s95, 4096\n\t" A_STEP(4096, 3584, 4096)
        "v_add_u32 v112, 4096, v112\n\t" "v_add_u32 v113, v114, v113\n\t"
        "buffer_load_dwordx4 v[168:171], v115, %[rs], s93 offen offset:0 sc1\n\t" "buffer_load_dwordx4 v[232:235], %[vl], %[rs], s95 offen offset:0 sc1\n\t" A_STEP(1024, 512, 1024)
        "buffer_load_dwordx4 v[172:175], v115, %[rs], s93 offen offset:1024 sc1\n\t" "buffer_load_dwordx4 v[236:239], %[vl], %[rs], s95 offen offset:2048 sc1\n\t" "s_add_u32 s95, s95, 4096\n\t" A_STEP(2048, 1536, 2048)
        "buffer_load_dwordx4 v[176:179], v115, %[rs], s93 offen offset:2048 sc1\n\t" "buffer_load_dwordx4 v[240:243], %[vl], %[rs], s95 offen offset:0 sc1\n\t" A_STEP(3072, 2560, 3072)
        "buffer_load_dwordx4 v[180:183], v115, %[rs], s93 offen offset:3072 sc1\n\t" "s_add_u32 s93, s93, 4096\n\t" "buffer_load_dwordx4 v[244:247], %[vl], %[rs], s95 offen offset:2048 sc1\n\t" "s_add_u32 s95, s95, 4096\n\t" A_STEP(4096, 3584, 4096)
        "v_add_u32 v112, 4096, v112\n\t" "v_add_u32 v113, v114, v113\n\t"
        "buffer_load_dwordx4 v[184:187], v115, %[rs], s94 offen offset:0 sc1\n\t" A_STEP(1024, 512, 1024)
        "buffer_load_dwordx4 v[188:191], v115, %[rs], s94 offen offset:1024 sc1\n\t" A_STEP(2048, 1536, 2048)
        "buffer_load_dwordx4 v[192:195], v115, %[rs], s94 offen offset:2048 sc1\n\t" A_STEP(3072, 2560, 3072)
        "buffer_load_dwordx4 v[196:199], v115, %[rs], s94 offen offset:3072 sc1\n\t" "s_add_u32 s94, s94, 4096\n\t" A_STEP(4096, 3584, 4096)
        "v_add_u32 v112, 4096, v112\n\t" "v_add_u32 v113, v114, v113\n\t"
        "buffer_load_dwordx4 v[200:203], v115, %[rs], s94 offen offset:0 sc1\n\t" A_STEP(1024, 512, 1024)
        "buffer_load_dwordx4 v[204:207], v115, %[rs], s94 offen offset:1024 sc1\n\t" A_STEP(2048, 1536, 2048)
        "buffer_load_dwordx4 v[208:211], v115, %[rs], s94 offen offset:2048 sc1\n\t" A_STEP(3072, 2560, 3072)
        "buffer_load_dwordx4 v[212:215], v115, %[rs], s94 offen offset:3072 sc1\n\t" "s_add_u32 s94, s94, 4096\n\t" A_STEP(4096, 3584, 4096)
        "v_add_u32 v112, 4096, v112\n\t" "v_add_u32 v113, v114, v113\n\t"
        "s_waitcnt lgkmcnt(0)\n\t"
        A_MPV(A_OB, A_V1, A_P1)
        "s_nop 15\n\t"
        : "+{v[24:39]}"(st.sa), "+{v[40:55]}"(st.sb), "+{v[72:87]}"(st.oa), "+{v[132:147]}"(st.ob), "+{v[92:95]}"(st.kf), "+{v[96:99]}"(st.v0), "+{v[100:103]}"(st.v1),
          "={v[104:107]}"(p0), "+{v[108:111]}"(st.p1), "+{v112}"(st.kaddr), "+{v113}"(st.vaddr), "+{s93}"(soa), "+{s94}"(sob), "+{s95}"(sow),
          "=&{v[152:167]}"(pf.a0), "=&{v[168:183]}"(pf.a1), "=&{v[184:199]}"(pf.b0), "=&{v[200:215]}"(pf.b1), "=&{v[216:231]}"(pf.w0), "=&{v[232:247]}"(pf.w1)
        : "{v[56:71]}"(st.negma), "{v[116:131]}"(st.negmb), "{v[88:91]}"(st.qa), "{v[148:151]}"(st.qb), "{v114}"(st.vstride), "{v115}"(voffw), [rs] "s"(rs), [vl] "v"(voffl)
        : "memory", "scc");
    }
}
template <int I> __device__ __forceinline__ u32x4 quad(const u32x16& v) { return (u32x4){v[4 * I], v[4 * I + 1], v[4 * I + 2], v[4 * I + 3]}; }

__device__ __forceinline__ void stats_acc(u32x4 f, float& s1, float& s2) {
#pragma unroll
    for (int d = 0; d < 4; ++d) { s1 = dot2(f[d], 0x3f803f80u, s1); s2 = dot2(f[d], f[d], s2); }
}
template <int N>
__device__ __forceinline__ void stat_sums(LAS char* lds, int slot0, int tok, float& t1, float& t2) {
    f32x2 v[N];
#pragma unroll
    for (int i = 0; i < N; ++i) v[i] = *(LAS f32x2*)(lds + L_STAT + ((slot0 + i) * 32 + tok) * 8);
    SBAR();
    t1 = 0.f; t2 = 0.f;
#pragma unroll
    for (int i = 0; i < N; ++i) { t1 += v[i][0]; t2 += v[i][1]; }
}
__device__ __forceinline__ void ln_from_sums(float t1, float t2, float& mu, float& rstd) {
    mu = t1 * (1.f / D);
    rstd = __builtin_amdgcn_rsqf(fmaxf(t2 * (1.f / D) - mu * mu, 0.f) + LN_EPS);
}

template <int TB>
__device__ __forceinline__ void run_layers(unsigned char* ws0, LAS char* lds) {
    const unsigned lbase = (unsigned)(uintptr_t)lds;
    const int tid0 = threadIdx.x, w0 = __builtin_amdgcn_readfirstlane(tid0 >> 6);
    const int kk0 = blockIdx.x >> 3, c0 = (blockIdx.x & 7) * 4 + (kk0 >> 3), j0 = kk0 & 7;
    typedef const __attribute__((address_space(4))) Params* kargs_t;
#define PHASE_IDS() \
    int tid = tid0, w = w0, c = c0, j = j0; unsigned epoch = epoch0; unsigned char* ws = ws0; \
    kargs_t kp = (kargs_t)__builtin_amdgcn_kernarg_segment_ptr();        \
    asm volatile("" : "+v"(tid)); asm volatile("" : "+s"(w), "+s"(c), "+s"(j), "+s"(epoch), "+s"(ws), "+s"(kp)); \
    const rsrc_t rw = __builtin_amdgcn_make_buffer_rsrc(ws, 0, WS_TOTAL, 0x00020000); \
    gu32* cnt = (gu32*)(ws + OFF_CNT); gu32* sync = (gu32*)(ws + OFF_SYNC); gu32* tmo = sync + 2 * CW; \
    (void)rw; (void)cnt; (void)tmo; (void)kp; \
    constexpr int tb = TB; const int lane = tid & 63, r = lane & 31, h = lane >> 5, kq = w & 3, tbg = 2 * c + tb; \
    const int vl = lane * 16;                                     \
    const int vw = (lane ^ ((kq & 1) << 4)) * 16;                 \
    const int cbA = 2 * j + (kq >> 1), cbB = 2 * j + ((kq >> 1) ^ 1); \
    const int sown = 4 * j + kq;                                  \
    (void)epoch; (void)r; (void)h; (void)tbg; (void)vl; (void)vw; (void)cbA; (void)cbB; (void)sown; (void)tb;
#define W1_IDS() \
    const int ncb = j < 3 ? 6 : 5, cb0 = j < 3 ? 6 * j : 18 + 5 * (j - 3);     \
    const int cb1 = TB == 0 ? kq : 4 + (kq >> 1);     \
    const bool d_active = cb1 < ncb; \
    const int cbg1 = cb0 + (d_active ? cb1 : 0); (void)cbg1; \
    const int par = TB == 0 ? 0 : (kq & 1); (void)par;

    STAMP(0);
    unsigned epoch0;
    { gu32* e = (gu32*)(ws0 + OFF_SYNC); asm volatile("" : "+s"(e)); epoch0 = __builtin_amdgcn_readfirstlane(__hip_atomic_load(e, RLX_AGENT)); }
    asm volatile("" : "+s"(epoch0));
    {
        PHASE_IDS();
        if (tid < 16) *(LAS unsigned*)(lds + (tid < 8 ? F_PAIR + tid * 4 : F_OCNT)) = 0u;
        if (j == tb && kq == 0 && lane < 4) __hip_atomic_store(cnt + cnt_word(j, lane, c), 0u, RLX_AGENT);
        if (c == 0 && j == 0 && tid >= 4 && tid < 6) __hip_atomic_store(sync + (tid - 3) * CW, 0u, RLX_AGENT);
    }

#pragma unroll 1
    for (int L = 0; L < 2; ++L) {
        u32x4 qown; float q2own;
        {
            PHASE_IDS();
            const bool kvm = j < 2;
            const bool kvteam = j == tb;
            if (kvm && kvteam) {
                __builtin_amdgcn_s_setprio(2);
                const uint16_t* src = ((lane & 2) ? kp->wv : kp->wk) + (128 * kq + (lane >> 2)) * 16 + (lane & 1) * 8;
#pragma unroll
                for (int i = 0; i < 8; ++i) glds16(src + i * 256, lbase + L_WKV + (kq * 8 + i) * 1024);
            } else {
                const int row = lane >> 3, pc = lane & 7;
                const uint16_t* src = kp->wq + (size_t)(64 * w + row) * D + 64 * j + (((pc >> 2) ^ ((row >> 1) & 1)) * 32 + (pc & 3) * 8);
                if (L == 0) {
#pragma unroll
                    for (int i = 0; i < 4; ++i) glds16(src + i * 8 * D, lbase + L_WQ + (w * 8 + i) * 1024);
                }
#pragma unroll
                for (int i = 4; i < 8; ++i) glds16(src + i * 8 * D, lbase + L_WQ + (w * 8 + i) * 1024);
                if (kvm) {
                    const int w2 = w ^ 4;
                    const uint16_t* src2 = kp->wq + (size_t)(64 * w2 + row) * D + 64 * j + (((pc >> 2) ^ ((row >> 1) & 1)) * 32 + (pc & 3) * 8);
                    if (L == 0) {
#pragma unroll
                        for (int i = 0; i < 4; ++i) glds16(src2 + i * 8 * D, lbase + L_WQ + (w2 * 8 + i) * 1024);
                    }
#pragma unroll
                    for (int i = 4; i < 8; ++i) glds16(src2 + i * 8 * D, lbase + L_WQ + (w2 * 8 + i) * 1024);
                }
            }
            auto prep_dst = [&](int i) -> int {
                const int u = c * 6 + (j - 2);
                if (u < 43) return OFF_W1 + (u * KSD + 4 * w + i) * 1024;
                const int f = ((u - 43) * 8 + w) + i * 1192;
                if (i >= 2 || f >= 1920) return -1;
                return f < 512 ? OFF_WO + f * 1024 : OFF_W2 + (f - 512) * 1024;
            };
            u32x4 xa[8] = {};
            if (L == 0) {
                const rsrc_t rx = __builtin_amdgcn_make_buffer_rsrc((void*)kp->x, 0, S * D * 2, 0x00020000);
#pragma unroll
                for (int i = 0; i < 8; ++i) xa[i] = ld_plain(rx, r * 1024 + h * 16, (64 * c + 32 * tb) * 1024 + (kq * 8 + i) * 32);
                SBAR();
                STAMP(2);
                if (!kvm || kvteam) WAITVM(8);
            } else {
                if (tid == (kvm ? 256 * j : 0)) poll_ge(cnt + cnt_word(0, CNT_X2, c), 8u, tmo);
                __builtin_amdgcn_fence(__ATOMIC_ACQUIRE, "wavefront");
                if (!kvm || kvteam) WAITVM(0);
            }
            LDS_BARRIER();
            STAMP(1 + L * 32);
            if (L == 1) {
#pragma unroll
                for (int i = 0; i < 8; ++i) xa[i] = ld_sc1(rw, vl, OFF_X2 + (tbg * KSD + kq * 8 + i) * 1024);
                SBAR();
            }
            LAS f32x2* st = (LAS f32x2*)(lds + L_STAT);
            LAS float* cst = (LAS float*)(lds + L_CS);
            const int tq = (lane & 15) >> 2, tp = lane & 3, tg = (lane >> 4) & 1;
            f32x16 aA = {}, aB = {};
            auto store_stats = [&]() {
                float s1 = 0.f, s2 = 0.f;
#pragma unroll
                for (int i = 0; i < 8; ++i) stats_acc(xa[i], s1, s2);
                s1 = half_sum(s1); s2 = half_sum(s2);
                if (h == 0) st[(tb * 8 + kq) * 32 + r] = (f32x2){s1, s2};
            };
            auto do_q = [&]() {
                u32x4 fa[8], fb[8];
                float csA = 0.f, csB = 0.f;
                const int sw = (tq >> 1) & 1, col = (tg ^ (kq & 1)) * 32 + tp * 8;
                const int a0 = L_WQ + (kq * 128 + 8 * h + tq) * 128 + col;
                const int aa = a0 + (((kq >> 1) ^ sw) * 64), ab = a0 + (((kq >> 1) ^ 1 ^ sw) * 64);
#pragma unroll
                for (int i = 0; i < 8; ++i) { fa[i] = tr_frag(lds, aa + i * 2048, 512); fb[i] = tr_frag(lds, ab + i * 2048, 512); }
                SBAR();
#pragma unroll
                for (int i = 0; i < 8; ++i) { csA = frag_sum(fa[i], csA); csB = frag_sum(fb[i], csB); }
                csA = half_sum(csA); csB = half_sum(csB);
                if (TB == 0 && h == 0) { const int n = r ^ ((kq & 1) << 4); cst[kq * 96 + (kq >> 1) * 32 + n] = csA; cst[kq * 96 + ((kq >> 1) ^ 1) * 32 + n] = csB; }
                SBAR();
#pragma unroll
                for (int i = 0; i < 8; ++i) { aA = mfma(fa[i], xa[i], aA); aB = mfma(fb[i], xa[i], aB); }
                PIN2(aA, aB);
            };
            if (kvm) {
                if (kvteam) {
                    u32x4 wk[8];
                    const int a = L_WKV + (kq * 128 + 8 * h + tq) * 64 + tg * 32 + tp * 8;
#pragma unroll
                    for (int i = 0; i < 8; ++i) wk[i] = tr_frag(lds, a + i * 1024, 256);
                    SBAR();
                    float cs = 0.f;
#pragma unroll
                    for (int i = 0; i < 8; ++i) cs = frag_sum(wk[i], cs);
                    cs = half_sum(cs);
                    if (h == 0) cst[kq * 96 + 64 + r] = cs;
                    SBAR();
                    f32x16 ak = {}, av = {};
#pragma unroll
                    for (int i = 0; i < 8; ++i) { ak = mfma(wk[i], xa[i], ak); av = mfma(xa[i], wk[i], av); }
                    store_stats();
                    if (kq == 3 && lane == 0) *(LAS unsigned*)(lds + F_KVCNT) = 0u;
                    const int o = L_KVRED + kq * 4096;
                    lds_stf(lds, o + vl, (f32x4){ak[0], ak[1], ak[2], ak[3]}); lds_stf(lds, o + 1024 + vl, (f32x4){ak[4], ak[5], ak[6], ak[7]});
                    if (r >= 16) {
#pragma unroll
                        for (int g = 0; g < 4; ++g) lds_stf(lds, o + 2048 + g * 512 + (h * 16 + r - 16) * 16, (f32x4){av[4 * g], av[4 * g + 1], av[4 * g + 2], av[4 * g + 3]});
                    }
                } else WAITVM(8);
                LDS_BARRIER();
                if (kvteam && kq < 3) {
                    f32x4 a = {}, e = {}, ta[4], te[4];
                    float t1, t2, mu, rstd;
                    if (kq == 0) {
                        f32x4 k0 = {}, k1 = {}, u0[4], u1[4];
#pragma unroll
                        for (int i = 0; i < 4; ++i) { ta[i] = lds_ldf(lds, L_KVRED + i * 4096 + vl); te[i] = lds_ldf(lds, L_KVRED + i * 4096 + 1024 + vl);
                                                      u0[i] = lds_ldf(lds, L_CS + (i * 96 + 64 + 4 * h) * 4); u1[i] = lds_ldf(lds, L_CS + (i * 96 + 64 + 8 + 4 * h) * 4); }
                        stat_sums<4>(lds, tb * 8, r, t1, t2); ln_from_sums(t1, t2, mu, rstd);
#pragma unroll
                        for (int i = 0; i < 4; ++i) { a += ta[i]; e += te[i]; k0 += u0[i]; k1 += u1[i]; }
#pragma unroll
                        for (int i = 0; i < 4; ++i) { a[i] = (a[i] - mu * k0[i]) * rstd; e[i] = (e[i] - mu * k1[i]) * rstd; }
                        st_sc1((u32x4){pk(a[0], a[1]), pk(a[2], a[3]), pk(e[0], e[1]), pk(e[2], e[3])}, rw, vl, OFF_KF + L * 65536 + tbg * 1024);
                        float n2 = (a[0] * a[0] + a[1] * a[1]) + (a[2] * a[2] + a[3] * a[3]) + (e[0] * e[0] + e[1] * e[1]) + (e[2] * e[2] + e[3] * e[3]);
                        n2 = wave_max(half_sum(n2));
                        if (lane == 0) *(LAS float*)(lds + F_N2) = n2;
                    } else {
                        const int g = kq - 1, lc = (h * 16 + (r & 15)) * 16;
                        float csv = 0.f, uc[4];
#pragma unroll
                        for (int i = 0; i < 4; ++i) { ta[i] = lds_ldf(lds, L_KVRED + i * 4096 + 2048 + (2 * g) * 512 + lc); te[i] = lds_ldf(lds, L_KVRED + i * 4096 + 2048 + (2 * g + 1) * 512 + lc);
                                                      uc[i] = cst[i * 96 + 64 + 16 + (r & 15)]; }
                        stat_sums<4>(lds, tb * 8, r, t1, t2); ln_from_sums(t1, t2, mu, rstd);
#pragma unroll
                        for (int i = 0; i < 4; ++i) { a += ta[i]; e += te[i]; csv += uc[i]; }
#pragma unroll
                        for (int i = 0; i < 8; ++i) {
                            const int tok = 16 * g + (i & 3) + 8 * (i >> 2) + 4 * h;
                            const float m2 = __uint_as_float(__builtin_amdgcn_ds_bpermute(tok * 4, __float_as_uint(mu))), r2 = __uint_as_float(__builtin_amdgcn_ds_bpermute(tok * 4, __float_as_uint(rstd)));
                            if (i < 4) a[i] = (a[i] - m2 * csv) * r2; else e[i - 4] = (e[i - 4] - m2 * csv) * r2;
                        }
                        if (r >= 16) st_sc1((u32x4){pk(a[0], a[1]), pk(a[2], a[3]), pk(e[0], e[1]), pk(e[2], e[3])}, rw, h * 256 + (r - 16) * 16, OFF_VF + L * 65536 + (2 * tbg + g) * 512);
                    }
                }
                if (kvteam && kq < 3) {
                    DRAIN();
                    unsigned old = 0;
                    if (lane == 0) old = __hip_atomic_fetch_add((LAS unsigned*)(lds + F_KVCNT), 1u, __ATOMIC_RELAXED, __HIP_MEMORY_SCOPE_WORKGROUP);
                    if (__builtin_amdgcn_readfirstlane(old) == 2u && lane < 32)
                        __hip_atomic_store((gu64*)(ws + OFF_KVFL) + (L * 32 + lane) * 64 + 2 * c + j,
                                           ((unsigned long long)(epoch + 1u + L) << 32) | __float_as_uint(*(volatile LAS float*)(lds + F_N2)), RLX_AGENT);
                }
                __builtin_amdgcn_s_setprio(0);
                STAMP(3 + L * 32);
                do_q();
                if (!kvteam) store_stats();
            } else {
                do_q(); store_stats();
                STAMP(3 + L * 32);
            }
            if (L == 0 && !kvm) {
#pragma unroll
                for (int i = 0; i < 4; ++i) {
                    const int d = prep_dst(i);
                    if (d < 0) continue;
                    const uint16_t* src; int ld, cb, s;
                    if (d >= OFF_W2) { const int g = (d - OFF_W2) >> 10; src = kp->w2; ld = D; cb = g / KSFP; s = min(g % KSFP, KSF - 1); }
                    else if (d >= OFF_W1) { const int g = (d - OFF_W1) >> 10; src = kp->w1; ld = F; cb = g >> 5; s = g & 31; }
                    else { const int g = (d - OFF_WO) >> 10; src = kp->wo; ld = D; cb = g >> 5; s = g & 31; }
                    glds16(src + (size_t)(16 * s + (lane >> 2)) * ld + 32 * cb + (lane & 3) * 8, lbase + L_PREP + (w * 4 + i) * 1024);
                }
            }
            STAMP(4 + L * 32);
            float q[8];
            reduce4(lds, kvm ? (kvteam ? 0 : 131072) : (TB == 0 ? 32768 : 131072), aA, aB, kq, lane, q);
            STAMP(5 + L * 32);
            float t1, t2;
            f32x4 cs0 = {}, cs1 = {}, u0[4], u1[4];
#pragma unroll
            for (int i = 0; i < 4; ++i) { u0[i] = lds_ldf(lds, L_CS + (i * 96 + 16 * kq + 4 * h) * 4); u1[i] = lds_ldf(lds, L_CS + (i * 96 + 16 * kq + 8 + 4 * h) * 4); }
            stat_sums<4>(lds, tb * 8, r, t1, t2);
#pragma unroll
            for (int i = 0; i < 4; ++i) { cs0 += u0[i]; cs1 += u1[i]; }
            float mu, rstd; ln_from_sums(t1, t2, mu, rstd);
            float n2 = 0.f;
#pragma unroll
            for (int i = 0; i < 4; ++i) { q[i] = (q[i] - mu * cs0[i]) * (rstd * QSCALE); q[4 + i] = (q[4 + i] - mu * cs1[i]) * (rstd * QSCALE); }
#pragma unroll
            for (int i = 0; i < 8; ++i) n2 += q[i] * q[i];
            q2own = half_sum(n2);
            qown = (u32x4){pk(q[0], q[1]), pk(q[2], q[3]), pk(q[4], q[5]), pk(q[6], q[7])};
            lds_st(lds, L_XCHQ + w * 1024 + vl, qown);
            *(LAS float*)(lds + L_STAT + (tb * 8 + 4 + kq) * 256 + lane * 4) = q2own;
            if (L == 0 && !kvm) {
                WAITVM(0);
                float cs = 0.f;
#pragma unroll
                for (int i = 0; i < 4; ++i) {
                    const int d = prep_dst(i);
                    if (d < 0) continue;
                    u32x4 fr = tr_frag(lds, L_PREP + (w * 4 + i) * 1024 + (8 * h + tq) * 64 + tg * 32 + tp * 8, 256);
                    if (d >= OFF_W2 && ((d - OFF_W2) >> 10) % KSFP >= KSF) fr = (u32x4){0u, 0u, 0u, 0u};
                    st_sc1(fr, rw, vl, d);
                    cs = frag_sum(fr, cs);
                }
                const int u = c * 6 + (j - 2);
                if (u < 43) {
                    cs = half_sum(cs);
                    LAS float* t = (LAS float*)(lds + L_WQ);
                    if (h == 0) t[w * 32 + r] = cs;
                    LDS_BARRIER();
                    if (tid < 32) { float v = 0.f, pv[8];
#pragma unroll
                        for (int i = 0; i < 8; ++i) pv[i] = t[i * 32 + tid];
                        SBAR();
#pragma unroll
                        for (int i = 0; i < 8; ++i) v += pv[i];
                        __hip_atomic_store((gu32*)(ws + OFF_CS1 + (32 * u + tid) * 4), __float_as_uint(v), RLX_AGENT); }
                }
                DRAIN();
            }
            STAMP(6 + L * 32);
        }
        u32x4 woA[8], woB[8];
        u32x4 w1f[32];
        {
            PHASE_IDS();
            const float km2 = wait_flags(tid, lds, (gu64*)(ws + OFF_KVFL) + (L * 32 + c) * 64, epoch + 1u + L, tmo);
            if (L == 0 && j >= 2 && tid == 0) __hip_atomic_fetch_add(sync + CW, 1u, RLX_AGENT);
            STAMP(7 + L * 32);
            {
                const unsigned char* src = ws + (TB == 0 ? OFF_KF : OFF_VF) + L * 65536 + ((w >> 1) & 1) * 32768 + vl;
                const unsigned dst = lbase + (TB == 0 ? L_K : L_V) + ((w >> 1) & 1) * 32768;
                const int t0 = (w & 1) * 6;
#pragma unroll
                for (int i = 0; i < 6; ++i) glds16_sc1(src + (t0 + i) * 1024, dst + (t0 + i) * 1024);
            }
            { unsigned one2 = 0x3f803f80u; asm volatile("" : "+v"(one2)); if (tid < 272) lds_st(lds, L_ONES + tid * 16, (u32x4){one2, one2, one2, one2}); }
            const u32x4 qpar = lds_ld(lds, L_XCHQ + (w ^ 1) * 1024 + vl);
            const float q2par = *(LAS float*)(lds + L_STAT + (tb * 8 + 4 + (kq ^ 1)) * 256 + lane * 4);
            float ma = __builtin_amdgcn_sqrtf(q2own * km2) * 1.02f, mb = __builtin_amdgcn_sqrtf(q2par * km2) * 1.02f;
            const bool unsafe = __any(!(ma <= SAFE_BOUND) || !(mb <= SAFE_BOUND));
            {
                LAS unsigned* fl = (LAS unsigned*)(lds + F_UNSAFE);
                if (lane == 0) fl[w] = unsafe ? 1u : 0u;
            }
            STAMP(8 + L * 32);
            WAITVM(0);
            LDS_BARRIER();
            {
                LAS unsigned* fl = (LAS unsigned*)(lds + F_UNSAFE);
                const u32x4 f0 = *(LAS u32x4*)fl, f1 = *(LAS u32x4*)(fl + 4);
                if (__builtin_amdgcn_readfirstlane(f0[0] | f0[1] | f0[2] | f0[3] | f1[0] | f1[1] | f1[2] | f1[3])) {
                    {
                        const unsigned char* src = ws + (TB == 0 ? OFF_KF : OFF_VF) + L * 65536 + ((w >> 1) & 1) * 32768 + (12 + (w & 1) * 10) * 1024 + vl;
                        const unsigned dst = lbase + (TB == 0 ? L_K : L_V) + ((w >> 1) & 1) * 32768 + (12 + (w & 1) * 10) * 1024;
#pragma unroll 1
                        for (int i = 0; i < 10; ++i) glds16_sc1(src + i * 1024, __builtin_amdgcn_readfirstlane(dst + i * 1024));
                    }
                    DRAIN();
                    LDS_BARRIER();
                    if (unsafe) {
                        float xa = -INFINITY, xb = -INFINITY;
#pragma unroll 1
                        for (int t = 0; t < 64; ++t) {
                            const u32x4 kf = lds_ld(lds, L_K + t * 1024 + vl);
                            const f32x16 sa = mfma(kf, qown, (f32x16){}), sb = mfma(kf, qpar, (f32x16){});
#pragma unroll
                            for (int i = 0; i < 16; ++i) { xa = fmaxf(xa, sa[i]); xb = fmaxf(xb, sb[i]); }
                        }
                        ma = half_max(xa); mb = half_max(xb);
                    }
                }
            }
            STAMP(9 + L * 32);
            const int kh = kq & 1;
            const bool real = r < 16;
            const unsigned kaddr = L_K + kh * 32768 + vl;
            const unsigned vaddr = real ? (unsigned)(L_V + kh * 32768 + h * 256 + r * 16) : (unsigned)(L_ONES + ((r >= 20 && r < 28) ? 64 : 0));
            AttnState st;
            attn_begin(st, lds, qown, qpar, ma, mb, kaddr, vaddr, real ? 4096u : 0u);
            attn_first(st, (unsigned long long)(uintptr_t)(ws + (TB == 0 ? OFF_KF : OFF_VF) + L * 65536 + ((w >> 1) & 1) * 32768 + (12 + (w & 1) * 10) * 1024),
                       lbase + (TB == 0 ? L_K : L_V) + ((w >> 1) & 1) * 32768 + (12 + (w & 1) * 10) * 1024, (unsigned)vl);
            STAMP(10 + L * 32);
            if (L == 0) { if (lane == 0) poll_ge(sync + CW, PREP_WGS, tmo); __builtin_amdgcn_fence(__ATOMIC_ACQUIRE, "wavefront"); }
            STAMP(11 + L * 32);
            Prefetch pf;
            { W1_IDS();
              const unsigned long long wsa = (unsigned long long)(uintptr_t)ws;
              const u32x4 rs = {(unsigned)wsa, (unsigned)(wsa >> 32) & 0xffffu, (unsigned)WS_TOTAL, 0x00020000u};
              attn_second<TB>(st, pf, rs, (unsigned)vw, (unsigned)vl, OFF_WO + (cbA * KSD + kq * 8) * 1024, OFF_WO + (cbB * KSD + kq * 8) * 1024, OFF_W1 + (cbg1 * KSD + par) * 1024); }
            STAMP(12 + L * 32);
            const f32x16 oa = st.oa, ob = st.ob;
            const int xo = L_XCHO + w * 2304;
            lds_stf(lds, xo + vl, (f32x4){ob[0], ob[1], ob[2], ob[3]}); lds_stf(lds, xo + 1024 + vl, (f32x4){ob[4], ob[5], ob[6], ob[7]});
            *(LAS float*)(lds + xo + 2048 + lane * 4) = ob[8];
            asm volatile("s_waitcnt lgkmcnt(0)" ::: "memory");
            *(volatile LAS unsigned*)(lds + F_PAIR + w * 4) = L + 1u;
            while (*(volatile LAS unsigned*)(lds + F_PAIR + (w ^ 1) * 4) != L + 1u) { }
            asm volatile("" ::: "memory");
            const int xi = L_XCHO + (w ^ 1) * 2304;
            const f32x4 e0 = lds_ldf(lds, xi + vl), e1 = lds_ldf(lds, xi + 1024 + vl);
            const float lpar = *(LAS float*)(lds + xi + 2048 + lane * 4);
            SBAR();
            const float rl = __builtin_amdgcn_rcpf(oa[8] + lpar);
            const u32x4 of = {pk((oa[0] + e0[0]) * rl, (oa[1] + e0[1]) * rl), pk((oa[2] + e0[2]) * rl, (oa[3] + e0[3]) * rl),
                              pk((oa[4] + e1[0]) * rl, (oa[5] + e1[1]) * rl), pk((oa[6] + e1[2]) * rl, (oa[7] + e1[3]) * rl)};
            st_sc1(swap_order(of), rw, vl, OFF_O + L * ACT_BYTES + (tbg * KSD + sown) * 1024);
            if (TB == 0) asm volatile("s_waitcnt vmcnt(0)" : "+{v[152:167]}"(pf.a0), "+{v[168:183]}"(pf.a1), "+{v[184:199]}"(pf.b0), "+{v[200:215]}"(pf.b1) :: "memory");
            else asm volatile("s_waitcnt vmcnt(0)" : "+{v[152:167]}"(pf.a0), "+{v[168:183]}"(pf.a1), "+{v[184:199]}"(pf.b0), "+{v[200:215]}"(pf.b1), "+{v[216:231]}"(pf.w0), "+{v[232:247]}"(pf.w1) :: "memory");
            unsigned old = 0;
            if (lane == 0) old = __hip_atomic_fetch_add((LAS unsigned*)(lds + F_OCNT), 1u, __ATOMIC_RELAXED, __HIP_MEMORY_SCOPE_WORKGROUP);
            if (__builtin_amdgcn_readfirstlane(old) == 8u * L + 7u && lane == 0) __hip_atomic_fetch_add(cnt + cnt_word(L, CNT_O, c), 1u, RLX_AGENT);
            STAMP(13 + L * 32);
            woA[0] = quad<0>(pf.a0); woA[1] = quad<1>(pf.a0); woA[2] = quad<2>(pf.a0); woA[3] = quad<3>(pf.a0);
            woA[4] = quad<0>(pf.a1); woA[5] = quad<1>(pf.a1); woA[6] = quad<2>(pf.a1); woA[7] = quad<3>(pf.a1);
            woB[0] = quad<0>(pf.b0); woB[1] = quad<1>(pf.b0); woB[2] = quad<2>(pf.b0); woB[3] = quad<3>(pf.b0);
            woB[4] = quad<0>(pf.b1); woB[5] = quad<1>(pf.b1); woB[6] = quad<2>(pf.b1); woB[7] = quad<3>(pf.b1);
            if (TB == 1) {
                w1f[0] = quad<0>(pf.w0); w1f[1] = quad<1>(pf.w0); w1f[2] = quad<2>(pf.w0); w1f[3] = quad<3>(pf.w0);
                w1f[4] = quad<0>(pf.w1); w1f[5] = quad<1>(pf.w1); w1f[6] = quad<2>(pf.w1); w1f[7] = quad<3>(pf.w1);
            }
            if (TB == 0) { W1_IDS();
#pragma unroll
                for (int i = 0; i < W1_EARLY; ++i) { w1f[i] = ld_sc1(rw, vl, OFF_W1 + (cbg1 * KSD + i) * 1024); SBAR(); __builtin_amdgcn_s_sleep(3); SBAR(); }
            }
        }
        u32x4 resid;
        {
            PHASE_IDS();
            if (L == 0) { const rsrc_t rx = __builtin_amdgcn_make_buffer_rsrc((void*)kp->x, 0, S * D * 2, 0x00020000);
                          resid = ld_plain(rx, r * 1024 + h * 16, (64 * c + 32 * tb) * 1024 + sown * 32); }
            else resid = ld_sc1(rw, vl, OFF_X2 + (tbg * KSD + sown) * 1024);
            STAMP(14 + L * 32);
            wait_cnt(tid, cnt + cnt_word(L, CNT_O, c), 8u, tmo);
            STAMP(15 + L * 32);
            u32x4 xa[8];
#pragma unroll
            for (int i = 0; i < 8; ++i) xa[i] = ld_sc1(rw, vl, OFF_O + L * ACT_BYTES + (tbg * KSD + kq * 8 + i) * 1024);
            SBAR();
            f32x16 aA = {}, aB = {};
#pragma unroll
            for (int i = 0; i < 8; ++i) { aA = mfma(woA[i], xa[i], aA); aB = mfma(woB[i], xa[i], aB); }
            PIN2(aA, aB);
            SBAR();
            STAMP(16 + L * 32);
            {
                const unsigned char* src = ws + OFF_W2 + ((2 * j + (w & 1)) * KSFP + (w >> 1) * 22 + W2_REG) * 1024 + vl;
#pragma unroll
                for (int i = 0; i < 6; ++i) glds16_sc1(src + i * 1024, lbase + L_W2A + (w * W2_A + i) * 1024);
            }
            STAMP(17 + L * 32);
            float o[8];
            reduce4(lds, tb * 24576, aA, aB, kq, lane, o);
            resid = swap_order(resid);
#pragma unroll
            for (int i = 0; i < 4; ++i) resid[i] = pk(bflo(resid[i]) + o[2 * i], bfhi(resid[i]) + o[2 * i + 1]);
            st_sc1(swap_order(resid), rw, vl, OFF_X1 + L * ACT_BYTES + (tbg * KSD + sown) * 1024);
            STAMP(18 + L * 32);
            publish(tid, cnt + cnt_word(L, CNT_X1, c));
            STAMP(19 + L * 32);
        }
        u32x4 w2A[W2_REG], w2B[W2_REG];
        {
            PHASE_IDS(); W1_IDS();
            if (w == 1 || w == 2 || w == 3) {
                const int col = min(32 * cb0 + tid - 64, F - 1);
                *(LAS float*)(lds + L_TAB + (tid - 64) * 4) = __uint_as_float(__builtin_amdgcn_raw_buffer_load_b32(rw, col * 4, OFF_CS1, AUX_SC1));
                *(LAS float*)(lds + L_TAB + 768 + (tid - 64) * 4) = bf2f(kp->b1[col]);
            }
            STAMP(20 + L * 32);
            wait_cnt(tid, cnt + cnt_word(L, CNT_X1, c), 8u, tmo);
            STAMP(21 + L * 32);
            const unsigned char* xsrc = ws + OFF_X1 + L * ACT_BYTES + (2 * c * KSD + 2 * w) * 1024 + vl;
#pragma unroll
            for (int i = 0; i < 4; ++i) glds16_sc1(xsrc + ((i >> 1) * KSD + (i & 1)) * 1024, lbase + L_ACT + ((i >> 1) * KSD + 2 * w + (i & 1)) * 1024);
            WAITVM(0);
            LDS_BARRIER();
#pragma unroll
            for (int i = 0; i < 4; ++i) glds16_sc1(xsrc + ((i >> 1) * KSD + 16 + (i & 1)) * 1024, lbase + L_ACT + ((i >> 1) * KSD + 16 + 2 * w + (i & 1)) * 1024);
            STAMP(22 + L * 32);
            f32x16 a0 = {}, a1 = {};
            float s1[2] = {0.f, 0.f}, s2[2] = {0.f, 0.f};
            const unsigned char* w2a_src = ws + OFF_W2 + ((2 * j + (w & 1)) * KSFP + (w >> 1) * 22 + W2_REG) * 1024 + vl;
            auto req = [&](int lo, int n) {
                if (lo == 0) {
                    if (TB == 0) { if (n < 32 - W1_EARLY) w1f[W1_EARLY + n] = ld_sc1(rw, vl, OFF_W1 + (cbg1 * KSD + W1_EARLY + n) * 1024); }
                    else if (n < 8) w1f[16 + n] = ld_sc1(rw, vl, OFF_W1 + (cbg1 * KSD + 16 + 2 * n + par) * 1024);
                } else {
                    if (n < 5) glds16_sc1(w2a_src + (6 + n) * 1024, lbase + L_W2A + (w * W2_A + 6 + n) * 1024);
                    else if (n < 5 + W2_REG) w2A[n - 5] = ld_sc1(rw, vw, OFF_W2 + (cbA * KSFP + kq * 22 + n - 5) * 1024);
                    else if (n < 5 + 2 * W2_REG) w2B[n - 5 - W2_REG] = ld_sc1(rw, vw, OFF_W2 + (cbB * KSFP + kq * 22 + n - 5 - W2_REG) * 1024);
                }
            };
            auto part = [&](int lo) {
                u32x4 b0[3], b1[3];
                if (TB == 0) {
#pragma unroll
                    for (int i = 0; i < 2; ++i) { b0[i] = lds_ld(lds, L_ACT + (lo + i) * 1024 + vl); b1[i] = lds_ld(lds, L_ACT + (KSD + lo + i) * 1024 + vl); }
#pragma unroll
                    for (int s = 0; s < 16; ++s) {
                        if (s + 2 < 16) { b0[(s + 2) % 3] = lds_ld(lds, L_ACT + (lo + s + 2) * 1024 + vl); b1[(s + 2) % 3] = lds_ld(lds, L_ACT + (KSD + lo + s + 2) * 1024 + vl); }
                        SBAR();
                        a0 = mfma(w1f[lo + s], b0[s % 3], a0); a1 = mfma(w1f[lo + s], b1[s % 3], a1);
                        PIN2(a0, a1);
                        req(lo, s);
                        SBAR();
                    }
                } else {
                    const int ab = L_ACT + (lo + par) * 1024 + vl;
#pragma unroll
                    for (int i = 0; i < 2; ++i) { b0[i] = lds_ld(lds, ab + 2 * i * 1024); b1[i] = lds_ld(lds, ab + (KSD + 2 * i) * 1024); }
#pragma unroll
                    for (int s = 0; s < 8; ++s) {
                        if (s + 2 < 8) { b0[(s + 2) % 3] = lds_ld(lds, ab + 2 * (s + 2) * 1024); b1[(s + 2) % 3] = lds_ld(lds, ab + (KSD + 2 * (s + 2)) * 1024); }
                        SBAR();
                        a0 = mfma(w1f[lo + s], b0[s % 3], a0); a1 = mfma(w1f[lo + s], b1[s % 3], a1);
                        PIN2(a0, a1);
                        if (lo == 0) req(lo, s); else { req(lo, 2 * s); req(lo, 2 * s + 1); }
                        SBAR();
                    }
                }
            };
            auto stats = [&](int lo) {
                u32x4 v[4];
#pragma unroll
                for (int i = 0; i < 4; ++i) v[i] = lds_ld(lds, L_ACT + ((i >> 1) * KSD + lo + 2 * w + (i & 1)) * 1024 + vl);
                SBAR();
#pragma unroll
                for (int i = 0; i < 4; ++i) stats_acc(v[i], s1[i >> 1], s2[i >> 1]);
            };
            stats(0);
            SBAR();
            part(0);
            STAMP(23 + L * 32);
            WAITVM(8);
            static_assert(32 - W1_EARLY == 8, "count above");
            LDS_BARRIER();
            stats(16);
            {
                LAS f32x2* st = (LAS f32x2*)(lds + L_STAT);
#pragma unroll
                for (int t = 0; t < 2; ++t) { const float u1 = half_sum(s1[t]), u2 = half_sum(s2[t]); if (h == 0) st[(t * 8 + w) * 32 + r] = (f32x2){u1, u2}; }
            }
            SBAR();
            part(16);
            STAMP(24 + L * 32);
            if (TB == 1) {
                const int xo = L_ACT + kq * 4096 + vl;
#pragma unroll
                for (int g = 0; g < 4; ++g) {
                    const f32x4 v = par ? (f32x4){a0[4 * g], a0[4 * g + 1], a0[4 * g + 2], a0[4 * g + 3]} : (f32x4){a1[4 * g], a1[4 * g + 1], a1[4 * g + 2], a1[4 * g + 3]};
                    lds_stf(lds, xo + g * 1024, v);
                }
            }
            LDS_BARRIER();
            if (TB == 1) {
                const int xi = L_ACT + (kq ^ 1) * 4096 + vl;
                f32x4 v[4];
#pragma unroll
                for (int g = 0; g < 4; ++g) v[g] = lds_ldf(lds, xi + g * 1024);
                SBAR();
#pragma unroll
                for (int g = 0; g < 4; ++g)
#pragma unroll
                    for (int i = 0; i < 4; ++i) a0[4 * g + i] = (par ? a1[4 * g + i] : a0[4 * g + i]) + v[g][i];
                PIN2(a0, a1);
                asm volatile("s_waitcnt lgkmcnt(0)" ::: "memory");
            }
            auto req_b = [&](int i) {
                const int f = (i < 4 ? (TB == 0 ? 16 + 8 * kq : 4 * (kq ^ 1)) : (TB == 0 ? 16 + 8 * kq : 44 + 4 * kq)) + i;
                glds16_sc1(ws + OFF_W2 + ((2 * j + ((f >> 3) & 1)) * KSFP + (f >> 4) * 22 + W2_REG + W2_A + (f & 7)) * 1024 + vl, lbase + L_W2B + f * 1024);
            };
            STAMP(25 + L * 32);
            if (d_active) {
                auto finish = [&](const f32x16& acc, int t, int nb) {
                    f32x4 tcs[4], tbb[4];
#pragma unroll
                    for (int g = 0; g < 4; ++g) { tcs[g] = lds_ldf(lds, L_TAB + (32 * cb1 + 8 * g + 4 * h) * 4); tbb[g] = lds_ldf(lds, L_TAB + 768 + (32 * cb1 + 8 * g + 4 * h) * 4); }
                    float t1, t2; stat_sums<8>(lds, t * 8, r, t1, t2);
                    float mu, rstd; ln_from_sums(t1, t2, mu, rstd);
                    const float nmr = -mu * rstd;
                    unsigned hp[8];
#pragma unroll
                    for (int g = 0; g < 4; ++g) {
                        const f32x4 cs = tcs[g], bb = tbb[g];
                        float y[4];
#pragma unroll
                        for (int i = 0; i < 4; ++i) {
                            const float x = fmaf(acc[4 * g + i], rstd, fmaf(cs[i], nmr, bb[i]));
                            const float t = x * fmaf(x * x, -2.f * 0.7978845608028654f * 0.044715f * 1.4426950408889634f, -2.f * 0.7978845608028654f * 1.4426950408889634f);
                            y[i] = x * __builtin_amdgcn_rcpf(1.f + __builtin_amdgcn_exp2f(t));
                        }
                        hp[2 * g] = pk(y[0], y[1]); hp[2 * g + 1] = pk(y[2], y[3]);
                        if (TB == 0) req_b(nb + g); else { req_b(2 * g); req_b(2 * g + 1); }
                        SBAR();
                    }
                    const int so = OFF_H + L * H_BYTES + ((2 * c + t) * KSF + 2 * cbg1) * 1024;
                    st_sc1(swap_order((u32x4){hp[0], hp[1], hp[2], hp[3]}), rw, vl, so);
                    st_sc1(swap_order((u32x4){hp[4], hp[5], hp[6], hp[7]}), rw, vl, so + 1024);
                };
                if (TB == 0) { finish(a0, 0, 0); finish(a1, 1, 4); } else finish(a0, par, 0);
            } else {
#pragma unroll
                for (int i = 0; i < 8; ++i) req_b(i);
            }
            STAMP(26 + L * 32);
            publish(tid, cnt + cnt_word(L, CNT_H, c));
            STAMP(27 + L * 32);
        }
        {
            PHASE_IDS();
            const u32x2 bb0 = *(const GAS u32x2*)(kp->b2 + 16 * sown + 4 * h), bb1 = *(const GAS u32x2*)(kp->b2 + 16 * sown + 8 + 4 * h);
            wait_cnt(tid, cnt + cnt_word(L, CNT_H, c), 8u, tmo);
            STAMP(28 + L * 32);
            u32x4 ha[22];
            const int hso = OFF_H + L * H_BYTES + tbg * KSF * 1024;
            constexpr int EWIN = 10;
#pragma unroll
            for (int i = 0; i < EWIN; ++i) ha[i] = ld_sc1(rw, vl, hso + min(kq * 22 + i, KSF - 1) * 1024);
            SBAR();
            f32x16 aA = {}, aB = {};
            const int aA0 = L_W2A + ((kq * 2 + (kq >> 1)) * W2_A) * 1024 + vw, aB0 = L_W2A + ((kq * 2 + ((kq >> 1) ^ 1)) * W2_A) * 1024 + vw;
            const int bA0 = L_W2B + ((kq * 2 + (kq >> 1)) * W2_B) * 1024 + vw, bB0 = L_W2B + ((kq * 2 + ((kq >> 1) ^ 1)) * W2_B) * 1024 + vw;
            u32x4 la[3], lb[3];
#pragma unroll
            for (int i = 0; i < 22; ++i) {
                if (i + EWIN < 22) ha[i + EWIN] = ld_sc1(rw, vl, hso + min(kq * 22 + i + EWIN, KSF - 1) * 1024);
                const int n = i + 2;
                if (n >= W2_REG && n < W2_REG + W2_A) { la[n % 3] = lds_ld(lds, aA0 + (n - W2_REG) * 1024); lb[n % 3] = lds_ld(lds, aB0 + (n - W2_REG) * 1024); }
                else if (n >= W2_REG + W2_A && n < 22) { la[n % 3] = lds_ld(lds, bA0 + (n - W2_REG - W2_A) * 1024); lb[n % 3] = lds_ld(lds, bB0 + (n - W2_REG - W2_A) * 1024); }
                SBAR();
                if (i < W2_REG) { aA = mfma(w2A[i], ha[i], aA); aB = mfma(w2B[i], ha[i], aB); }
                else { aA = mfma(la[i % 3], ha[i], aA); aB = mfma(lb[i % 3], ha[i], aB); }
                PIN2(aA, aB);
                SBAR();
            }
            STAMP(29 + L * 32);
            LDS_BARRIER();
            if (L == 0) {
                const int row = lane >> 3, pc = lane & 7;
                const uint16_t* src = kp->wq + (size_t)(64 * w + row) * D + 64 * j + (((pc >> 2) ^ ((row >> 1) & 1)) * 32 + (pc & 3) * 8);
                const bool mine = !(j == tb);
                const uint16_t* src2 = kp->wq + (size_t)(64 * (w ^ 4) + row) * D + 64 * j + (((pc >> 2) ^ ((row >> 1) & 1)) * 32 + (pc & 3) * 8);
                if (mine) {
#pragma unroll
                    for (int i = 0; i < 4; ++i) glds16(src + i * 8 * D, lbase + L_WQ + (w * 8 + i) * 1024);
                    if (j < 2) {
#pragma unroll
                        for (int i = 0; i < 4; ++i) glds16(src2 + i * 8 * D, lbase + L_WQ + ((w ^ 4) * 8 + i) * 1024);
                    }
                }
            }
            float o[8];
            reduce4(lds, tb * 24576, aA, aB, kq, lane, o);
#pragma unroll
            for (int i = 0; i < 2; ++i) { o[2 * i] += bflo(bb0[i]); o[2 * i + 1] += bfhi(bb0[i]); o[4 + 2 * i] += bflo(bb1[i]); o[4 + 2 * i + 1] += bfhi(bb1[i]); }
#pragma unroll
            for (int i = 0; i < 4; ++i) resid[i] = pk(bflo(resid[i]) + o[2 * i], bfhi(resid[i]) + o[2 * i + 1]);
            STAMP(30 + L * 32);
            const u32x4 xo = swap_order(resid);
            if (L == 0) {
                st_sc1(xo, rw, vl, OFF_X2 + (tbg * KSD + sown) * 1024);
                publish(tid, cnt + cnt_word(0, CNT_X2, c));
            } else {
                *(GAS u32x4*)(kp->out + (size_t)(64 * c + 32 * tb + r) * D + 16 * sown + 8 * h) = xo;
            }
            STAMP(31 + L * 32);
        }
    }
    { unsigned e = epoch0; asm volatile("" : "+s"(e)); if (blockIdx.x == 0 && threadIdx.x == 0) __hip_atomic_store((gu32*)(ws0 + OFF_SYNC), e + 2u, RLX_AGENT); }
}

__global__ void __launch_bounds__(512, 2) main_kernel(Params p) {
    extern __shared__ __attribute__((aligned(16))) char lds_dyn[];
    LAS char* lds = (LAS char*)lds_dyn;
    if (__builtin_amdgcn_readfirstlane(threadIdx.x >> 8) == 0) run_layers<0>(p.ws, lds); else run_layers<1>(p.ws, lds);
}

}

extern "C" void kernel_launch(void* const* d_in, const int* in_sizes, int n_in, void* d_out, int out_size, void* d_ws, size_t ws_size, hipStream_t stream) {
    Params p;
    p.x = (const uint16_t*)d_in[0]; p.wq = (const uint16_t*)d_in[1]; p.wk = (const uint16_t*)d_in[2]; p.wv = (const uint16_t*)d_in[3]; p.wo = (const uint16_t*)d_in[4];
    p.w1 = (const uint16_t*)d_in[6]; p.b1 = (const uint16_t*)d_in[7]; p.w2 = (const uint16_t*)d_in[8]; p.b2 = (const uint16_t*)d_in[9];
    p.out = (uint16_t*)d_out; p.ws = (unsigned char*)d_ws;
    (void)hipFuncSetAttribute((const void*)main_kernel, hipFuncAttributeMaxDynamicSharedMemorySize, LDS_BYTES);
    hipLaunchKernelGGL(main_kernel, dim3(256), dim3(512), LDS_BYTES, stream, p);
}
```

```cpp
#include <hip/hip_runtime.h>
#include <hip/hip_bf16.h>
#include <stdint.h>

#define GAS __attribute__((address_space(1)))
#define LAS __attribute__((address_space(3)))
typedef __bf16 bf16x8 __attribute__((ext_vector_type(8)));
typedef __bf16 bf16x2 __attribute__((ext_vector_type(2)));
typedef float f32x16 __attribute__((ext_vector_type(16)));
typedef float f32x4 __attribute__((ext_vector_type(4)));
typedef float f32x2 __attribute__((ext_vector_type(2)));
typedef unsigned u32x4 __attribute__((ext_vector_type(4)));
typedef GAS unsigned gu32;
#define RLX_AGENT __ATOMIC_RELAXED, __HIP_MEMORY_SCOPE_AGENT

namespace {

constexpr int S = 2048, D = 512, F = 1376;
constexpr int KSD = 32;
constexpr int KSF = 86;
constexpr int KSFP = 88;
constexpr float LN_EPS = 1e-5f;
constexpr float QSCALE = 0.25f * 1.4426950408889634f;
constexpr float SAFE_BOUND = 50.f;

constexpr int CW = 64;
constexpr int OFF_CNT = 0;
constexpr int CNT_O = 0, CNT_X1 = 1, CNT_H = 2, CNT_X2 = 3;
__device__ __forceinline__ int cnt_word(int layer, int kind, int c) { return ((layer * 4 + kind) * 32 + c) * CW; }
constexpr int OFF_PREPC = 65536;
constexpr int OFF_SYNC = OFF_PREPC + 32 * 256;
constexpr int OFF_KVFL = OFF_SYNC + 1024;
constexpr int OFF_WO = OFF_KVFL + 2 * 32 * 512;
constexpr int OFF_W1 = OFF_WO + 16 * KSD * 1024;
constexpr int OFF_W2 = OFF_W1 + 43 * KSD * 1024;
constexpr int OFF_CS1 = OFF_W2 + 16 * KSFP * 1024;
constexpr int OFF_KF = (OFF_CS1 + 1408 * 4 + 1023) / 1024 * 1024;
constexpr int OFF_VF = OFF_KF + 2 * 65536;
constexpr int OFF_O = OFF_VF + 2 * 65536;
constexpr int ACT_BYTES = 64 * KSD * 1024;
constexpr int OFF_X1 = OFF_O + 2 * ACT_BYTES;
constexpr int OFF_X2 = OFF_X1 + 2 * ACT_BYTES;
constexpr int H_BYTES = 64 * KSF * 1024;
constexpr int OFF_H = OFF_X2 + ACT_BYTES;
constexpr int WS_TOTAL = OFF_H + 2 * H_BYTES;
constexpr unsigned SPIN_CAP = 1u << 15;

constexpr int L_K = 0, L_V = 65536, L_ONES = 131072, L_XCHO = 135424;
constexpr int L_WQ = 65536, L_WKV0 = 49152, L_WKV1 = 131072, L_PREP = 0, L_KVRED = 32768, L_XCHQ = 57344;
constexpr int L_ACT = 0, L_W2A = 65536, L_W2B = 0;
constexpr int L_STAT = 155648, L_TAB = 159744, L_CS = L_TAB, L_FLAG = 163584;
constexpr int F_UNSAFE = L_FLAG, F_KM2 = L_FLAG + 32, F_N2 = L_FLAG + 36, F_KVCNT = L_FLAG + 40, F_OCNT = L_FLAG + 44, F_PAIR = L_FLAG + 64;
constexpr int LDS_BYTES = 163840;
constexpr int W2_REG = 3, W2_A = 11, W2_B = 8;
constexpr int W1_EARLY = 24;

#define TLB_WARM 0
#define PRIO 1
#define ZROWS 1
#define EWIN_ 10
#define PROF 0
#define PROF_TID 0
constexpr int L_PROF = 161280;
constexpr int OFF_PROF = 64 << 20;
#define STAMP(i) do { } while (0)

struct Params {
    const uint16_t *x, *wq, *wk, *wv, *wo, *w1, *b1, *w2, *b2;
    uint16_t* out;
    unsigned char* ws;
};

__device__ __forceinline__ f32x16 mfma(u32x4 a, u32x4 b, f32x16 c) {
    return __builtin_amdgcn_mfma_f32_32x32x16_bf16(__builtin_bit_cast(bf16x8, a), __builtin_bit_cast(bf16x8, b), c, 0, 0, 0);
}
__device__ __forceinline__ unsigned pk(float a, float b) { f32x2 v = {a, b}; return __builtin_bit_cast(unsigned, __builtin_convertvector(v, bf16x2)); }
__device__ __forceinline__ float bflo(unsigned u) { return __uint_as_float(u << 16); }
__device__ __forceinline__ float bfhi(unsigned u) { return __uint_as_float(u & 0xffff0000u); }
__device__ __forceinline__ float bf2f(uint16_t v) { return __uint_as_float((unsigned)v << 16); }
__device__ __forceinline__ float dot2(unsigned a, unsigned b, float c) {
    return __builtin_amdgcn_fdot2_f32_bf16(__builtin_bit_cast(bf16x2, a), __builtin_bit_cast(bf16x2, b), c, false);
}
__device__ __forceinline__ float half_sum(float v) {
    auto r = __builtin_amdgcn_permlane32_swap(__float_as_uint(v), __float_as_uint(v), false, false);
    return __uint_as_float(r[0]) + __uint_as_float(r[1]);
}
__device__ __forceinline__ float half_max(float v) {
    auto r = __builtin_amdgcn_permlane32_swap(__float_as_uint(v), __float_as_uint(v), false, false);
    return fmaxf(__uint_as_float(r[0]), __uint_as_float(r[1]));
}
__device__ __forceinline__ u32x4 swap_order(u32x4 a) {
    auto r0 = __builtin_amdgcn_permlane32_swap(a[0], a[2], false, false);
    auto r1 = __builtin_amdgcn_permlane32_swap(a[1], a[3], false, false);
    return (u32x4){r0[0], r1[0], r0[1], r1[1]};
}
template <int CTRL> __device__ __forceinline__ float dpp(float v) {
    return __uint_as_float(__builtin_amdgcn_update_dpp(0u, __float_as_uint(v), CTRL, 0xf, 0xf, true));
}
__device__ __forceinline__ float wave_max(float v) {
    v = fmaxf(v, dpp<0xB1>(v));
    v = fmaxf(v, dpp<0x4E>(v));
    v = fmaxf(v, dpp<0x141>(v));
    v = fmaxf(v, dpp<0x140>(v));
    auto r = __builtin_amdgcn_permlane16_swap(__float_as_uint(v), __float_as_uint(v), false, false);
    return half_max(fmaxf(__uint_as_float(r[0]), __uint_as_float(r[1])));
}
#define SBAR() __builtin_amdgcn_sched_barrier(0)
#define PIN8(x) asm volatile("" : "+v"(x[0]), "+v"(x[1]), "+v"(x[2]), "+v"(x[3]), "+v"(x[4]), "+v"(x[5]), "+v"(x[6]), "+v"(x[7]))
#define PIN16(x) asm volatile("" : "+v"(x[0]), "+v"(x[1]), "+v"(x[2]), "+v"(x[3]), "+v"(x[4]), "+v"(x[5]), "+v"(x[6]), "+v"(x[7]), \
    "+v"(x[8]), "+v"(x[9]), "+v"(x[10]), "+v"(x[11]), "+v"(x[12]), "+v"(x[13]), "+v"(x[14]), "+v"(x[15]))
#define PIN2(a, b) asm volatile("" : "+v"(a), "+v"(b))
#define LDS_BARRIER() asm volatile("s_waitcnt lgkmcnt(0)\n\ts_barrier" ::: "memory")
#define DRAIN() asm volatile("s_waitcnt vmcnt(0)" ::: "memory")
constexpr int AUX_SC1 = 16;
typedef __amdgpu_buffer_rsrc_t rsrc_t;
__device__ __forceinline__ u32x4 ld_sc1(rsrc_t r, int voff, int soff) { return __builtin_amdgcn_raw_buffer_load_b128(r, voff, soff, AUX_SC1); }
__device__ __forceinline__ u32x4 ld_plain(rsrc_t r, int voff, int soff) { return __builtin_amdgcn_raw_buffer_load_b128(r, voff, soff, 0); }
__device__ __forceinline__ void st_sc1(u32x4 v, rsrc_t r, int voff, int soff) {
    __builtin_amdgcn_raw_buffer_store_b128(v, r, voff, soff, AUX_SC1);
    asm volatile("s_nop 1" :: "v"(v) : "memory");
}
__device__ __forceinline__ u32x4 lds_ld(LAS char* l, int off) { return *(LAS u32x4*)(l + off); }
__device__ __forceinline__ void lds_st(LAS char* l, int off, u32x4 v) { *(LAS u32x4*)(l + off) = v; }
__device__ __forceinline__ f32x4 lds_ldf(LAS char* l, int off) { return *(LAS f32x4*)(l + off); }
__device__ __forceinline__ void lds_stf(LAS char* l, int off, f32x4 v) { *(LAS f32x4*)(l + off) = v; }

__device__ __forceinline__ void publish(int tid, gu32* cnt) {
    DRAIN();
    LDS_BARRIER();
    if (tid == 0) __hip_atomic_fetch_add(cnt, 1u, RLX_AGENT);
}
__device__ __forceinline__ void poll_ge(gu32* cnt, unsigned want, gu32* tmo) {
    unsigned spins = 0;
    while (__hip_atomic_load(cnt, RLX_AGENT) < want) {
        if (++spins > SPIN_CAP) { __hip_atomic_store(tmo, 1u, RLX_AGENT); break; }
    }
}
__device__ __forceinline__ void wait_cnt(int tid, gu32* cnt, unsigned want, gu32* tmo) {
    if (tid == 0) poll_ge(cnt, want, tmo);
    __builtin_amdgcn_fence(__ATOMIC_ACQUIRE, "wavefront");
    LDS_BARRIER();
}
typedef GAS unsigned long long gu64;
__device__ __forceinline__ float wait_flags(int tid, LAS char* lds, gu64* fl, unsigned tag, gu32* tmo) {
    if (tid < 64) {
        unsigned spins = 0; unsigned long long v;
        while (v = __hip_atomic_load(fl + tid, RLX_AGENT), !__all((unsigned)(v >> 32) == tag)) {
            if (++spins > SPIN_CAP) { if (tid == 0) __hip_atomic_store(tmo, 1u, RLX_AGENT); break; }
        }
        const float m = wave_max(__uint_as_float((unsigned)v));
        if (tid == 0) *(LAS float*)(lds + F_KM2) = m;
    }
    __builtin_amdgcn_fence(__ATOMIC_ACQUIRE, "wavefront");
    LDS_BARRIER();
    return *(LAS float*)(lds + F_KM2);
}
__device__ __forceinline__ void glds16(const void* g, unsigned lds_dst) {
    unsigned keep;
    asm volatile("s_mov_b32 %0, m0\n\ts_mov_b32 m0, %2\n\ts_nop 0\n\tglobal_load_lds_dwordx4 %1, off\n\ts_mov_b32 m0, %0" : "=&s"(keep) : "v"(g), "s"(lds_dst) : "memory");
}
__device__ __forceinline__ void glds16_sc1(const void* g, unsigned lds_dst) {
    unsigned keep;
    asm volatile("s_mov_b32 %0, m0\n\ts_mov_b32 m0, %2\n\ts_nop 0\n\tglobal_load_lds_dwordx4 %1, off sc1\n\ts_mov_b32 m0, %0" : "=&s"(keep) : "v"(g), "s"(lds_dst) : "memory");
}
#define WAITVM(n) asm volatile("s_waitcnt vmcnt(" #n ")" ::: "memory")

__device__ __forceinline__ void reduce4(LAS char* lds, int region, const f32x16& aA, const f32x16& aB, int kq, int lane, float (&o)[8]) {
    const int base = region + lane * 16;
    { const int a = base + ((kq ^ 1) * 3 + 0) * 2048;
      lds_stf(lds, a, (f32x4){aA[8], aA[9], aA[10], aA[11]}); lds_stf(lds, a + 1024, (f32x4){aA[12], aA[13], aA[14], aA[15]}); }
    { const int a = base + ((kq ^ 2) * 3 + 1) * 2048;
      lds_stf(lds, a, (f32x4){aB[0], aB[1], aB[2], aB[3]}); lds_stf(lds, a + 1024, (f32x4){aB[4], aB[5], aB[6], aB[7]}); }
    { const int a = base + ((kq ^ 3) * 3 + 2) * 2048;
      lds_stf(lds, a, (f32x4){aB[8], aB[9], aB[10], aB[11]}); lds_stf(lds, a + 1024, (f32x4){aB[12], aB[13], aB[14], aB[15]}); }
    LDS_BARRIER();
    f32x4 lo = {aA[0], aA[1], aA[2], aA[3]}, hi = {aA[4], aA[5], aA[6], aA[7]}, t[6];
#pragma unroll
    for (int x = 0; x < 6; ++x) t[x] = lds_ldf(lds, base + kq * 3 * 2048 + x * 1024);
    SBAR();
#pragma unroll
    for (int x = 0; x < 3; ++x) { lo += t[2 * x]; hi += t[2 * x + 1]; }
#pragma unroll
    for (int i = 0; i < 4; ++i) { o[i] = lo[i]; o[4 + i] = hi[i]; }
}
typedef short v4i16 __attribute__((ext_vector_type(4)));
typedef unsigned u32x2 __attribute__((ext_vector_type(2)));
__device__ __forceinline__ u32x4 tr_frag(LAS char* lds, int a, int step) {
    const u32x2 lo = __builtin_bit_cast(u32x2, __builtin_amdgcn_ds_read_tr16_b64_v4i16((LAS v4i16*)(lds + a)));
    const u32x2 hi = __builtin_bit_cast(u32x2, __builtin_amdgcn_ds_read_tr16_b64_v4i16((LAS v4i16*)(lds + a + step)));
    return (u32x4){lo[0], lo[1], hi[0], hi[1]};
}
__device__ __forceinline__ float frag_sum(u32x4 f, float s) {
#pragma unroll
    for (int d = 0; d < 4; ++d) s = dot2(f[d], 0x3f803f80u, s);
    return s;
}

#define A_E1(a) "v_exp_f32 v" #a ", v" #a "\n\t"
#define A_E4(a, b, c, d) A_E1(a) A_E1(b) A_E1(c) A_E1(d)
#define A_CVT(d, a, b) "v_cvt_pk_bf16_f32 v" #d ", v" #a ", v" #b "\n\t"
#define A_DSK(off) "ds_read_b128 v[92:95], v112 offset:" #off "\n\t"
#define A_DSV0(off) "ds_read_b128 v[96:99], v113 offset:" #off "\n\t"
#define A_DSV1(off) "ds_read_b128 v[100:103], v113 offset:" #off "\n\t"
#define A_WAIT(n) "s_waitcnt lgkmcnt(" #n ")\n\t"
#define A_MSB "v_mfma_f32_32x32x16_bf16 v[40:55], v[92:95], v[148:151], v[116:131]\n\t"
#define A_MSA "v_mfma_f32_32x32x16_bf16 v[24:39], v[92:95], v[88:91], v[56:71]\n\t"
#define A_MPV(O, V, P) "v_mfma_f32_32x32x16_bf16 " O ", " V ", " P ", " O "\n\t"
#define A_OA "v[72:87]"
#define A_OB "v[132:147]"
#define A_V0 "v[96:99]"
#define A_V1 "v[100:103]"
#define A_P0 "v[104:107]"
#define A_P1 "v[108:111]"
#define A_STEP(koff, v1off, v0off) \
    A_MSB A_DSK(koff) A_E4(24, 25, 26, 27) \
    A_MPV(A_OB, A_V1, A_P1) A_DSV1(v1off) A_E4(28, 29, 30, 31) \
    A_CVT(104, 24, 25) A_CVT(105, 26, 27) A_CVT(106, 28, 29) A_CVT(107, 30, 31) \
    A_E4(32, 33, 34, 35) \
    A_WAIT(2) A_MPV(A_OA, A_V0, A_P0) A_E4(36, 37, 38, 39) \
    A_CVT(108, 32, 33) A_CVT(109, 34, 35) A_CVT(110, 36, 37) A_CVT(111, 38, 39) \
    A_WAIT(1) A_MSA A_E4(40, 41, 42, 43) \
    A_WAIT(0) A_MPV(A_OA, A_V1, A_P1) A_E4(44, 45, 46, 47) \
    A_CVT(104, 40, 41) A_CVT(105, 42, 43) A_CVT(106, 44, 45) A_CVT(107, 46, 47) \
    A_E4(48, 49, 50, 51) \
    A_MPV(A_OB, A_V0, A_P0) A_DSV0(v0off) A_E4(52, 53, 54, 55) \
    A_CVT(108, 48, 49) A_CVT(109, 50, 51) A_CVT(110, 52, 53) A_CVT(111, 54, 55)

struct AttnState { f32x16 sa, sb, oa, ob, negma, negmb; u32x4 qa, qb, kf, v0, v1, p1; unsigned kaddr, vaddr, vstride; };
#define A_OPERANDS(st, cnt) \
        : "+{v[24:39]}"(st.sa), "+{v[40:55]}"(st.sb), "+{v[72:87]}"(st.oa), "+{v[132:147]}"(st.ob), "+{v[92:95]}"(st.kf), "+{v[96:99]}"(st.v0), "+{v[100:103]}"(st.v1), \
          "={v[104:107]}"(p0), "+{v[108:111]}"(st.p1), "+{v112}"(st.kaddr), "+{v113}"(st.vaddr), [n] "+s"(cnt) \
        : "{v[56:71]}"(st.negma), "{v[116:131]}"(st.negmb), "{v[88:91]}"(st.qa), "{v[148:151]}"(st.qb), "{v114}"(st.vstride) \
        : "memory", "scc"
#define A_ITER \
        A_STEP(1024, 512, 1024) \
        A_STEP(2048, 1536, 2048) \
        A_STEP(3072, 2560, 3072) \
        A_STEP(4096, 3584, 4096) \
        "v_add_u32 v112, 4096, v112\n\t" \
        "v_add_u32 v113, v114, v113\n\t"
__device__ __forceinline__ void attn_begin(AttnState& st, LAS char* lds, u32x4 qa, u32x4 qb, float ma, float mb, unsigned kaddr, unsigned vaddr, unsigned vstride) {
#pragma unroll
    for (int i = 0; i < 16; ++i) { st.negma[i] = -ma; st.negmb[i] = -mb; }
    st.qa = qa; st.qb = qb;
    st.kf = lds_ld(lds, kaddr); st.v0 = lds_ld(lds, vaddr); st.v1 = st.v0; st.p1 = (u32x4){0u, 0u, 0u, 0u};
    st.sa = mfma(st.kf, qa, st.negma); st.sb = st.sa;
    st.oa = (f32x16){}; st.ob = (f32x16){};
    const unsigned lbase = (unsigned)(uintptr_t)lds;
    st.kaddr = kaddr + lbase; st.vaddr = vaddr + lbase; st.vstride = vstride;
}
#define A_DMA \
    "s_mov_b32 m0, s92\n\t" \
    "s_add_u32 s92, s92, 1024\n\t" \
    "global_load_lds_dwordx4 v115, s[90:91] sc1\n\t" \
    "s_add_u32 s90, s90, 1024\n\t" \
    "s_addc_u32 s91, s91, 0\n\t"
#define A_ITER_DMA \
        A_DMA A_DMA A_STEP(1024, 512, 1024) \
        A_DMA A_STEP(2048, 1536, 2048) \
        A_DMA A_STEP(3072, 2560, 3072) \
        A_DMA A_STEP(4096, 3584, 4096) \
        "v_add_u32 v112, 4096, v112\n\t" \
        "v_add_u32 v113, v114, v113\n\t"
__device__ __forceinline__ void attn_first(AttnState& st, unsigned long long src, unsigned dst, unsigned voff) {
    unsigned cnt = 2, keep; u32x4 p0;
    asm volatile(
        "s_mov_b32 %[keep], m0\n\t"
        "s_nop 15\n\t"
        A_ITER_DMA
        A_ITER_DMA
        "s_mov_b32 m0, %[keep]\n\t"
        "s_waitcnt vmcnt(0)\n\t"
        "s_barrier\n\t"
        "ATT1_LOOP_%=:\n\t"
        A_ITER
        "s_sub_u32 %[n], %[n], 1\n\t"
        "s_cmp_lg_u32 %[n], 0\n\t"
        "s_cbranch_scc1 ATT1_LOOP_%=\n\t"
        : "+{v[24:39]}"(st.sa), "+{v[40:55]}"(st.sb), "+{v[72:87]}"(st.oa), "+{v[132:147]}"(st.ob), "+{v[92:95]}"(st.kf), "+{v[96:99]}"(st.v0), "+{v[100:103]}"(st.v1),
          "={v[104:107]}"(p0), "+{v[108:111]}"(st.p1), "+{v112}"(st.kaddr), "+{v113}"(st.vaddr), [n] "+s"(cnt), [keep] "=&s"(keep), "+{s[90:91]}"(src), "+{s92}"(dst)
        : "{v[56:71]}"(st.negma), "{v[116:131]}"(st.negmb), "{v[88:91]}"(st.qa), "{v[148:151]}"(st.qb), "{v114}"(st.vstride), "{v115}"(voff)
        : "memory", "scc");
}
typedef unsigned u32x16 __attribute__((ext_vector_type(16)));
struct Prefetch { u32x16 a0, a1, b0, b1, w0, w1; };
template <int TB>
__device__ __forceinline__ void attn_second(AttnState& st, Prefetch& pf, u32x4 rs, unsigned voffw, unsigned voffl, unsigned soa, unsigned sob, unsigned sow) {
    u32x4 p0;
    if constexpr (TB == 0) {
    asm volatile(
        "buffer_load_dwordx4 v[152:155], v115, %[rs], s93 offen offset:0 sc1\n\t" A_STEP(1024, 512, 1024)
        "buffer_load_dwordx4 v[156:159], v115, %[rs], s93 offen offset:1024 sc1\n\t" A_STEP(2048, 1536, 2048)
        "buffer_load_dwordx4 v[160:163], v115, %[rs], s93 offen offset:2048 sc1\n\t" A_STEP(3072, 2560, 3072)
        "buffer_load_dwordx4 v[164:167], v115, %[rs], s93 offen offset:3072 sc1\n\t" "s_add_u32 s93, s93, 4096\n\t" A_STEP(4096, 3584, 4096)
        "v_add_u32 v112, 4096, v112\n\t" "v_add_u32 v113, v114, v113\n\t"
        "buffer_load_dwordx4 v[168:171], v115, %[rs], s93 offen offset:0 sc1\n\t" A_STEP(1024, 512, 1024)
        "buffer_load_dwordx4 v[172:175], v115, %[rs], s93 offen offset:1024 sc1\n\t" A_STEP(2048, 1536, 2048)
        "buffer_load_dwordx4 v[176:179], v115, %[rs], s93 offen offset:2048 sc1\n\t" A_STEP(3072, 2560, 3072)
        "buffer_load_dwordx4 v[180:183], v115, %[rs], s93 offen offset:3072 sc1\n\t" "s_add_u32 s93, s93, 4096\n\t" A_STEP(4096, 3584, 4096)
        "v_add_u32 v112, 4096, v112\n\t" "v_add_u32 v113, v114, v113\n\t"
        "buffer_load_dwordx4 v[184:187], v115, %[rs], s94 offen offset:0 sc1\n\t" A_STEP(1024, 512, 1024)
        "buffer_load_dwordx4 v[188:191], v115, %[rs], s94 offen offset:1024 sc1\n\t" A_STEP(2048, 1536, 2048)
        "buffer_load_dwordx4 v[192:195], v115, %[rs], s94 offen offset:2048 sc1\n\t" A_STEP(3072, 2560, 3072)
        "buffer_load_dwordx4 v[196:199], v115, %[rs], s94 offen offset:3072 sc1\n\t" "s_add_u32 s94, s94, 4096\n\t" A_STEP(4096, 3584, 4096)
        "v_add_u32 v112, 4096, v112\n\t" "v_add_u32 v113, v114, v113\n\t"
        "buffer_load_dwordx4 v[200:203], v115, %[rs], s94 offen offset:0 sc1\n\t" A_STEP(1024, 512, 1024)
        "buffer_load_dwordx4 v[204:207], v115, %[rs], s94 offen offset:1024 sc1\n\t" A_STEP(2048, 1536, 2048)
        "buffer_load_dwordx4 v[208:211], v115, %[rs], s94 offen offset:2048 sc1\n\t" A_STEP(3072, 2560, 3072)
        "buffer_load_dwordx4 v[212:215], v115, %[rs], s94 offen offset:3072 sc1\n\t" "s_add_u32 s94, s94, 4096\n\t" A_STEP(4096, 3584, 4096)
        "v_add_u32 v112, 4096, v112\n\t" "v_add_u32 v113, v114, v113\n\t"
        "s_waitcnt lgkmcnt(0)\n\t"
        A_MPV(A_OB, A_V1, A_P1)
        "s_nop 15\n\t"
        : "+{v[24:39]}"(st.sa), "+{v[40:55]}"(st.sb), "+{v[72:87]}"(st.oa), "+{v[132:147]}"(st.ob), "+{v[92:95]}"(st.kf), "+{v[96:99]}"(st.v0), "+{v[100:103]}"(st.v1),
          "={v[104:107]}"(p0), "+{v[108:111]}"(st.p1), "+{v112}"(st.kaddr), "+{v113}"(st.vaddr), "+{s93}"(soa), "+{s94}"(sob),
          "=&{v[152:167]}"(pf.a0), "=&{v[168:183]}"(pf.a1), "=&{v[184:199]}"(pf.b0), "=&{v[200:215]}"(pf.b1)
        : "{v[56:71]}"(st.negma), "{v[116:131]}"(st.negmb), "{v[88:91]}"(st.qa), "{v[148:151]}"(st.qb), "{v114}"(st.vstride), "{v115}"(voffw), [rs] "s"(rs)
        : "memory", "scc");
    } else {
    asm volatile(
        "buffer_load_dwordx4 v[152:155], v115, %[rs], s93 offen offset:0 sc1\n\t" "buffer_load_dwordx4 v[216:219], %[vl], %[rs], s95 offen offset:0 sc1\n\t" A_STEP(1024, 512, 1024)
        "buffer_load_dwordx4 v[156:159], v115, %[rs], s93 offen offset:1024 sc1\n\t" "buffer_load_dwordx4 v[220:223], %[vl], %[rs], s95 offen offset:2048 sc1\n\t" "s_add_u32 s95, s95, 4096\n\t" A_STEP(2048, 1536, 2048)
        "buffer_load_dwordx4 v[160:163], v115, %[rs], s93 offen offset:2048 sc1\n\t" "buffer_load_dwordx4 v[224:227], %[vl], %[rs], s95 offen offset:0 sc1\n\t" A_STEP(3072, 2560, 3072)
        "buffer_load_dwordx4 v[164:167], v115, %[rs], s93 offen offset:3072 sc1\n\t" "s_add_u32 s93, s93, 4096\n\t" "buffer_load_dwordx4 v[228:231], %[vl], %[rs], s95 offen offset:2048 sc1\n\t" "s_add_u32 s95, s95, 4096\n\t" A_STEP(4096, 3584, 4096)
        "v_add_u32 v112, 4096, v112\n\t" "v_add_u32 v113, v114, v113\n\t"
        "buffer_load_dwordx4 v[168:171], v115, %[rs], s93 offen offset:0 sc1\n\t" "buffer_load_dwordx4 v[232:235], %[vl], %[rs], s95 offen offset:0 sc1\n\t" A_STEP(1024, 512, 1024)
        "buffer_load_dwordx4 v[172:175], v115, %[rs], s93 offen offset:1024 sc1\n\t" "buffer_load_dwordx4 v[236:239], %[vl], %[rs], s95 offen offset:2048 sc1\n\t" "s_add_u32 s95, s95, 4096\n\t" A_STEP(2048, 1536, 2048)
        "buffer_load_dwordx4 v[176:179], v115, %[rs], s93 offen offset:2048 sc1\n\t" "buffer_load_dwordx4 v[240:243], %[vl], %[rs], s95 offen offset:0 sc1\n\t" A_STEP(3072, 2560, 3072)
        "buffer_load_dwordx4 v[180:183], v115, %[rs], s93 offen offset:3072 sc1\n\t" "s_add_u32 s93, s93, 4096\n\t" "buffer_load_dwordx4 v[244:247], %[vl], %[rs], s95 offen offset:2048 sc1\n\t" "s_add_u32 s95, s95, 4096\n\t" A_STEP(4096, 3584, 4096)
        "v_add_u32 v112, 4096, v112\n\t" "v_add_u32 v113, v114, v113\n\t"
        "buffer_load_dwordx4 v[184:187], v115, %[rs], s94 offen offset:0 sc1\n\t" A_STEP(1024, 512, 1024)
        "buffer_load_dwordx4 v[188:191], v115, %[rs], s94 offen offset:1024 sc1\n\t" A_STEP(2048, 1536, 2048)
        "buffer_load_dwordx4 v[192:195], v115, %[rs], s94 offen offset:2048 sc1\n\t" A_STEP(3072, 2560, 3072)
        "buffer_load_dwordx4 v[196:199], v115, %[rs], s94 offen offset:3072 sc1\n\t" "s_add_u32 s94, s94, 4096\n\t" A_STEP(4096, 3584, 4096)
        "v_add_u32 v112, 4096, v112\n\t" "v_add_u32 v113, v114, v113\n\t"
        "buffer_load_dwordx4 v[200:203], v115, %[rs], s94 offen offset:0 sc1\n\t" A_STEP(1024, 512, 1024)
        "buffer_load_dwordx4 v[204:207], v115, %[rs], s94 offen offset:1024 sc1\n\t" A_STEP(2048, 1536, 2048)
        "buffer_load_dwordx4 v[208:211], v115, %[rs], s94 offen offset:2048 sc1\n\t" A_STEP(3072, 2560, 3072)
        "buffer_load_dwordx4 v[212:215], v115, %[rs], s94 offen offset:3072 sc1\n\t" "s_add_u32 s94, s94, 4096\n\t" A_STEP(4096, 3584, 4096)
        "v_add_u32 v112, 4096, v112\n\t" "v_add_u32 v113, v114, v113\n\t"
        "s_waitcnt lgkmcnt(0)\n\t"
        A_MPV(A_OB, A_V1, A_P1)
        "s_nop 15\n\t"
        : "+{v[24:39]}"(st.sa), "+{v[40:55]}"(st.sb), "+{v[72:87]}"(st.oa), "+{v[132:147]}"(st.ob), "+{v[92:95]}"(st.kf), "+{v[96:99]}"(st.v0), "+{v[100:103]}"(st.v1),
          "={v[104:107]}"(p0), "+{v[108:111]}"(st.p1), "+{v112}"(st.kaddr), "+{v113}"(st.vaddr), "+{s93}"(soa), "+{s94}"(sob), "+{s95}"(sow),
          "=&{v[152:167]}"(pf.a0), "=&{v[168:183]}"(pf.a1), "=&{v[184:199]}"(pf.b0), "=&{v[200:215]}"(pf.b1), "=&{v[216:231]}"(pf.w0), "=&{v[232:247]}"(pf.w1)
        : "{v[56:71]}"(st.negma), "{v[116:131]}"(st.negmb), "{v[88:91]}"(st.qa), "{v[148:151]}"(st.qb), "{v114}"(st.vstride), "{v115}"(voffw), [rs] "s"(rs), [vl] "v"(voffl)
        : "memory", "scc");
    }
}
template <int I> __device__ __forceinline__ u32x4 quad(const u32x16& v) { return (u32x4){v[4 * I], v[4 * I + 1], v[4 * I + 2], v[4 * I + 3]}; }

__device__ __forceinline__ void stats_acc(u32x4 f, float& s1, float& s2) {
#pragma unroll
    for (int d = 0; d < 4; ++d) { s1 = dot2(f[d], 0x3f803f80u, s1); s2 = dot2(f[d], f[d], s2); }
}
template <int N>
__device__ __forceinline__ void stat_sums(LAS char* lds, int slot0, int tok, float& t1, float& t2) {
    f32x2 v[N];
#pragma unroll
    for (int i = 0; i < N; ++i) v[i] = *(LAS f32x2*)(lds + L_STAT + ((slot0 + i) * 32 + tok) * 8);
    SBAR();
    t1 = 0.f; t2 = 0.f;
#pragma unroll
    for (int i = 0; i < N; ++i) { t1 += v[i][0]; t2 += v[i][1]; }
}
__device__ __forceinline__ void ln_from_sums(float t1, float t2, float& mu, float& rstd) {
    mu = t1 * (1.f / D);
    rstd = __builtin_amdgcn_rsqf(fmaxf(t2 * (1.f / D) - mu * mu, 0.f) + LN_EPS);
}

template <int TB>
__device__ __forceinline__ void run_layers(unsigned char* ws0, LAS char* lds) {
    const unsigned lbase = (unsigned)(uintptr_t)lds;
    const int tid0 = threadIdx.x, w0 = __builtin_amdgcn_readfirstlane(tid0 >> 6);
    const int kk0 = blockIdx.x >> 3, c0 = (blockIdx.x & 7) * 4 + (kk0 >> 3), j0 = kk0 & 7;
    typedef const __attribute__((address_space(4))) Params* kargs_t;
#define PHASE_IDS() \
    int tid = tid0, w = w0, c = c0, j = j0; unsigned epoch = epoch0; unsigned char* ws = ws0; \
    kargs_t kp = (kargs_t)__builtin_amdgcn_kernarg_segment_ptr();        \
    asm volatile("" : "+v"(tid)); asm volatile("" : "+s"(w), "+s"(c), "+s"(j), "+s"(epoch), "+s"(ws), "+s"(kp)); \
    const rsrc_t rw = __builtin_amdgcn_make_buffer_rsrc(ws, 0, WS_TOTAL, 0x00020000); \
    gu32* cnt = (gu32*)(ws + OFF_CNT); gu32* sync = (gu32*)(ws + OFF_SYNC); gu32* tmo = sync + 2 * CW; \
    (void)rw; (void)cnt; (void)tmo; (void)kp; \
    constexpr int tb = TB; const int lane = tid & 63, r = lane & 31, h = lane >> 5, kq = w & 3, tbg = 2 * c + tb; \
    const int vl = lane * 16;                                     \
    const int vw = (lane ^ ((kq & 1) << 4)) * 16;                 \
    const int cbA = 2 * j + (kq >> 1), cbB = 2 * j + ((kq >> 1) ^ 1); \
    const int sown = 4 * j + kq;                                  \
    (void)epoch; (void)r; (void)h; (void)tbg; (void)vl; (void)vw; (void)cbA; (void)cbB; (void)sown; (void)tb;
#define W1_IDS() \
    const int ncb = j < 3 ? 6 : 5, cb0 = j < 3 ? 6 * j : 18 + 5 * (j - 3);     \
    const int cb1 = TB == 0 ? kq : 4 + (kq >> 1);     \
    const bool d_active = cb1 < ncb; \
    const int cbg1 = cb0 + (d_active ? cb1 : 0); (void)cbg1; \
    const int par = TB == 0 ? 0 : (kq & 1); (void)par;

#define REQ_WKV() do { \
        const uint16_t* src_ = ((lane & 2) ? kp->wv : kp->wk) + (128 * kq + (lane >> 2)) * 16 + (lane & 1) * 8; \
        _Pragma("unroll") for (int i = 0; i < 8; ++i) glds16(src_ + i * 256, lbase + ((kq & 2) ? L_WKV1 : L_WKV0) + ((kq & 1) * 8 + i) * 1024); } while (0)

    STAMP(0);
    unsigned epoch_v, epoch0 = 0;
    { gu32* e = (gu32*)(ws0 + OFF_SYNC); asm volatile("" : "+s"(e)); epoch_v = __hip_atomic_load(e, RLX_AGENT); }
    {
        PHASE_IDS();
        if (tid < 16) *(LAS unsigned*)(lds + (tid < 8 ? F_PAIR + tid * 4 : F_OCNT)) = 0u;
        if (j == tb && kq == 0 && lane < 4) __hip_atomic_store(cnt + cnt_word(j, lane, c), 0u, RLX_AGENT);
        if (j == 0 && tid == 4) __hip_atomic_store((gu32*)(ws + OFF_PREPC) + c * CW, 0u, RLX_AGENT);
        if (c == 0 && j == 0 && tid == 5) __hip_atomic_store(tmo, 0u, RLX_AGENT);
    }

#pragma unroll 1
    for (int L = 0; L < 2; ++L) {
        u32x4 qown; float q2own;
        {
            PHASE_IDS();
            const bool kvm = j < 2;
            const bool kvteam = j == tb;
            if (kvm && kvteam) {
                __builtin_amdgcn_s_setprio(2);
                if (L == 0) REQ_WKV();
            } else {
                const int row = lane >> 3, pc = lane & 7;
                const uint16_t* src = kp->wq + (size_t)(64 * w + row) * D + 64 * j + (((pc >> 2) ^ ((row >> 1) & 1)) * 32 + (pc & 3) * 8);
                if (L == 0) {
#pragma unroll
                    for (int i = 0; i < 4; ++i) glds16(src + i * 8 * D, lbase + L_WQ + (w * 8 + i) * 1024);
                }
#pragma unroll
                for (int i = 4; i < 8; ++i) glds16(src + i * 8 * D, lbase + L_WQ + (w * 8 + i) * 1024);
                if (kvm) {
                    const int w2 = w ^ 4;
                    const uint16_t* src2 = kp->wq + (size_t)(64 * w2 + row) * D + 64 * j + (((pc >> 2) ^ ((row >> 1) & 1)) * 32 + (pc & 3) * 8);
                    if (L == 0) {
#pragma unroll
                        for (int i = 0; i < 4; ++i) glds16(src2 + i * 8 * D, lbase + L_WQ + (w2 * 8 + i) * 1024);
                    }
#pragma unroll
                    for (int i = 4; i < 8; ++i) glds16(src2 + i * 8 * D, lbase + L_WQ + (w2 * 8 + i) * 1024);
                }
            }
            auto prep_dst = [&](int i) -> int {
                const int u = c * 6 + (j - 2);
                if (u < 43) return OFF_W1 + (u * KSD + 4 * w + i) * 1024;
                const int f = ((u - 43) * 8 + w) + i * 1192;
                if (i >= 2 || f >= 1920) return -1;
                return f < 512 ? OFF_WO + f * 1024 : OFF_W2 + (f - 512) * 1024;
            };
            u32x4 xa[8] = {};
            if (L == 0) {
                const rsrc_t rx = __builtin_amdgcn_make_buffer_rsrc((void*)kp->x, 0, S * D * 2, 0x00020000);
#pragma unroll
                for (int i = 0; i < 8; ++i) xa[i] = ld_plain(rx, r * 1024 + h * 16, (64 * c + 32 * tb) * 1024 + (kq * 8 + i) * 32);
                SBAR();
                STAMP(2);
                epoch0 = __builtin_amdgcn_readfirstlane(epoch_v); asm volatile("" : "+s"(epoch0)); epoch = epoch0;
                if (!kvm || kvteam) WAITVM(8);
            } else {
                if (tid == (kvm ? 256 * j : 0)) poll_ge(cnt + cnt_word(0, CNT_X2, c), 8u, tmo);
                __builtin_amdgcn_fence(__ATOMIC_ACQUIRE, "wavefront");
                if (!kvm || kvteam) WAITVM(0);
            }
            if (!(L == 0 && kvm)) LDS_BARRIER();
            STAMP(1 + L * 32);
            if (L == 1) {
#pragma unroll
                for (int i = 0; i < 8; ++i) xa[i] = ld_sc1(rw, vl, OFF_X2 + (tbg * KSD + kq * 8 + i) * 1024);
                SBAR();
            }
            LAS f32x2* st = (LAS f32x2*)(lds + L_STAT);
            LAS float* cst = (LAS float*)(lds + L_CS);
            const int tq = (lane & 15) >> 2, tp = lane & 3, tg = (lane >> 4) & 1;
            f32x16 aA = {}, aB = {};
            auto store_stats = [&]() {
                float s1 = 0.f, s2 = 0.f;
#pragma unroll
                for (int i = 0; i < 8; ++i) stats_acc(xa[i], s1, s2);
                s1 = half_sum(s1); s2 = half_sum(s2);
                if (h == 0) st[(tb * 8 + kq) * 32 + r] = (f32x2){s1, s2};
            };
            auto do_q = [&]() {
                u32x4 fa[8], fb[8];
                float csA = 0.f, csB = 0.f;
                const int sw = (tq >> 1) & 1, col = (tg ^ (kq & 1)) * 32 + tp * 8;
                const int a0 = L_WQ + (kq * 128 + 8 * h + tq) * 128 + col;
                const int aa = a0 + (((kq >> 1) ^ sw) * 64), ab = a0 + (((kq >> 1) ^ 1 ^ sw) * 64);
#pragma unroll
                for (int i = 0; i < 8; ++i) { fa[i] = tr_frag(lds, aa + i * 2048, 512); fb[i] = tr_frag(lds, ab + i * 2048, 512); }
                SBAR();
#pragma unroll
                for (int i = 0; i < 8; ++i) { csA = frag_sum(fa[i], csA); csB = frag_sum(fb[i], csB); }
                csA = half_sum(csA); csB = half_sum(csB);
                if (TB == 0 && h == 0) { const int n = r ^ ((kq & 1) << 4); cst[kq * 96 + (kq >> 1) * 32 + n] = csA; cst[kq * 96 + ((kq >> 1) ^ 1) * 32 + n] = csB; }
                SBAR();
#pragma unroll
                for (int i = 0; i < 8; ++i) { aA = mfma(fa[i], xa[i], aA); aB = mfma(fb[i], xa[i], aB); }
                PIN2(aA, aB);
            };
            if (kvm) {
                if (kvteam) {
                    u32x4 wk[8];
                    const int a = ((kq & 2) ? L_WKV1 : L_WKV0) + ((kq & 1) * 128 + 8 * h + tq) * 64 + tg * 32 + tp * 8;
#pragma unroll
                    for (int i = 0; i < 8; ++i) wk[i] = tr_frag(lds, a + i * 1024, 256);
                    SBAR();
                    float cs = 0.f;
#pragma unroll
                    for (int i = 0; i < 8; ++i) cs = frag_sum(wk[i], cs);
                    cs = half_sum(cs);
                    if (h == 0) cst[kq * 96 + 64 + r] = cs;
                    SBAR();
                    f32x16 ak = {}, av = {};
#pragma unroll
                    for (int i = 0; i < 8; ++i) { ak = mfma(wk[i], xa[i], ak); av = mfma(xa[i], wk[i], av); }
                    store_stats();
                    if (kq == 3 && lane == 0) *(LAS unsigned*)(lds + F_KVCNT) = 0u;
                    const int o = L_KVRED + kq * 4096;
                    lds_stf(lds, o + vl, (f32x4){ak[0], ak[1], ak[2], ak[3]}); lds_stf(lds, o + 1024 + vl, (f32x4){ak[4], ak[5], ak[6], ak[7]});
                    if (r >= 16) {
#pragma unroll
                        for (int g = 0; g < 4; ++g) lds_stf(lds, o + 2048 + g * 512 + (h * 16 + r - 16) * 16, (f32x4){av[4 * g], av[4 * g + 1], av[4 * g + 2], av[4 * g + 3]});
                    }
                } else WAITVM(8);
                LDS_BARRIER();
                if (kvteam && kq < 3) {
                    f32x4 a = {}, e = {}, ta[4], te[4];
                    float t1, t2, mu, rstd;
                    if (kq == 0) {
                        f32x4 k0 = {}, k1 = {}, u0[4], u1[4];
#pragma unroll
                        for (int i = 0; i < 4; ++i) { ta[i] = lds_ldf(lds, L_KVRED + i * 4096 + vl); te[i] = lds_ldf(lds, L_KVRED + i * 4096 + 1024 + vl);
                                                      u0[i] = lds_ldf(lds, L_CS + (i * 96 + 64 + 4 * h) * 4); u1[i] = lds_ldf(lds, L_CS + (i * 96 + 64 + 8 + 4 * h) * 4); }
                        stat_sums<4>(lds, tb * 8, r, t1, t2); ln_from_sums(t1, t2, mu, rstd);
#pragma unroll
                        for (int i = 0; i < 4; ++i) { a += ta[i]; e += te[i]; k0 += u0[i]; k1 += u1[i]; }
#pragma unroll
                        for (int i = 0; i < 4; ++i) { a[i] = (a[i] - mu * k0[i]) * rstd; e[i] = (e[i] - mu * k1[i]) * rstd; }
                        st_sc1((u32x4){pk(a[0], a[1]), pk(a[2], a[3]), pk(e[0], e[1]), pk(e[2], e[3])}, rw, vl, OFF_KF + L * 65536 + tbg * 1024);
                        float n2 = (a[0] * a[0] + a[1] * a[1]) + (a[2] * a[2] + a[3] * a[3]) + (e[0] * e[0] + e[1] * e[1]) + (e[2] * e[2] + e[3] * e[3]);
                        n2 = wave_max(half_sum(n2));
                        if (lane == 0) *(LAS float*)(lds + F_N2) = n2;
                    } else {
                        const int g = kq - 1, lc = (h * 16 + (r & 15)) * 16;
                        float csv = 0.f, uc[4];
#pragma unroll
                        for (int i = 0; i < 4; ++i) { ta[i] = lds_ldf(lds, L_KVRED + i * 4096 + 2048 + (2 * g) * 512 + lc); te[i] = lds_ldf(lds, L_KVRED + i * 4096 + 2048 + (2 * g + 1) * 512 + lc);
                                                      uc[i] = cst[i * 96 + 64 + 16 + (r & 15)]; }
                        stat_sums<4>(lds, tb * 8, r, t1, t2); ln_from_sums(t1, t2, mu, rstd);
#pragma unroll
                        for (int i = 0; i < 4; ++i) { a += ta[i]; e += te[i]; csv += uc[i]; }
#pragma unroll
                        for (int i = 0; i < 8; ++i) {
                            const int tok = 16 * g + (i & 3) + 8 * (i >> 2) + 4 * h;
                            const float m2 = __uint_as_float(__builtin_amdgcn_ds_bpermute(tok * 4, __float_as_uint(mu))), r2 = __uint_as_float(__builtin_amdgcn_ds_bpermute(tok * 4, __float_as_uint(rstd)));
                            if (i < 4) a[i] = (a[i] - m2 * csv) * r2; else e[i - 4] = (e[i - 4] - m2 * csv) * r2;
                        }
                        if (r >= 16) st_sc1((u32x4){pk(a[0], a[1]), pk(a[2], a[3]), pk(e[0], e[1]), pk(e[2], e[3])}, rw, h * 256 + (r - 16) * 16, OFF_VF + L * 65536 + (2 * tbg + g) * 512);
                    }
                }
                if (kvteam && kq < 3) {
                    DRAIN();
                    unsigned old = 0;
                    if (lane == 0) old = __hip_atomic_fetch_add((LAS unsigned*)(lds + F_KVCNT), 1u, __ATOMIC_RELAXED, __HIP_MEMORY_SCOPE_WORKGROUP);
                    if (__builtin_amdgcn_readfirstlane(old) == 2u && lane < 32)
                        __hip_atomic_store((gu64*)(ws + OFF_KVFL) + (L * 32 + lane) * 64 + 2 * c + j,
                                           ((unsigned long long)(epoch + 1u + L) << 32) | __float_as_uint(*(volatile LAS float*)(lds + F_N2)), RLX_AGENT);
                }
                __builtin_amdgcn_s_setprio(0);
                STAMP(3 + L * 32);
                do_q();
                if (!kvteam) store_stats();
            } else {
                do_q(); store_stats();
                STAMP(3 + L * 32);
            }
            if (L == 0 && !kvm) {
#pragma unroll
                for (int i = 0; i < 4; ++i) {
                    const int d = prep_dst(i);
                    if (d < 0) continue;
                    const uint16_t* src; int ld, cb, s;
                    if (d >= OFF_W2) { const int g = (d - OFF_W2) >> 10; src = kp->w2; ld = D; cb = g / KSFP; s = min(g % KSFP, KSF - 1); }
                    else if (d >= OFF_W1) { const int g = (d - OFF_W1) >> 10; src = kp->w1; ld = F; cb = g >> 5; s = g & 31; }
                    else { const int g = (d - OFF_WO) >> 10; src = kp->wo; ld = D; cb = g >> 5; s = g & 31; }
                    glds16(src + (size_t)(16 * s + (lane >> 2)) * ld + 32 * cb + (lane & 3) * 8, lbase + L_PREP + (w * 4 + i) * 1024);
                }
            }
            STAMP(4 + L * 32);
            float q[8];
            reduce4(lds, kvm ? (kvteam ? 0 : 131072) : (TB == 0 ? 32768 : 131072), aA, aB, kq, lane, q);
            STAMP(5 + L * 32);
            float t1, t2;
            f32x4 cs0 = {}, cs1 = {}, u0[4], u1[4];
#pragma unroll
            for (int i = 0; i < 4; ++i) { u0[i] = lds_ldf(lds, L_CS + (i * 96 + 16 * kq + 4 * h) * 4); u1[i] = lds_ldf(lds, L_CS + (i * 96 + 16 * kq + 8 + 4 * h) * 4); }
            stat_sums<4>(lds, tb * 8, r, t1, t2);
#pragma unroll
            for (int i = 0; i < 4; ++i) { cs0 += u0[i]; cs1 += u1[i]; }
            float mu, rstd; ln_from_sums(t1, t2, mu, rstd);
            float n2 = 0.f;
#pragma unroll
            for (int i = 0; i < 4; ++i) { q[i] = (q[i] - mu * cs0[i]) * (rstd * QSCALE); q[4 + i] = (q[4 + i] - mu * cs1[i]) * (rstd * QSCALE); }
#pragma unroll
            for (int i = 0; i < 8; ++i) n2 += q[i] * q[i];
            q2own = half_sum(n2);
            qown = (u32x4){pk(q[0], q[1]), pk(q[2], q[3]), pk(q[4], q[5]), pk(q[6], q[7])};
            lds_st(lds, L_XCHQ + w * 1024 + vl, qown);
            *(LAS float*)(lds + L_STAT + (tb * 8 + 4 + kq) * 256 + lane * 4) = q2own;
            if (L == 0 && !kvm) {
                WAITVM(0);
                float cs = 0.f;
#pragma unroll
                for (int i = 0; i < 4; ++i) {
                    const int d = prep_dst(i);
                    if (d < 0) continue;
                    u32x4 fr = tr_frag(lds, L_PREP + (w * 4 + i) * 1024 + (8 * h + tq) * 64 + tg * 32 + tp * 8, 256);
                    if (d >= OFF_W2 && ((d - OFF_W2) >> 10) % KSFP >= KSF) fr = (u32x4){0u, 0u, 0u, 0u};
                    st_sc1(fr, rw, vl, d);
                    cs = frag_sum(fr, cs);
                }
                const int u = c * 6 + (j - 2);
                if (u < 43) {
                    cs = half_sum(cs);
                    LAS float* t = (LAS float*)(lds + L_WQ);
                    if (h == 0) t[w * 32 + r] = cs;
                    LDS_BARRIER();
                    if (tid < 32) { float v = 0.f, pv[8];
#pragma unroll
                        for (int i = 0; i < 8; ++i) pv[i] = t[i * 32 + tid];
                        SBAR();
#pragma unroll
                        for (int i = 0; i < 8; ++i) v += pv[i];
                        __hip_atomic_store((gu32*)(ws + OFF_CS1 + (32 * u + tid) * 4), __float_as_uint(v), RLX_AGENT); }
                }
                DRAIN();
            }
            STAMP(6 + L * 32);
        }
        u32x4 woA[8], woB[8];
        u32x4 w1f[32];
        {
            PHASE_IDS();
            const float km2 = wait_flags(tid, lds, (gu64*)(ws + OFF_KVFL) + (L * 32 + c) * 64, epoch + 1u + L, tmo);
            if (L == 0 && j >= 2 && tid == 0) __hip_atomic_fetch_add((gu32*)(ws + OFF_PREPC) + c * CW, 1u, RLX_AGENT);
            STAMP(7 + L * 32);
            {
                const unsigned char* src = ws + (TB == 0 ? OFF_KF : OFF_VF) + L * 65536 + ((w >> 1) & 1) * 32768 + vl;
                const unsigned dst = lbase + (TB == 0 ? L_K : L_V) + ((w >> 1) & 1) * 32768;
                const int t0 = (w & 1) * 6;
#pragma unroll
                for (int i = 0; i < 6; ++i) glds16_sc1(src + (t0 + i) * 1024, dst + (t0 + i) * 1024);
            }
            { unsigned one2 = (!ZROWS || (tid & 31) == 0) ? 0x3f803f80u : 0u; asm volatile("" : "+v"(one2)); if (tid < 272) lds_st(lds, L_ONES + tid * 16, (u32x4){one2, one2, one2, one2}); }
            const u32x4 qpar = lds_ld(lds, L_XCHQ + (w ^ 1) * 1024 + vl);
            const float q2par = *(LAS float*)(lds + L_STAT + (tb * 8 + 4 + (kq ^ 1)) * 256 + lane * 4);
            float ma = __builtin_amdgcn_sqrtf(q2own * km2) * 1.02f, mb = __builtin_amdgcn_sqrtf(q2par * km2) * 1.02f;
            const bool unsafe = __any(!(ma <= SAFE_BOUND) || !(mb <= SAFE_BOUND));
            {
                LAS unsigned* fl = (LAS unsigned*)(lds + F_UNSAFE);
                if (lane == 0) fl[w] = unsafe ? 1u : 0u;
            }
            STAMP(8 + L * 32);
            WAITVM(0);
            LDS_BARRIER();
            {
                LAS unsigned* fl = (LAS unsigned*)(lds + F_UNSAFE);
                const u32x4 f0 = *(LAS u32x4*)fl, f1 = *(LAS u32x4*)(fl + 4);
                if (__builtin_amdgcn_readfirstlane(f0[0] | f0[1] | f0[2] | f0[3] | f1[0] | f1[1] | f1[2] | f1[3])) {
                    {
                        const unsigned char* src = ws + (TB == 0 ? OFF_KF : OFF_VF) + L * 65536 + ((w >> 1) & 1) * 32768 + (12 + (w & 1) * 10) * 1024 + vl;
                        const unsigned dst = lbase + (TB == 0 ? L_K : L_V) + ((w >> 1) & 1) * 32768 + (12 + (w & 1) * 10) * 1024;
#pragma unroll 1
                        for (int i = 0; i < 10; ++i) glds16_sc1(src + i * 1024, __builtin_amdgcn_readfirstlane(dst + i * 1024));
                    }
                    DRAIN();
                    LDS_BARRIER();
                    if (unsafe) {
                        float xa = -INFINITY, xb = -INFINITY;
#pragma unroll 1
                        for (int t = 0; t < 64; ++t) {
                            const u32x4 kf = lds_ld(lds, L_K + t * 1024 + vl);
                            const f32x16 sa = mfma(kf, qown, (f32x16){}), sb = mfma(kf, qpar, (f32x16){});
#pragma unroll
                            for (int i = 0; i < 16; ++i) { xa = fmaxf(xa, sa[i]); xb = fmaxf(xb, sb[i]); }
                        }
                        ma = half_max(xa); mb = half_max(xb);
                    }
                }
            }
            STAMP(9 + L * 32);
            const int kh = kq & 1;
            const bool real = r < 16;
            const unsigned kaddr = L_K + kh * 32768 + vl;
            const unsigned vaddr = real ? (unsigned)(L_V + kh * 32768 + h * 256 + r * 16) : (unsigned)(L_ONES + ((r >= 20 && r < 28) ? 64 : (ZROWS && r != 16) ? 16 : 0));
            if (PRIO >= 1 && TB == 1) __builtin_amdgcn_s_setprio(1);
            AttnState st;
            attn_begin(st, lds, qown, qpar, ma, mb, kaddr, vaddr, real ? 4096u : 0u);
            attn_first(st, (unsigned long long)(uintptr_t)(ws + (TB == 0 ? OFF_KF : OFF_VF) + L * 65536 + ((w >> 1) & 1) * 32768 + (12 + (w & 1) * 10) * 1024),
                       lbase + (TB == 0 ? L_K : L_V) + ((w >> 1) & 1) * 32768 + (12 + (w & 1) * 10) * 1024, (unsigned)vl);
            STAMP(10 + L * 32);
            if (L == 0) {
                unsigned spins = 0;
                while (!__all(__hip_atomic_load((gu32*)(ws + OFF_PREPC) + (lane & 31) * CW, RLX_AGENT) >= 6u)) {
                    if (++spins > SPIN_CAP) { if (lane == 0) __hip_atomic_store(tmo, 1u, RLX_AGENT); break; }
                }
                __builtin_amdgcn_fence(__ATOMIC_ACQUIRE, "wavefront");
            }
            STAMP(11 + L * 32);
            if (PRIO == 1 && TB == 1) __builtin_amdgcn_s_setprio(0);
            Prefetch pf;
            { W1_IDS();
              const unsigned long long wsa = (unsigned long long)(uintptr_t)ws;
              const u32x4 rs = {(unsigned)wsa, (unsigned)(wsa >> 32) & 0xffffu, (unsigned)WS_TOTAL, 0x00020000u};
              attn_second<TB>(st, pf, rs, (unsigned)vw, (unsigned)vl, OFF_WO + (cbA * KSD + kq * 8) * 1024, OFF_WO + (cbB * KSD + kq * 8) * 1024, OFF_W1 + (cbg1 * KSD + par) * 1024); }
            STAMP(12 + L * 32);
            if (PRIO == 2 && TB == 1) __builtin_amdgcn_s_setprio(0);
            const f32x16 oa = st.oa, ob = st.ob;
            const int xo = L_XCHO + w * 2304;
            lds_stf(lds, xo + vl, (f32x4){ob[0], ob[1], ob[2], ob[3]}); lds_stf(lds, xo + 1024 + vl, (f32x4){ob[4], ob[5], ob[6], ob[7]});
            *(LAS float*)(lds + xo + 2048 + lane * 4) = ZROWS ? half_sum(ob[8]) : ob[8];
            asm volatile("s_waitcnt lgkmcnt(0)" ::: "memory");
            *(volatile LAS unsigned*)(lds + F_PAIR + w * 4) = L + 1u;
            while (*(volatile LAS unsigned*)(lds + F_PAIR + (w ^ 1) * 4) != L + 1u) { }
            asm volatile("" ::: "memory");
            const int xi = L_XCHO + (w ^ 1) * 2304;
            const f32x4 e0 = lds_ldf(lds, xi + vl), e1 = lds_ldf(lds, xi + 1024 + vl);
            const float lpar = *(LAS float*)(lds + xi + 2048 + lane * 4);
            SBAR();
            const float rl = __builtin_amdgcn_rcpf((ZROWS ? half_sum(oa[8]) : oa[8]) + lpar);
            const u32x4 of = {pk((oa[0] + e0[0]) * rl, (oa[1] + e0[1]) * rl), pk((oa[2] + e0[2]) * rl, (oa[3] + e0[3]) * rl),
                              pk((oa[4] + e1[0]) * rl, (oa[5] + e1[1]) * rl), pk((oa[6] + e1[2]) * rl, (oa[7] + e1[3]) * rl)};
            st_sc1(swap_order(of), rw, vl, OFF_O + L * ACT_BYTES + (tbg * KSD + sown) * 1024);
            if (TB == 0) asm volatile("s_waitcnt vmcnt(0)" : "+{v[152:167]}"(pf.a0), "+{v[168:183]}"(pf.a1), "+{v[184:199]}"(pf.b0), "+{v[200:215]}"(pf.b1) :: "memory");
            else asm volatile("s_waitcnt vmcnt(0)" : "+{v[152:167]}"(pf.a0), "+{v[168:183]}"(pf.a1), "+{v[184:199]}"(pf.b0), "+{v[200:215]}"(pf.b1), "+{v[216:231]}"(pf.w0), "+{v[232:247]}"(pf.w1) :: "memory");
            unsigned old = 0;
            if (lane == 0) old = __hip_atomic_fetch_add((LAS unsigned*)(lds + F_OCNT), 1u, __ATOMIC_RELAXED, __HIP_MEMORY_SCOPE_WORKGROUP);
            if (__builtin_amdgcn_readfirstlane(old) == 8u * L + 7u && lane == 0) __hip_atomic_fetch_add(cnt + cnt_word(L, CNT_O, c), 1u, RLX_AGENT);
            STAMP(13 + L * 32);
            woA[0] = quad<0>(pf.a0); woA[1] = quad<1>(pf.a0); woA[2] = quad<2>(pf.a0); woA[3] = quad<3>(pf.a0);
            woA[4] = quad<0>(pf.a1); woA[5] = quad<1>(pf.a1); woA[6] = quad<2>(pf.a1); woA[7] = quad<3>(pf.a1);
            woB[0] = quad<0>(pf.b0); woB[1] = quad<1>(pf.b0); woB[2] = quad<2>(pf.b0); woB[3] = quad<3>(pf.b0);
            woB[4] = quad<0>(pf.b1); woB[5] = quad<1>(pf.b1); woB[6] = quad<2>(pf.b1); woB[7] = quad<3>(pf.b1);
            if (TB == 1) {
                w1f[0] = quad<0>(pf.w0); w1f[1] = quad<1>(pf.w0); w1f[2] = quad<2>(pf.w0); w1f[3] = quad<3>(pf.w0);
                w1f[4] = quad<0>(pf.w1); w1f[5] = quad<1>(pf.w1); w1f[6] = quad<2>(pf.w1); w1f[7] = quad<3>(pf.w1);
            }
            if (TB == 0) { W1_IDS();
#pragma unroll
                for (int i = 0; i < W1_EARLY; ++i) { w1f[i] = ld_sc1(rw, vl, OFF_W1 + (cbg1 * KSD + i) * 1024); SBAR(); __builtin_amdgcn_s_sleep(3); SBAR(); }
            }
        }
        u32x4 resid;
        {
            PHASE_IDS();
            if (L == 0) { const rsrc_t rx = __builtin_amdgcn_make_buffer_rsrc((void*)kp->x, 0, S * D * 2, 0x00020000);
                          resid = ld_plain(rx, r * 1024 + h * 16, (64 * c + 32 * tb) * 1024 + sown * 32); }
            else resid = ld_sc1(rw, vl, OFF_X2 + (tbg * KSD + sown) * 1024);
            STAMP(14 + L * 32);
            wait_cnt(tid, cnt + cnt_word(L, CNT_O, c), 8u, tmo);
            STAMP(15 + L * 32);
            u32x4 xa[8];
#pragma unroll
            for (int i = 0; i < 8; ++i) xa[i] = ld_sc1(rw, vl, OFF_O + L * ACT_BYTES + (tbg * KSD + kq * 8 + i) * 1024);
            SBAR();
            f32x16 aA = {}, aB = {};
#pragma unroll
            for (int i = 0; i < 8; ++i) { aA = mfma(woA[i], xa[i], aA); aB = mfma(woB[i], xa[i], aB); }
            PIN2(aA, aB);
            SBAR();
            STAMP(16 + L * 32);
            {
#pragma unroll
                for (int i = 0; i < (TB == 0 ? 9 : 3); ++i) {
                    const int run = TB == 0 ? (i < 6 ? kq : kq + 4) : w, pc = TB == 0 ? (i < 6 ? i : i - 6) : 3 + i;
                    glds16_sc1(ws + OFF_W2 + ((2 * j + (run & 1)) * KSFP + (run >> 1) * 22 + W2_REG + pc) * 1024 + vl, lbase + L_W2A + (run * W2_A + pc) * 1024);
                }
            }
            STAMP(17 + L * 32);
            float o[8];
            reduce4(lds, tb * 24576, aA, aB, kq, lane, o);
            resid = swap_order(resid);
#pragma unroll
            for (int i = 0; i < 4; ++i) resid[i] = pk(bflo(resid[i]) + o[2 * i], bfhi(resid[i]) + o[2 * i + 1]);
            st_sc1(swap_order(resid), rw, vl, OFF_X1 + L * ACT_BYTES + (tbg * KSD + sown) * 1024);
            STAMP(18 + L * 32);
            publish(tid, cnt + cnt_word(L, CNT_X1, c));
            STAMP(19 + L * 32);
        }
        u32x4 w2A[W2_REG], w2B[W2_REG];
        {
            PHASE_IDS(); W1_IDS();
            if (w == 1 || w == 2 || w == 3) {
                const int col = min(32 * cb0 + tid - 64, F - 1);
                *(LAS float*)(lds + L_TAB + (tid - 64) * 4) = __uint_as_float(__builtin_amdgcn_raw_buffer_load_b32(rw, col * 4, OFF_CS1, AUX_SC1));
                *(LAS float*)(lds + L_TAB + 768 + (tid - 64) * 4) = bf2f(kp->b1[col]);
            }
            STAMP(20 + L * 32);
            wait_cnt(tid, cnt + cnt_word(L, CNT_X1, c), 8u, tmo);
            STAMP(21 + L * 32);
            const unsigned char* xsrc = ws + OFF_X1 + L * ACT_BYTES + (2 * c * KSD + 2 * w) * 1024 + vl;
#pragma unroll
            for (int i = 0; i < 4; ++i) glds16_sc1(xsrc + ((i >> 1) * KSD + (i & 1)) * 1024, lbase + L_ACT + ((i >> 1) * KSD + 2 * w + (i & 1)) * 1024);
            WAITVM(0);
            LDS_BARRIER();
#pragma unroll
            for (int i = 0; i < 4; ++i) glds16_sc1(xsrc + ((i >> 1) * KSD + 16 + (i & 1)) * 1024, lbase + L_ACT + ((i >> 1) * KSD + 16 + 2 * w + (i & 1)) * 1024);
            STAMP(22 + L * 32);
            f32x16 a0 = {}, a1 = {};
            float s1[2] = {0.f, 0.f}, s2[2] = {0.f, 0.f};
            const unsigned char* w2a_src = ws + OFF_W2 + ((2 * j + (w & 1)) * KSFP + (w >> 1) * 22 + W2_REG) * 1024 + vl;
            auto req = [&](int lo, int n) {
                if (lo == 0) {
                    if (n < 8) { if (TB == 0) w1f[W1_EARLY + n] = ld_sc1(rw, vl, OFF_W1 + (cbg1 * KSD + W1_EARLY + n) * 1024);
                                 else w1f[16 + n] = ld_sc1(rw, vl, OFF_W1 + (cbg1 * KSD + 16 + 2 * n + par) * 1024); }
                } else {
                    if (n < 5) glds16_sc1(w2a_src + (6 + n) * 1024, lbase + L_W2A + (w * W2_A + 6 + n) * 1024);
                    else if (n < 5 + W2_REG) w2A[n - 5] = ld_sc1(rw, vw, OFF_W2 + (cbA * KSFP + kq * 22 + n - 5) * 1024);
                    else if (n < 5 + 2 * W2_REG) w2B[n - 5 - W2_REG] = ld_sc1(rw, vw, OFF_W2 + (cbB * KSFP + kq * 22 + n - 5 - W2_REG) * 1024);
                }
            };
            u32x4 sv;
            auto stat_step = [&](int lo, int s) {
                if (s >= 1 && s <= 4) stats_acc(sv, s1[(s - 1) >> 1], s2[(s - 1) >> 1]);
                if (s < 4) sv = lds_ld(lds, L_ACT + ((s >> 1) * KSD + lo + 2 * w + (s & 1)) * 1024 + vl);
            };
            auto part = [&](int lo) {
                u32x4 b0[3], b1[3];
                if (TB == 0) {
#pragma unroll
                    for (int i = 0; i < 2; ++i) { b0[i] = lds_ld(lds, L_ACT + (lo + i) * 1024 + vl); b1[i] = lds_ld(lds, L_ACT + (KSD + lo + i) * 1024 + vl); }
#pragma unroll
                    for (int s = 0; s < 16; ++s) {
                        if (s + 2 < 16) { b0[(s + 2) % 3] = lds_ld(lds, L_ACT + (lo + s + 2) * 1024 + vl); b1[(s + 2) % 3] = lds_ld(lds, L_ACT + (KSD + lo + s + 2) * 1024 + vl); }
                        SBAR();
                        a0 = mfma(w1f[lo + s], b0[s % 3], a0); a1 = mfma(w1f[lo + s], b1[s % 3], a1);
                        PIN2(a0, a1);
                        req(lo, s); stat_step(lo, s);
                        SBAR();
                    }
                } else {
                    const int ab = L_ACT + (lo + par) * 1024 + vl;
#pragma unroll
                    for (int i = 0; i < 2; ++i) { b0[i] = lds_ld(lds, ab + 2 * i * 1024); b1[i] = lds_ld(lds, ab + (KSD + 2 * i) * 1024); }
#pragma unroll
                    for (int s = 0; s < 8; ++s) {
                        if (s + 2 < 8) { b0[(s + 2) % 3] = lds_ld(lds, ab + 2 * (s + 2) * 1024); b1[(s + 2) % 3] = lds_ld(lds, ab + (KSD + 2 * (s + 2)) * 1024); }
                        SBAR();
                        a0 = mfma(w1f[lo + s], b0[s % 3], a0); a1 = mfma(w1f[lo + s], b1[s % 3], a1);
                        PIN2(a0, a1);
                        if (lo == 0) req(lo, s); else { req(lo, 2 * s); req(lo, 2 * s + 1); }
                        stat_step(lo, s);
                        SBAR();
                    }
                }
            };
            part(0);
            STAMP(23 + L * 32);
            WAITVM(8);
            LDS_BARRIER();
            if (TB == 0) asm volatile("" : "+v"(w1f[24]), "+v"(w1f[25]), "+v"(w1f[26]), "+v"(w1f[27]), "+v"(w1f[28]), "+v"(w1f[29]), "+v"(w1f[30]), "+v"(w1f[31]));
            else asm volatile("" : "+v"(w1f[16]), "+v"(w1f[17]), "+v"(w1f[18]), "+v"(w1f[19]), "+v"(w1f[20]), "+v"(w1f[21]), "+v"(w1f[22]), "+v"(w1f[23]));
            part(16);
            {
                LAS f32x2* st = (LAS f32x2*)(lds + L_STAT);
#pragma unroll
                for (int t = 0; t < 2; ++t) { const float u1 = half_sum(s1[t]), u2 = half_sum(s2[t]); if (h == 0) st[(t * 8 + w) * 32 + r] = (f32x2){u1, u2}; }
            }
            STAMP(24 + L * 32);
            if (TB == 1) {
                const int xo = L_ACT + kq * 4096 + vl;
#pragma unroll
                for (int g = 0; g < 4; ++g) {
                    const f32x4 v = par ? (f32x4){a0[4 * g], a0[4 * g + 1], a0[4 * g + 2], a0[4 * g + 3]} : (f32x4){a1[4 * g], a1[4 * g + 1], a1[4 * g + 2], a1[4 * g + 3]};
                    lds_stf(lds, xo + g * 1024, v);
                }
            }
            LDS_BARRIER();
            if (TB == 1) {
                const int xi = L_ACT + (kq ^ 1) * 4096 + vl;
                f32x4 v[4];
#pragma unroll
                for (int g = 0; g < 4; ++g) v[g] = lds_ldf(lds, xi + g * 1024);
                SBAR();
#pragma unroll
                for (int g = 0; g < 4; ++g)
#pragma unroll
                    for (int i = 0; i < 4; ++i) a0[4 * g + i] = (par ? a1[4 * g + i] : a0[4 * g + i]) + v[g][i];
                PIN2(a0, a1);
            }
            STAMP(25 + L * 32);
            if (d_active) {
                auto finish = [&](const f32x16& acc, int t) {
                    f32x4 tcs[4], tbb[4];
#pragma unroll
                    for (int g = 0; g < 4; ++g) { tcs[g] = lds_ldf(lds, L_TAB + (32 * cb1 + 8 * g + 4 * h) * 4); tbb[g] = lds_ldf(lds, L_TAB + 768 + (32 * cb1 + 8 * g + 4 * h) * 4); }
                    float t1, t2; stat_sums<8>(lds, t * 8, r, t1, t2);
                    float mu, rstd; ln_from_sums(t1, t2, mu, rstd);
                    const float nmr = -mu * rstd;
                    f32x2 x[8], e[8];
#pragma unroll
                    for (int i = 0; i < 8; ++i) {
                        const f32x2 a2 = {acc[2 * i], acc[2 * i + 1]}, c2 = {tcs[i >> 1][2 * (i & 1)], tcs[i >> 1][2 * (i & 1) + 1]}, b2 = {tbb[i >> 1][2 * (i & 1)], tbb[i >> 1][2 * (i & 1) + 1]};
                        x[i] = __builtin_elementwise_fma(a2, (f32x2){rstd, rstd}, __builtin_elementwise_fma(c2, (f32x2){nmr, nmr}, b2));
                    }
                    PIN8(x);
                    constexpr float G1 = -2.f * 0.7978845608028654f * 1.4426950408889634f, G3 = G1 * 0.044715f;
#pragma unroll
                    for (int i = 0; i < 8; ++i) e[i] = x[i] * __builtin_elementwise_fma(x[i] * x[i], (f32x2){G3, G3}, (f32x2){G1, G1});
                    PIN8(e);
#pragma unroll
                    for (int i = 0; i < 8; ++i) { e[i][0] = __builtin_amdgcn_exp2f(e[i][0]); e[i][1] = __builtin_amdgcn_exp2f(e[i][1]); }
                    PIN8(e);
#pragma unroll
                    for (int i = 0; i < 8; ++i) e[i] = e[i] + (f32x2){1.f, 1.f};
                    PIN8(e);
#pragma unroll
                    for (int i = 0; i < 8; ++i) { e[i][0] = __builtin_amdgcn_rcpf(e[i][0]); e[i][1] = __builtin_amdgcn_rcpf(e[i][1]); }
                    PIN8(e);
                    unsigned hp[8];
#pragma unroll
                    for (int i = 0; i < 8; ++i) { const f32x2 y = x[i] * e[i]; hp[i] = pk(y[0], y[1]); }
                    const int so = OFF_H + L * H_BYTES + ((2 * c + t) * KSF + 2 * cbg1) * 1024;
                    st_sc1(swap_order((u32x4){hp[0], hp[1], hp[2], hp[3]}), rw, vl, so);
                    st_sc1(swap_order((u32x4){hp[4], hp[5], hp[6], hp[7]}), rw, vl, so + 1024);
                };
                if (TB == 0) { finish(a0, 0); finish(a1, 1); } else finish(a0, par);
            }
            STAMP(26 + L * 32);
            publish(tid, cnt + cnt_word(L, CNT_H, c));
            STAMP(27 + L * 32);
        }
        {
            PHASE_IDS();
#pragma unroll
            for (int i = 0; i < 8; ++i) { const int f = 8 * w + i;
                glds16_sc1(ws + OFF_W2 + ((2 * j + (w & 1)) * KSFP + (w >> 1) * 22 + W2_REG + W2_A + i) * 1024 + vl, lbase + L_W2B + f * 1024); }
            const u32x2 bb0 = *(const GAS u32x2*)(kp->b2 + 16 * sown + 4 * h), bb1 = *(const GAS u32x2*)(kp->b2 + 16 * sown + 8 + 4 * h);
            if (tid == 0) poll_ge(cnt + cnt_word(L, CNT_H, c), 8u, tmo);
            __builtin_amdgcn_fence(__ATOMIC_ACQUIRE, "wavefront");
            WAITVM(0);
            LDS_BARRIER();
            STAMP(28 + L * 32);
            u32x4 ha[22];
            const int hso = OFF_H + L * H_BYTES + tbg * KSF * 1024;
            constexpr int EWIN = EWIN_;
#pragma unroll
            for (int i = 0; i < EWIN; ++i) ha[i] = ld_sc1(rw, vl, hso + min(kq * 22 + i, KSF - 1) * 1024);
            SBAR();
            f32x16 aA = {}, aB = {};
            const int aA0 = L_W2A + ((kq * 2 + (kq >> 1)) * W2_A) * 1024 + vw, aB0 = L_W2A + ((kq * 2 + ((kq >> 1) ^ 1)) * W2_A) * 1024 + vw;
            const int bA0 = L_W2B + ((kq * 2 + (kq >> 1)) * W2_B) * 1024 + vw, bB0 = L_W2B + ((kq * 2 + ((kq >> 1) ^ 1)) * W2_B) * 1024 + vw;
            u32x4 la[3], lb[3];
#pragma unroll
            for (int i = 0; i < 22; ++i) {
                if (i + EWIN < 22) ha[i + EWIN] = ld_sc1(rw, vl, hso + min(kq * 22 + i + EWIN, KSF - 1) * 1024);
                const int n = i + 2;
                if (n >= W2_REG && n < W2_REG + W2_A) { la[n % 3] = lds_ld(lds, aA0 + (n - W2_REG) * 1024); lb[n % 3] = lds_ld(lds, aB0 + (n - W2_REG) * 1024); }
                else if (n >= W2_REG + W2_A && n < 22) { la[n % 3] = lds_ld(lds, bA0 + (n - W2_REG - W2_A) * 1024); lb[n % 3] = lds_ld(lds, bB0 + (n - W2_REG - W2_A) * 1024); }
                SBAR();
                if (i < W2_REG) { aA = mfma(w2A[i], ha[i], aA); aB = mfma(w2B[i], ha[i], aB); }
                else { aA = mfma(la[i % 3], ha[i], aA); aB = mfma(lb[i % 3], ha[i], aB); }
                PIN2(aA, aB);
                SBAR();
            }
            STAMP(29 + L * 32);
            LDS_BARRIER();
            if (L == 0) {
                const int row = lane >> 3, pc = lane & 7;
                const uint16_t* src = kp->wq + (size_t)(64 * w + row) * D + 64 * j + (((pc >> 2) ^ ((row >> 1) & 1)) * 32 + (pc & 3) * 8);
                const bool mine = !(j == tb);
                const uint16_t* src2 = kp->wq + (size_t)(64 * (w ^ 4) + row) * D + 64 * j + (((pc >> 2) ^ ((row >> 1) & 1)) * 32 + (pc & 3) * 8);
                if (!mine) REQ_WKV();
                if (mine) {
#pragma unroll
                    for (int i = 0; i < 4; ++i) glds16(src + i * 8 * D, lbase + L_WQ + (w * 8 + i) * 1024);
                    if (j < 2) {
#pragma unroll
                        for (int i = 0; i < 4; ++i) glds16(src2 + i * 8 * D, lbase + L_WQ + ((w ^ 4) * 8 + i) * 1024);
                    }
                }
            }
            float o[8];
            reduce4(lds, tb * 24576, aA, aB, kq, lane, o);
#pragma unroll
            for (int i = 0; i < 2; ++i) { o[2 * i] += bflo(bb0[i]); o[2 * i + 1] += bfhi(bb0[i]); o[4 + 2 * i] += bflo(bb1[i]); o[4 + 2 * i + 1] += bfhi(bb1[i]); }
#pragma unroll
            for (int i = 0; i < 4; ++i) resid[i] = pk(bflo(resid[i]) + o[2 * i], bfhi(resid[i]) + o[2 * i + 1]);
            STAMP(30 + L * 32);
            const u32x4 xo = swap_order(resid);
            if (L == 0) {
                st_sc1(xo, rw, vl, OFF_X2 + (tbg * KSD + sown) * 1024);
                publish(tid, cnt + cnt_word(0, CNT_X2, c));
            } else {
                *(GAS u32x4*)(kp->out + (size_t)(64 * c + 32 * tb + r) * D + 16 * sown + 8 * h) = xo;
            }
            STAMP(31 + L * 32);
        }
    }
    { unsigned e = epoch0; asm volatile("" : "+s"(e)); if (blockIdx.x == 0 && threadIdx.x == 0) __hip_atomic_store((gu32*)(ws0 + OFF_SYNC), e + 2u, RLX_AGENT); }
}

__global__ void __launch_bounds__(512, 2) main_kernel(Params p) {
    extern __shared__ __attribute__((aligned(16))) char lds_dyn[];
    LAS char* lds = (LAS char*)lds_dyn;
    asm volatile("" :: "s"(p.x), "s"(p.wq), "s"(p.wk), "s"(p.wv), "s"(p.w1), "s"(p.b2), "s"(p.out));
    if (__builtin_amdgcn_readfirstlane(threadIdx.x >> 8) == 0) run_layers<0>(p.ws, lds); else run_layers<1>(p.ws, lds);
}

}

extern "C" void kernel_launch(void* const* d_in, const int* in_sizes, int n_in, void* d_out, int out_size, void* d_ws, size_t ws_size, hipStream_t stream) {
    Params p;
    p.x = (const uint16_t*)d_in[0]; p.wq = (const uint16_t*)d_in[1]; p.wk = (const uint16_t*)d_in[2]; p.wv = (const uint16_t*)d_in[3]; p.wo = (const uint16_t*)d_in[4];
    p.w1 = (const uint16_t*)d_in[6]; p.b1 = (const uint16_t*)d_in[7]; p.w2 = (const uint16_t*)d_in[8]; p.b2 = (const uint16_t*)d_in[9];
    p.out = (uint16_t*)d_out; p.ws = (unsigned char*)d_ws;
    (void)hipFuncSetAttribute((const void*)main_kernel, hipFuncAttributeMaxDynamicSharedMemorySize, LDS_BYTES);
    hipLaunchKernelGGL(main_kernel, dim3(256), dim3(512), LDS_BYTES, stream, p);
}
```

```cpp
#include <hip/hip_runtime.h>
#include <hip/hip_bf16.h>
#include <stdint.h>

#define GAS __attribute__((address_space(1)))
#define LAS __attribute__((address_space(3)))
typedef __bf16 bf16x8 __attribute__((ext_vector_type(8)));
typedef __bf16 bf16x2 __attribute__((ext_vector_type(2)));
typedef float f32x16 __attribute__((ext_vector_type(16)));
typedef float f32x4 __attribute__((ext_vector_type(4)));
typedef float f32x2 __attribute__((ext_vector_type(2)));
typedef unsigned u32x4 __attribute__((ext_vector_type(4)));
typedef GAS unsigned gu32;
#define RLX_AGENT __ATOMIC_RELAXED, __HIP_MEMORY_SCOPE_AGENT

namespace {

constexpr int S = 2048, D = 512, F = 1376;
constexpr int KSD = 32;
constexpr int KSF = 86;
constexpr int KSFP = 88;
constexpr float LN_EPS = 1e-5f;
constexpr float QSCALE = 0.25f * 1.4426950408889634f;
constexpr float SAFE_BOUND = 50.f;

constexpr int CW = 64;
constexpr int OFF_CNT = 0;
constexpr int CNT_O = 0, CNT_X1 = 1, CNT_H = 2, CNT_X2 = 3;
__device__ __forceinline__ int cnt_word(int layer, int kind, int c) { return ((layer * 4 + kind) * 32 + c) * CW; }
constexpr int OFF_PREPC = 65536;
constexpr int OFF_SYNC = OFF_PREPC + 32 * 256;
constexpr int OFF_KVFL = OFF_SYNC + 1024;
constexpr int OFF_WO = OFF_KVFL + 2 * 32 * 512;
constexpr int OFF_W1 = OFF_WO + 16 * KSD * 1024;
constexpr int OFF_W2 = OFF_W1 + 43 * KSD * 1024;
constexpr int OFF_CS1 = OFF_W2 + 16 * KSFP * 1024;
constexpr int OFF_KF = (OFF_CS1 + 1408 * 4 + 1023) / 1024 * 1024;
constexpr int OFF_VF = OFF_KF + 2 * 65536;
constexpr int OFF_O = OFF_VF + 2 * 65536;
constexpr int ACT_BYTES = 64 * KSD * 1024;
constexpr int OFF_X1 = OFF_O + 2 * ACT_BYTES;
constexpr int OFF_X2 = OFF_X1 + 2 * ACT_BYTES;
constexpr int H_BYTES = 64 * KSF * 1024;
constexpr int OFF_H = OFF_X2 + ACT_BYTES;
constexpr int WS_TOTAL = OFF_H + 2 * H_BYTES;
constexpr unsigned SPIN_CAP = 1u << 15;

constexpr int L_K = 0, L_V = 65536, L_ONES = 131072, L_XCHO = 135424;
constexpr int L_WQ = 65536, L_WKV0 = 49152, L_WKV1 = 131072, L_PREP = 0, L_KVRED = 32768, L_XCHQ = 57344;
constexpr int L_ACT = 0, L_W2A = 65536, L_W2B = 0;
constexpr int L_STAT = 155648, L_TAB = 159744, L_CS = L_TAB, L_FLAG = 163584;
constexpr int F_UNSAFE = L_FLAG, F_KM2 = L_FLAG + 32, F_N2 = L_FLAG + 36, F_KVCNT = L_FLAG + 40, F_OCNT = L_FLAG + 44, F_PAIR = L_FLAG + 64;
constexpr int LDS_BYTES = 163840;
constexpr int W2_REG = 3, W2_A = 11, W2_B = 8;
constexpr int W1_EARLY = 24;

#define LR_C 0
#define LR_E 0
#define LR_W 0
#define NCH 4
#define TLB_WARM 0
#define W1_SLEEP 3
#define PRIO 1
#define ZROWS 1
#define EWIN_ 5
#define PROF 0
#define PROF_TID 0
constexpr int L_PROF = 161280;
constexpr int OFF_PROF = 64 << 20;
#define STAMP(i) do { } while (0)

struct Params {
    const uint16_t *x, *wq, *wk, *wv, *wo, *w1, *b1, *w2, *b2;
    uint16_t* out;
    unsigned char* ws;
};

__device__ __forceinline__ f32x16 mfma(u32x4 a, u32x4 b, f32x16 c) {
    return __builtin_amdgcn_mfma_f32_32x32x16_bf16(__builtin_bit_cast(bf16x8, a), __builtin_bit_cast(bf16x8, b), c, 0, 0, 0);
}
__device__ __forceinline__ unsigned pk(float a, float b) { f32x2 v = {a, b}; return __builtin_bit_cast(unsigned, __builtin_convertvector(v, bf16x2)); }
__device__ __forceinline__ float bflo(unsigned u) { return __uint_as_float(u << 16); }
__device__ __forceinline__ float bfhi(unsigned u) { return __uint_as_float(u & 0xffff0000u); }
__device__ __forceinline__ float bf2f(uint16_t v) { return __uint_as_float((unsigned)v << 16); }
__device__ __forceinline__ float dot2(unsigned a, unsigned b, float c) {
    return __builtin_amdgcn_fdot2_f32_bf16(__builtin_bit_cast(bf16x2, a), __builtin_bit_cast(bf16x2, b), c, false);
}
__device__ __forceinline__ float half_sum(float v) {
    auto r = __builtin_amdgcn_permlane32_swap(__float_as_uint(v), __float_as_uint(v), false, false);
    return __uint_as_float(r[0]) + __uint_as_float(r[1]);
}
__device__ __forceinline__ float half_max(float v) {
    auto r = __builtin_amdgcn_permlane32_swap(__float_as_uint(v), __float_as_uint(v), false, false);
    return fmaxf(__uint_as_float(r[0]), __uint_as_float(r[1]));
}
__device__ __forceinline__ u32x4 swap_order(u32x4 a) {
    auto r0 = __builtin_amdgcn_permlane32_swap(a[0], a[2], false, false);
    auto r1 = __builtin_amdgcn_permlane32_swap(a[1], a[3], false, false);
    return (u32x4){r0[0], r1[0], r0[1], r1[1]};
}
template <int CTRL> __device__ __forceinline__ float dpp(float v) {
    return __uint_as_float(__builtin_amdgcn_update_dpp(0u, __float_as_uint(v), CTRL, 0xf, 0xf, true));
}
__device__ __forceinline__ float wave_max(float v) {
    v = fmaxf(v, dpp<0xB1>(v));
    v = fmaxf(v, dpp<0x4E>(v));
    v = fmaxf(v, dpp<0x141>(v));
    v = fmaxf(v, dpp<0x140>(v));
    auto r = __builtin_amdgcn_permlane16_swap(__float_as_uint(v), __float_as_uint(v), false, false);
    return half_max(fmaxf(__uint_as_float(r[0]), __uint_as_float(r[1])));
}
#define SBAR() __builtin_amdgcn_sched_barrier(0)
#define PIN8(x) asm volatile("" : "+v"(x[0]), "+v"(x[1]), "+v"(x[2]), "+v"(x[3]), "+v"(x[4]), "+v"(x[5]), "+v"(x[6]), "+v"(x[7]))
#define PIN16(x) asm volatile("" : "+v"(x[0]), "+v"(x[1]), "+v"(x[2]), "+v"(x[3]), "+v"(x[4]), "+v"(x[5]), "+v"(x[6]), "+v"(x[7]), \
    "+v"(x[8]), "+v"(x[9]), "+v"(x[10]), "+v"(x[11]), "+v"(x[12]), "+v"(x[13]), "+v"(x[14]), "+v"(x[15]))
#define PIN2(a, b) asm volatile("" : "+v"(a), "+v"(b))
#define LDS_BARRIER() asm volatile("s_waitcnt lgkmcnt(0)\n\ts_barrier" ::: "memory")
#define DRAIN() asm volatile("s_waitcnt vmcnt(0)" ::: "memory")
constexpr int AUX_SC1 = 16;
typedef __amdgpu_buffer_rsrc_t rsrc_t;
__device__ __forceinline__ u32x4 ld_sc1(rsrc_t r, int voff, int soff) { return __builtin_amdgcn_raw_buffer_load_b128(r, voff, soff, AUX_SC1); }
__device__ __forceinline__ u32x4 ld_run(rsrc_t r, int voff, int so, int i) { return __builtin_amdgcn_raw_buffer_load_b128(r, voff + (i & 3) * 1024, so + (i >> 2) * 4096, AUX_SC1); }
__device__ __forceinline__ u32x4 ld_plain(rsrc_t r, int voff, int soff) { return __builtin_amdgcn_raw_buffer_load_b128(r, voff, soff, 0); }
__device__ __forceinline__ void st_sc1(u32x4 v, rsrc_t r, int voff, int soff) {
    __builtin_amdgcn_raw_buffer_store_b128(v, r, voff, soff, AUX_SC1);
    asm volatile("s_nop 1" :: "v"(v) : "memory");
}
__device__ __forceinline__ u32x4 lds_ld(LAS char* l, int off) { return *(LAS u32x4*)(l + off); }
__device__ __forceinline__ void lds_st(LAS char* l, int off, u32x4 v) { *(LAS u32x4*)(l + off) = v; }
__device__ __forceinline__ f32x4 lds_ldf(LAS char* l, int off) { return *(LAS f32x4*)(l + off); }
__device__ __forceinline__ void lds_stf(LAS char* l, int off, f32x4 v) { *(LAS f32x4*)(l + off) = v; }

__device__ __forceinline__ void publish(int tid, gu32* cnt) {
    DRAIN();
    LDS_BARRIER();
    if (tid == 0) __hip_atomic_fetch_add(cnt, 1u, RLX_AGENT);
}
#define POLL_PIPE 0
__device__ __forceinline__ void poll_ge(gu32* cnt, unsigned want, gu32* tmo) {
    unsigned spins = 0;
    while (__hip_atomic_load(cnt, RLX_AGENT) < want) {
        if (++spins > SPIN_CAP) { __hip_atomic_store(tmo, 1u, RLX_AGENT); break; }
    }
}
__device__ __forceinline__ void wait_cnt(int tid, gu32* cnt, unsigned want, gu32* tmo) {
    if (tid == 0) poll_ge(cnt, want, tmo);
    __builtin_amdgcn_fence(__ATOMIC_ACQUIRE, "wavefront");
    LDS_BARRIER();
}
typedef GAS unsigned long long gu64;
__device__ __forceinline__ float wait_flags(int tid, LAS char* lds, gu64* fl, unsigned tag, gu32* tmo) {
    if (tid < 64) {
        unsigned spins = 0; unsigned long long v;
        while (v = __hip_atomic_load(fl + tid, RLX_AGENT), !__all((unsigned)(v >> 32) == tag)) {
            if (++spins > SPIN_CAP) { if (tid == 0) __hip_atomic_store(tmo, 1u, RLX_AGENT); break; }
        }
        const float m = wave_max(__uint_as_float((unsigned)v));
        if (tid == 0) *(LAS float*)(lds + F_KM2) = m;
    }
    __builtin_amdgcn_fence(__ATOMIC_ACQUIRE, "wavefront");
    LDS_BARRIER();
    return *(LAS float*)(lds + F_KM2);
}
__device__ __forceinline__ void glds16(const void* g, unsigned lds_dst) {
    unsigned keep;
    asm volatile("s_mov_b32 %0, m0\n\ts_mov_b32 m0, %2\n\ts_nop 0\n\tglobal_load_lds_dwordx4 %1, off\n\ts_mov_b32 m0, %0" : "=&s"(keep) : "v"(g), "s"(lds_dst) : "memory");
}
__device__ __forceinline__ void glds16_sc1(const void* g, unsigned lds_dst) {
    unsigned keep;
    asm volatile("s_mov_b32 %0, m0\n\ts_mov_b32 m0, %2\n\ts_nop 0\n\tglobal_load_lds_dwordx4 %1, off sc1\n\ts_mov_b32 m0, %0" : "=&s"(keep) : "v"(g), "s"(lds_dst) : "memory");
}
template <int N>
__device__ __forceinline__ void dma4_sc1(const unsigned char* g, unsigned voff, unsigned lds_dst) {
    static_assert(N >= 1 && N <= 4, "13-bit offset");
    if constexpr (N == 1) asm volatile("s_mov_b32 m0, %2\n\ts_nop 0\n\tglobal_load_lds_dwordx4 %0, %1 sc1" :: "v"(voff), "s"(g), "s"(lds_dst) : "memory");
    if constexpr (N == 2) asm volatile("s_mov_b32 m0, %2\n\ts_nop 0\n\tglobal_load_lds_dwordx4 %0, %1 sc1\n\tglobal_load_lds_dwordx4 %0, %1 offset:1024 sc1" :: "v"(voff), "s"(g), "s"(lds_dst) : "memory");
    if constexpr (N == 3) asm volatile("s_mov_b32 m0, %2\n\ts_nop 0\n\tglobal_load_lds_dwordx4 %0, %1 sc1\n\tglobal_load_lds_dwordx4 %0, %1 offset:1024 sc1\n\t"
                                       "global_load_lds_dwordx4 %0, %1 offset:2048 sc1" :: "v"(voff), "s"(g), "s"(lds_dst) : "memory");
    if constexpr (N == 4) asm volatile("s_mov_b32 m0, %2\n\ts_nop 0\n\tglobal_load_lds_dwordx4 %0, %1 sc1\n\tglobal_load_lds_dwordx4 %0, %1 offset:1024 sc1\n\t"
                                       "global_load_lds_dwordx4 %0, %1 offset:2048 sc1\n\tglobal_load_lds_dwordx4 %0, %1 offset:3072 sc1" :: "v"(voff), "s"(g), "s"(lds_dst) : "memory");
}
template <int N>
__device__ __forceinline__ void dma_run_sc1(const unsigned char* g, unsigned voff, unsigned lds_dst) {
    if constexpr (N <= 4) dma4_sc1<N>(g, voff, lds_dst);
    else { dma4_sc1<4>(g, voff, lds_dst); dma_run_sc1<N - 4>(g + 4096, voff, lds_dst + 4096); }
}
#define WAITVM(n) asm volatile("s_waitcnt vmcnt(" #n ")" ::: "memory")

__device__ __forceinline__ void reduce4(LAS char* lds, int region, const f32x16& aA, const f32x16& aB, int kq, int lane, float (&o)[8]) {
    const int base = region + lane * 16;
    { const int a = base + ((kq ^ 1) * 3 + 0) * 2048;
      lds_stf(lds, a, (f32x4){aA[8], aA[9], aA[10], aA[11]}); lds_stf(lds, a + 1024, (f32x4){aA[12], aA[13], aA[14], aA[15]}); }
    { const int a = base + ((kq ^ 2) * 3 + 1) * 2048;
      lds_stf(lds, a, (f32x4){aB[0], aB[1], aB[2], aB[3]}); lds_stf(lds, a + 1024, (f32x4){aB[4], aB[5], aB[6], aB[7]}); }
    { const int a = base + ((kq ^ 3) * 3 + 2) * 2048;
      lds_stf(lds, a, (f32x4){aB[8], aB[9], aB[10], aB[11]}); lds_stf(lds, a + 1024, (f32x4){aB[12], aB[13], aB[14], aB[15]}); }
    LDS_BARRIER();
    f32x4 lo = {aA[0], aA[1], aA[2], aA[3]}, hi = {aA[4], aA[5], aA[6], aA[7]}, t[6];
#pragma unroll
    for (int x = 0; x < 6; ++x) t[x] = lds_ldf(lds, base + kq * 3 * 2048 + x * 1024);
    SBAR();
#pragma unroll
    for (int x = 0; x < 3; ++x) { lo += t[2 * x]; hi += t[2 * x + 1]; }
#pragma unroll
    for (int i = 0; i < 4; ++i) { o[i] = lo[i]; o[4 + i] = hi[i]; }
}
typedef short v4i16 __attribute__((ext_vector_type(4)));
typedef unsigned u32x2 __attribute__((ext_vector_type(2)));
__device__ __forceinline__ u32x4 tr_frag(LAS char* lds, int a, int step) {
    const u32x2 lo = __builtin_bit_cast(u32x2, __builtin_amdgcn_ds_read_tr16_b64_v4i16((LAS v4i16*)(lds + a)));
    const u32x2 hi = __builtin_bit_cast(u32x2, __builtin_amdgcn_ds_read_tr16_b64_v4i16((LAS v4i16*)(lds + a + step)));
    return (u32x4){lo[0], lo[1], hi[0], hi[1]};
}
struct ColSum { float a[4];
    __device__ __forceinline__ void clear() { _Pragma("unroll") for (int d = 0; d < 4; ++d) a[d] = 0.f; }
    __device__ __forceinline__ void add(u32x4 f) { _Pragma("unroll") for (int d = 0; d < 4; ++d) a[d % NCH] = dot2(f[d], 0x3f803f80u, a[d % NCH]); }
    __device__ __forceinline__ float sum() const { return (a[0] + a[1]) + (a[2] + a[3]); }
};

#define ALIGN_LOG 5
#define A_STR_(x) #x
#define A_STR(x) A_STR_(x)
#define A_ALIGN ".p2align " A_STR(ALIGN_LOG) "\n\t"
#define A_E1(a) "v_exp_f32 v" #a ", v" #a "\n\t"
#define A_E4(a, b, c, d) A_E1(a) A_E1(b) A_E1(c) A_E1(d)
#define A_CVT(d, a, b) "v_cvt_pk_bf16_f32 v" #d ", v" #a ", v" #b "\n\t"
#define A_DSK(off) "ds_read_b128 v[92:95], v112 offset:" #off "\n\t"
#define A_DSV0(off) "ds_read_b128 v[96:99], v113 offset:" #off "\n\t"
#define A_DSV1(off) "ds_read_b128 v[100:103], v113 offset:" #off "\n\t"
#define A_WAIT(n) "s_waitcnt lgkmcnt(" #n ")\n\t"
#define A_MSB "v_mfma_f32_32x32x16_bf16 v[40:55], v[92:95], v[148:151], v[116:131]\n\t"
#define A_MSA "v_mfma_f32_32x32x16_bf16 v[24:39], v[92:95], v[88:91], v[56:71]\n\t"
#define A_MPV(O, V, P) "v_mfma_f32_32x32x16_bf16 " O ", " V ", " P ", " O "\n\t"
#define A_OA "v[72:87]"
#define A_OB "v[132:147]"
#define A_V0 "v[96:99]"
#define A_V1 "v[100:103]"
#define A_P0 "v[104:107]"
#define A_P1 "v[108:111]"
#define A_STEP(koff, v1off, v0off) \
    A_MSB A_DSK(koff) A_E4(24, 25, 26, 27) \
    A_MPV(A_OB, A_V1, A_P1) A_DSV1(v1off) A_E4(28, 29, 30, 31) \
    A_CVT(104, 24, 25) A_CVT(105, 26, 27) A_CVT(106, 28, 29) A_CVT(107, 30, 31) \
    A_E4(32, 33, 34, 35) \
    A_WAIT(2) A_MPV(A_OA, A_V0, A_P0) A_E4(36, 37, 38, 39) \
    A_CVT(108, 32, 33) A_CVT(109, 34, 35) A_CVT(110, 36, 37) A_CVT(111, 38, 39) \
    A_WAIT(1) A_MSA A_E4(40, 41, 42, 43) \
    A_WAIT(0) A_MPV(A_OA, A_V1, A_P1) A_E4(44, 45, 46, 47) \
    A_CVT(104, 40, 41) A_CVT(105, 42, 43) A_CVT(106, 44, 45) A_CVT(107, 46, 47) \
    A_E4(48, 49, 50, 51) \
    A_MPV(A_OB, A_V0, A_P0) A_DSV0(v0off) A_E4(52, 53, 54, 55) \
    A_CVT(108, 48, 49) A_CVT(109, 50, 51) A_CVT(110, 52, 53) A_CVT(111, 54, 55)

struct AttnState { f32x16 sa, sb, oa, ob, negma, negmb; u32x4 qa, qb, kf, v0, v1, p1; unsigned kaddr, vaddr, vstride; };
#define A_OPERANDS(st, cnt) \
        : "+{v[24:39]}"(st.sa), "+{v[40:55]}"(st.sb), "+{v[72:87]}"(st.oa), "+{v[132:147]}"(st.ob), "+{v[92:95]}"(st.kf), "+{v[96:99]}"(st.v0), "+{v[100:103]}"(st.v1), \
          "={v[104:107]}"(p0), "+{v[108:111]}"(st.p1), "+{v112}"(st.kaddr), "+{v113}"(st.vaddr), [n] "+s"(cnt) \
        : "{v[56:71]}"(st.negma), "{v[116:131]}"(st.negmb), "{v[88:91]}"(st.qa), "{v[148:151]}"(st.qb), "{v114}"(st.vstride) \
        : "memory", "scc"
#define A_ITER \
        A_STEP(1024, 512, 1024) \
        A_STEP(2048, 1536, 2048) \
        A_STEP(3072, 2560, 3072) \
        A_STEP(4096, 3584, 4096) \
        "v_add_u32 v112, 4096, v112\n\t" \
        "v_add_u32 v113, v114, v113\n\t"
__device__ __forceinline__ void attn_begin(AttnState& st, LAS char* lds, u32x4 qa, u32x4 qb, float ma, float mb, unsigned kaddr, unsigned vaddr, unsigned vstride) {
#pragma unroll
    for (int i = 0; i < 16; ++i) { st.negma[i] = -ma; st.negmb[i] = -mb; }
    st.qa = qa; st.qb = qb;
    st.kf = lds_ld(lds, kaddr); st.v0 = lds_ld(lds, vaddr); st.v1 = st.v0; st.p1 = (u32x4){0u, 0u, 0u, 0u};
    st.sa = mfma(st.kf, qa, st.negma); st.sb = st.sa;
    st.oa = (f32x16){}; st.ob = (f32x16){};
    const unsigned lbase = (unsigned)(uintptr_t)lds;
    st.kaddr = kaddr + lbase; st.vaddr = vaddr + lbase; st.vstride = vstride;
}
#define A_DMA \
    "s_mov_b32 m0, s92\n\t" \
    "s_add_u32 s92, s92, 1024\n\t" \
    "global_load_lds_dwordx4 v115, s[90:91] sc1\n\t" \
    "s_add_u32 s90, s90, 1024\n\t" \
    "s_addc_u32 s91, s91, 0\n\t"
#define A_ITER_DMA \
        A_DMA A_DMA A_STEP(1024, 512, 1024) \
        A_DMA A_STEP(2048, 1536, 2048) \
        A_DMA A_STEP(3072, 2560, 3072) \
        A_DMA A_STEP(4096, 3584, 4096) \
        "v_add_u32 v112, 4096, v112\n\t" \
        "v_add_u32 v113, v114, v113\n\t"
__device__ __forceinline__ void attn_first(AttnState& st, unsigned long long src, unsigned dst, unsigned voff) {
    unsigned cnt = 2, keep; u32x4 p0;
    asm volatile(
        "s_mov_b32 %[keep], m0\n\t"
        "s_nop 15\n\t"
        A_ALIGN
        A_ITER_DMA
        A_ITER_DMA
        "s_mov_b32 m0, %[keep]\n\t"
        "s_waitcnt vmcnt(0)\n\t"
        "s_barrier\n\t"
        A_ALIGN
        "ATT1_LOOP_%=:\n\t"
        A_ITER
        "s_sub_u32 %[n], %[n], 1\n\t"
        "s_cmp_lg_u32 %[n], 0\n\t"
        "s_cbranch_scc1 ATT1_LOOP_%=\n\t"
        : "+{v[24:39]}"(st.sa), "+{v[40:55]}"(st.sb), "+{v[72:87]}"(st.oa), "+{v[132:147]}"(st.ob), "+{v[92:95]}"(st.kf), "+{v[96:99]}"(st.v0), "+{v[100:103]}"(st.v1),
          "={v[104:107]}"(p0), "+{v[108:111]}"(st.p1), "+{v112}"(st.kaddr), "+{v113}"(st.vaddr), [n] "+s"(cnt), [keep] "=&s"(keep), "+{s[90:91]}"(src), "+{s92}"(dst)
        : "{v[56:71]}"(st.negma), "{v[116:131]}"(st.negmb), "{v[88:91]}"(st.qa), "{v[148:151]}"(st.qb), "{v114}"(st.vstride), "{v115}"(voff)
        : "memory", "scc");
}
#define PRIO_AT 4
#define A_PRIO_(k, at) A_PRIO_##k##_##at
#define A_PRIO(k) A_PRIO_X(k, PRIO_AT)
#define A_PRIO_X(k, at) A_PRIO_Y(k, at)
#define A_PRIO_Y(k, at) ".if " #k " == " #at "\n\ts_setprio 0\n\t.endif\n\t"
typedef unsigned u32x16 __attribute__((ext_vector_type(16)));
struct Prefetch { u32x16 a0, a1, b0, b1, w0, w1; };
template <int TB>
__device__ __forceinline__ void attn_second(AttnState& st, Prefetch& pf, u32x4 rs, unsigned voffw, unsigned voffl, unsigned soa, unsigned sob, unsigned sow) {
    u32x4 p0;
    if constexpr (TB == 0) {
    asm volatile(
        A_ALIGN
        "buffer_load_dwordx4 v[152:155], v115, %[rs], s93 offen offset:0 sc1\n\t" A_STEP(1024, 512, 1024)
        "buffer_load_dwordx4 v[156:159], v115, %[rs], s93 offen offset:1024 sc1\n\t" A_STEP(2048, 1536, 2048)
        "buffer_load_dwordx4 v[160:163], v115, %[rs], s93 offen offset:2048 sc1\n\t" A_STEP(3072, 2560, 3072)
        "buffer_load_dwordx4 v[164:167], v115, %[rs], s93 offen offset:3072 sc1\n\t" "s_add_u32 s93, s93, 4096\n\t" A_STEP(4096, 3584, 4096)
        "v_add_u32 v112, 4096, v112\n\t" "v_add_u32 v113, v114, v113\n\t"
        "buffer_load_dwordx4 v[168:171], v115, %[rs], s93 offen offset:0 sc1\n\t" A_STEP(1024, 512, 1024)
        "buffer_load_dwordx4 v[172:175], v115, %[rs], s93 offen offset:1024 sc1\n\t" A_STEP(2048, 1536, 2048)
        "buffer_load_dwordx4 v[176:179], v115, %[rs], s93 offen offset:2048 sc1\n\t" A_STEP(3072, 2560, 3072)
        "buffer_load_dwordx4 v[180:183], v115, %[rs], s93 offen offset:3072 sc1\n\t" "s_add_u32 s93, s93, 4096\n\t" A_STEP(4096, 3584, 4096)
        "v_add_u32 v112, 4096, v112\n\t" "v_add_u32 v113, v114, v113\n\t"
        "buffer_load_dwordx4 v[184:187], v115, %[rs], s94 offen offset:0 sc1\n\t" A_STEP(1024, 512, 1024)
        "buffer_load_dwordx4 v[188:191], v115, %[rs], s94 offen offset:1024 sc1\n\t" A_STEP(2048, 1536, 2048)
        "buffer_load_dwordx4 v[192:195], v115, %[rs], s94 offen offset:2048 sc1\n\t" A_STEP(3072, 2560, 3072)
        "buffer_load_dwordx4 v[196:199], v115, %[rs], s94 offen offset:3072 sc1\n\t" "s_add_u32 s94, s94, 4096\n\t" A_STEP(4096, 3584, 4096)
        "v_add_u32 v112, 4096, v112\n\t" "v_add_u32 v113, v114, v113\n\t"
        "buffer_load_dwordx4 v[200:203], v115, %[rs], s94 offen offset:0 sc1\n\t" A_STEP(1024, 512, 1024)
        "buffer_load_dwordx4 v[204:207], v115, %[rs], s94 offen offset:1024 sc1\n\t" A_STEP(2048, 1536, 2048)
        "buffer_load_dwordx4 v[208:211], v115, %[rs], s94 offen offset:2048 sc1\n\t" A_STEP(3072, 2560, 3072)
        "buffer_load_dwordx4 v[212:215], v115, %[rs], s94 offen offset:3072 sc1\n\t" "s_add_u32 s94, s94, 4096\n\t" A_STEP(4096, 3584, 4096)
        "v_add_u32 v112, 4096, v112\n\t" "v_add_u32 v113, v114, v113\n\t"
        "s_waitcnt lgkmcnt(0)\n\t"
        A_MPV(A_OB, A_V1, A_P1)
        "s_nop 15\n\t"
        : "+{v[24:39]}"(st.sa), "+{v[40:55]}"(st.sb), "+{v[72:87]}"(st.oa), "+{v[132:147]}"(st.ob), "+{v[92:95]}"(st.kf), "+{v[96:99]}"(st.v0), "+{v[100:103]}"(st.v1),
          "={v[104:107]}"(p0), "+{v[108:111]}"(st.p1), "+{v112}"(st.kaddr), "+{v113}"(st.vaddr), "+{s93}"(soa), "+{s94}"(sob),
          "=&{v[152:167]}"(pf.a0), "=&{v[168:183]}"(pf.a1), "=&{v[184:199]}"(pf.b0), "=&{v[200:215]}"(pf.b1)
        : "{v[56:71]}"(st.negma), "{v[116:131]}"(st.negmb), "{v[88:91]}"(st.qa), "{v[148:151]}"(st.qb), "{v114}"(st.vstride), "{v115}"(voffw), [rs] "s"(rs)
        : "memory", "scc");
    } else {
    asm volatile(
        A_ALIGN
        A_PRIO(0)
        "buffer_load_dwordx4 v[152:155], v115, %[rs], s93 offen offset:0 sc1\n\t" "buffer_load_dwordx4 v[216:219], %[vl], %[rs], s95 offen offset:0 sc1\n\t" A_STEP(1024, 512, 1024)
        A_PRIO(1)
        "buffer_load_dwordx4 v[156:159], v115, %[rs], s93 offen offset:1024 sc1\n\t" "buffer_load_dwordx4 v[220:223], %[vl], %[rs], s95 offen offset:2048 sc1\n\t" "s_add_u32 s95, s95, 4096\n\t" A_STEP(2048, 1536, 2048)
        A_PRIO(2)
        "buffer_load_dwordx4 v[160:163], v115, %[rs], s93 offen offset:2048 sc1\n\t" "buffer_load_dwordx4 v[224:227], %[vl], %[rs], s95 offen offset:0 sc1\n\t" A_STEP(3072, 2560, 3072)
        A_PRIO(3)
        "buffer_load_dwordx4 v[164:167], v115, %[rs], s93 offen offset:3072 sc1\n\t" "s_add_u32 s93, s93, 4096\n\t" "buffer_load_dwordx4 v[228:231], %[vl], %[rs], s95 offen offset:2048 sc1\n\t" "s_add_u32 s95, s95, 4096\n\t" A_STEP(4096, 3584, 4096)
        "v_add_u32 v112, 4096, v112\n\t" "v_add_u32 v113, v114, v113\n\t"
        A_PRIO(4)
        "buffer_load_dwordx4 v[168:171], v115, %[rs], s93 offen offset:0 sc1\n\t" "buffer_load_dwordx4 v[232:235], %[vl], %[rs], s95 offen offset:0 sc1\n\t" A_STEP(1024, 512, 1024)
        A_PRIO(5)
        "buffer_load_dwordx4 v[172:175], v115, %[rs], s93 offen offset:1024 sc1\n\t" "buffer_load_dwordx4 v[236:239], %[vl], %[rs], s95 offen offset:2048 sc1\n\t" "s_add_u32 s95, s95, 4096\n\t" A_STEP(2048, 1536, 2048)
        A_PRIO(6)
        "buffer_load_dwordx4 v[176:179], v115, %[rs], s93 offen offset:2048 sc1\n\t" "buffer_load_dwordx4 v[240:243], %[vl], %[rs], s95 offen offset:0 sc1\n\t" A_STEP(3072, 2560, 3072)
        A_PRIO(7)
        "buffer_load_dwordx4 v[180:183], v115, %[rs], s93 offen offset:3072 sc1\n\t" "s_add_u32 s93, s93, 4096\n\t" "buffer_load_dwordx4 v[244:247], %[vl], %[rs], s95 offen offset:2048 sc1\n\t" "s_add_u32 s95, s95, 4096\n\t" A_STEP(4096, 3584, 4096)
        "v_add_u32 v112, 4096, v112\n\t" "v_add_u32 v113, v114, v113\n\t"
        A_PRIO(8)
        "buffer_load_dwordx4 v[184:187], v115, %[rs], s94 offen offset:0 sc1\n\t" A_STEP(1024, 512, 1024)
        A_PRIO(9)
        "buffer_load_dwordx4 v[188:191], v115, %[rs], s94 offen offset:1024 sc1\n\t" A_STEP(2048, 1536, 2048)
        A_PRIO(10)
        "buffer_load_dwordx4 v[192:195], v115, %[rs], s94 offen offset:2048 sc1\n\t" A_STEP(3072, 2560, 3072)
        A_PRIO(11)
        "buffer_load_dwordx4 v[196:199], v115, %[rs], s94 offen offset:3072 sc1\n\t" "s_add_u32 s94, s94, 4096\n\t" A_STEP(4096, 3584, 4096)
        "v_add_u32 v112, 4096, v112\n\t" "v_add_u32 v113, v114, v113\n\t"
        A_PRIO(12)
        "buffer_load_dwordx4 v[200:203], v115, %[rs], s94 offen offset:0 sc1\n\t" A_STEP(1024, 512, 1024)
        A_PRIO(13)
        "buffer_load_dwordx4 v[204:207], v115, %[rs], s94 offen offset:1024 sc1\n\t" A_STEP(2048, 1536, 2048)
        A_PRIO(14)
        "buffer_load_dwordx4 v[208:211], v115, %[rs], s94 offen offset:2048 sc1\n\t" A_STEP(3072, 2560, 3072)
        A_PRIO(15)
        "buffer_load_dwordx4 v[212:215], v115, %[rs], s94 offen offset:3072 sc1\n\t" "s_add_u32 s94, s94, 4096\n\t" A_STEP(4096, 3584, 4096)
        "v_add_u32 v112, 4096, v112\n\t" "v_add_u32 v113, v114, v113\n\t"
        "s_waitcnt lgkmcnt(0)\n\t"
        A_MPV(A_OB, A_V1, A_P1)
        "s_nop 15\n\t"
        : "+{v[24:39]}"(st.sa), "+{v[40:55]}"(st.sb), "+{v[72:87]}"(st.oa), "+{v[132:147]}"(st.ob), "+{v[92:95]}"(st.kf), "+{v[96:99]}"(st.v0), "+{v[100:103]}"(st.v1),
          "={v[104:107]}"(p0), "+{v[108:111]}"(st.p1), "+{v112}"(st.kaddr), "+{v113}"(st.vaddr), "+{s93}"(soa), "+{s94}"(sob), "+{s95}"(sow),
          "=&{v[152:167]}"(pf.a0), "=&{v[168:183]}"(pf.a1), "=&{v[184:199]}"(pf.b0), "=&{v[200:215]}"(pf.b1), "=&{v[216:231]}"(pf.w0), "=&{v[232:247]}"(pf.w1)
        : "{v[56:71]}"(st.negma), "{v[116:131]}"(st.negmb), "{v[88:91]}"(st.qa), "{v[148:151]}"(st.qb), "{v114}"(st.vstride), "{v115}"(voffw), [rs] "s"(rs), [vl] "v"(voffl)
        : "memory", "scc");
    }
}
template <int I> __device__ __forceinline__ u32x4 quad(const u32x16& v) { return (u32x4){v[4 * I], v[4 * I + 1], v[4 * I + 2], v[4 * I + 3]}; }

struct Sums { float a[4], b[4];
    __device__ __forceinline__ void clear() { _Pragma("unroll") for (int d = 0; d < 4; ++d) { a[d] = 0.f; b[d] = 0.f; } }
    __device__ __forceinline__ void add(u32x4 f) { _Pragma("unroll") for (int d = 0; d < 4; ++d) { a[d % NCH] = dot2(f[d], 0x3f803f80u, a[d % NCH]); b[d % NCH] = dot2(f[d], f[d], b[d % NCH]); } }
    __device__ __forceinline__ float sum() const { return (a[0] + a[1]) + (a[2] + a[3]); }
    __device__ __forceinline__ float sumsq() const { return (b[0] + b[1]) + (b[2] + b[3]); }
};
template <int N>
__device__ __forceinline__ void stat_sums(LAS char* lds, int slot0, int tok, float& t1, float& t2) {
    f32x2 v[N];
#pragma unroll
    for (int i = 0; i < N; ++i) v[i] = *(LAS f32x2*)(lds + L_STAT + ((slot0 + i) * 32 + tok) * 8);
    SBAR();
    t1 = 0.f; t2 = 0.f;
#pragma unroll
    for (int i = 0; i < N; ++i) { t1 += v[i][0]; t2 += v[i][1]; }
}
__device__ __forceinline__ void ln_from_sums(float t1, float t2, float& mu, float& rstd) {
    mu = t1 * (1.f / D);
    rstd = __builtin_amdgcn_rsqf(fmaxf(t2 * (1.f / D) - mu * mu, 0.f) + LN_EPS);
}

template <int TB>
__device__ __forceinline__ void run_layers(unsigned char* ws0, LAS char* lds) {
    const unsigned lbase = (unsigned)(uintptr_t)lds;
    const int tid0 = threadIdx.x, w0 = __builtin_amdgcn_readfirstlane(tid0 >> 6);
    const int kk0 = blockIdx.x >> 3, c0 = (blockIdx.x & 7) * 4 + (kk0 >> 3), j0 = kk0 & 7;
    typedef const __attribute__((address_space(4))) Params* kargs_t;
#define PHASE_IDS() \
    int tid = tid0, w = w0, c = c0, j = j0; unsigned epoch = epoch0; unsigned char* ws = ws0; \
    kargs_t kp = (kargs_t)__builtin_amdgcn_kernarg_segment_ptr();        \
    asm volatile("" : "+v"(tid)); asm volatile("" : "+s"(w), "+s"(c), "+s"(j), "+s"(epoch), "+s"(ws), "+s"(kp)); \
    const rsrc_t rw = __builtin_amdgcn_make_buffer_rsrc(ws, 0, WS_TOTAL, 0x00020000); \
    gu32* cnt = (gu32*)(ws + OFF_CNT); gu32* sync = (gu32*)(ws + OFF_SYNC); gu32* tmo = sync + 2 * CW; \
    (void)rw; (void)cnt; (void)tmo; (void)kp; \
    constexpr int tb = TB; const int lane = tid & 63, r = lane & 31, h = lane >> 5, kq = w & 3, tbg = 2 * c + tb; \
    const int vl = lane * 16;                                     \
    const int vw = (lane ^ ((kq & 1) << 4)) * 16;                 \
    const int cbA = 2 * j + (kq >> 1), cbB = 2 * j + ((kq >> 1) ^ 1); \
    const int sown = 4 * j + kq;                                  \
    (void)epoch; (void)r; (void)h; (void)tbg; (void)vl; (void)vw; (void)cbA; (void)cbB; (void)sown; (void)tb;
#define W1_IDS() \
    const int ncb = j < 3 ? 6 : 5, cb0 = j < 3 ? 6 * j : 18 + 5 * (j - 3);     \
    const int cb1 = TB == 0 ? kq : 4 + (kq >> 1);     \
    const bool d_active = cb1 < ncb; \
    const int cbg1 = cb0 + (d_active ? cb1 : 0); (void)cbg1; \
    const int par = TB == 0 ? 0 : (kq & 1); (void)par;

#define REQ_WKV() do { \
        const uint16_t* src_ = ((lane & 2) ? kp->wv : kp->wk) + (128 * kq + (lane >> 2)) * 16 + (lane & 1) * 8; \
        _Pragma("unroll") for (int i = 0; i < 8; ++i) glds16(src_ + i * 256, lbase + ((kq & 2) ? L_WKV1 : L_WKV0) + ((kq & 1) * 8 + i) * 1024); } while (0)

    STAMP(0);
    unsigned epoch_v, epoch0 = 0;
    { gu32* e = (gu32*)(ws0 + OFF_SYNC); asm volatile("" : "+s"(e)); epoch_v = __hip_atomic_load(e, RLX_AGENT); }
    {
        PHASE_IDS();
        if (tid < 16) *(LAS unsigned*)(lds + (tid < 8 ? F_PAIR + tid * 4 : F_OCNT)) = 0u;
        if (j == tb && kq == 0 && lane < 4) __hip_atomic_store(cnt + cnt_word(j, lane, c), 0u, RLX_AGENT);
        if (j == 0 && tid == 4) __hip_atomic_store((gu32*)(ws + OFF_PREPC) + c * CW, 0u, RLX_AGENT);
        if (c == 0 && j == 0 && tid == 5) __hip_atomic_store(tmo, 0u, RLX_AGENT);
    }

#pragma unroll 1
    for (int L = 0; L < 2; ++L) {
        u32x4 qown; float q2own;
        {
            PHASE_IDS();
            const bool kvm = j < 2;
            const bool kvteam = j == tb;
            if (kvm && kvteam) {
                __builtin_amdgcn_s_setprio(2);
                if (L == 0) REQ_WKV();
            } else {
                const int row = lane >> 3, pc = lane & 7;
                const uint16_t* src = kp->wq + (size_t)(64 * w + row) * D + 64 * j + (((pc >> 2) ^ ((row >> 1) & 1)) * 32 + (pc & 3) * 8);
                if (L == 0) {
#pragma unroll
                    for (int i = 0; i < 4; ++i) glds16(src + i * 8 * D, lbase + L_WQ + (w * 8 + i) * 1024);
                }
#pragma unroll
                for (int i = 4; i < 8; ++i) glds16(src + i * 8 * D, lbase + L_WQ + (w * 8 + i) * 1024);
                if (kvm) {
                    const int w2 = w ^ 4;
                    const uint16_t* src2 = kp->wq + (size_t)(64 * w2 + row) * D + 64 * j + (((pc >> 2) ^ ((row >> 1) & 1)) * 32 + (pc & 3) * 8);
                    if (L == 0) {
#pragma unroll
                        for (int i = 0; i < 4; ++i) glds16(src2 + i * 8 * D, lbase + L_WQ + (w2 * 8 + i) * 1024);
                    }
#pragma unroll
                    for (int i = 4; i < 8; ++i) glds16(src2 + i * 8 * D, lbase + L_WQ + (w2 * 8 + i) * 1024);
                }
            }
            auto prep_dst = [&](int i) -> int {
                const int u = c * 6 + (j - 2);
                if (u < 43) return OFF_W1 + (u * KSD + 4 * w + i) * 1024;
                const int f = ((u - 43) * 8 + w) + i * 1192;
                if (i >= 2 || f >= 1920) return -1;
                return f < 512 ? OFF_WO + f * 1024 : OFF_W2 + (f - 512) * 1024;
            };
            int xso = OFF_X2 + (tbg * KSD + kq * 8) * 1024, xvo = vl;
            asm volatile("" : "+s"(xso)); asm volatile("" : "+v"(xvo));
            u32x4 xa[8] = {};
            if (L == 0) {
                const rsrc_t rx = __builtin_amdgcn_make_buffer_rsrc((void*)kp->x, 0, S * D * 2, 0x00020000);
#pragma unroll
                for (int i = 0; i < 8; ++i) xa[i] = ld_plain(rx, r * 1024 + h * 16, (64 * c + 32 * tb) * 1024 + (kq * 8 + i) * 32);
                SBAR();
                STAMP(2);
                epoch0 = __builtin_amdgcn_readfirstlane(epoch_v); asm volatile("" : "+s"(epoch0)); epoch = epoch0;
                if (!kvm || kvteam) WAITVM(8);
            } else {
                if (tid == (kvm ? 256 * j : 0)) poll_ge(cnt + cnt_word(0, CNT_X2, c), 8u, tmo);
                __builtin_amdgcn_fence(__ATOMIC_ACQUIRE, "wavefront");
                if (!kvm || kvteam) WAITVM(0);
            }
            if (!(L == 0 && kvm)) LDS_BARRIER();
            STAMP(1 + L * 32);
            if (L == 1) {
#pragma unroll
                for (int i = 0; i < 8; ++i) xa[i] = LR_C ? ld_run(rw, xvo, xso, i) : ld_sc1(rw, vl, OFF_X2 + (tbg * KSD + kq * 8 + i) * 1024);
                SBAR();
            }
            LAS f32x2* st = (LAS f32x2*)(lds + L_STAT);
            LAS float* cst = (LAS float*)(lds + L_CS);
            const int tq = (lane & 15) >> 2, tp = lane & 3, tg = (lane >> 4) & 1;
            f32x16 aA = {}, aB = {};
            auto store_stats = [&]() {
                Sums sm; sm.clear();
#pragma unroll
                for (int i = 0; i < 8; ++i) sm.add(xa[i]);
                const float s1 = half_sum(sm.sum()), s2 = half_sum(sm.sumsq());
                if (h == 0) st[(tb * 8 + kq) * 32 + r] = (f32x2){s1, s2};
            };
            auto do_q = [&]() {
                u32x4 fa[8], fb[8];
                ColSum cA, cB; cA.clear(); cB.clear();
                const int sw = (tq >> 1) & 1, col = (tg ^ (kq & 1)) * 32 + tp * 8;
                const int a0 = L_WQ + (kq * 128 + 8 * h + tq) * 128 + col;
                const int aa = a0 + (((kq >> 1) ^ sw) * 64), ab = a0 + (((kq >> 1) ^ 1 ^ sw) * 64);
#pragma unroll
                for (int i = 0; i < 8; ++i) { fa[i] = tr_frag(lds, aa + i * 2048, 512); fb[i] = tr_frag(lds, ab + i * 2048, 512); }
                SBAR();
#pragma unroll
                for (int i = 0; i < 8; ++i) { cA.add(fa[i]); cB.add(fb[i]); }
                const float csA = half_sum(cA.sum()), csB = half_sum(cB.sum());
                if (TB == 0 && h == 0) { const int n = r ^ ((kq & 1) << 4); cst[kq * 96 + (kq >> 1) * 32 + n] = csA; cst[kq * 96 + ((kq >> 1) ^ 1) * 32 + n] = csB; }
                SBAR();
#pragma unroll
                for (int i = 0; i < 8; ++i) { aA = mfma(fa[i], xa[i], aA); aB = mfma(fb[i], xa[i], aB); }
                PIN2(aA, aB);
            };
            if (kvm) {
                if (kvteam) {
                    u32x4 wk[8];
                    const int a = ((kq & 2) ? L_WKV1 : L_WKV0) + ((kq & 1) * 128 + 8 * h + tq) * 64 + tg * 32 + tp * 8;
#pragma unroll
                    for (int i = 0; i < 8; ++i) wk[i] = tr_frag(lds, a + i * 1024, 256);
                    SBAR();
                    ColSum ck; ck.clear();
#pragma unroll
                    for (int i = 0; i < 8; ++i) ck.add(wk[i]);
                    const float cs = half_sum(ck.sum());
                    if (h == 0) cst[kq * 96 + 64 + r] = cs;
                    SBAR();
                    f32x16 ak = {}, av = {};
#pragma unroll
                    for (int i = 0; i < 8; ++i) { ak = mfma(wk[i], xa[i], ak); av = mfma(xa[i], wk[i], av); }
                    store_stats();
                    if (kq == 3 && lane == 0) *(LAS unsigned*)(lds + F_KVCNT) = 0u;
                    const int o = L_KVRED + kq * 4096;
                    lds_stf(lds, o + vl, (f32x4){ak[0], ak[1], ak[2], ak[3]}); lds_stf(lds, o + 1024 + vl, (f32x4){ak[4], ak[5], ak[6], ak[7]});
                    if (r >= 16) {
#pragma unroll
                        for (int g = 0; g < 4; ++g) lds_stf(lds, o + 2048 + g * 512 + (h * 16 + r - 16) * 16, (f32x4){av[4 * g], av[4 * g + 1], av[4 * g + 2], av[4 * g + 3]});
                    }
                } else WAITVM(8);
                LDS_BARRIER();
                if (kvteam && kq < 3) {
                    f32x4 a = {}, e = {}, ta[4], te[4];
                    float t1, t2, mu, rstd;
                    if (kq == 0) {
                        f32x4 k0 = {}, k1 = {}, u0[4], u1[4];
#pragma unroll
                        for (int i = 0; i < 4; ++i) { ta[i] = lds_ldf(lds, L_KVRED + i * 4096 + vl); te[i] = lds_ldf(lds, L_KVRED + i * 4096 + 1024 + vl);
                                                      u0[i] = lds_ldf(lds, L_CS + (i * 96 + 64 + 4 * h) * 4); u1[i] = lds_ldf(lds, L_CS + (i * 96 + 64 + 8 + 4 * h) * 4); }
                        stat_sums<4>(lds, tb * 8, r, t1, t2); ln_from_sums(t1, t2, mu, rstd);
#pragma unroll
                        for (int i = 0; i < 4; ++i) { a += ta[i]; e += te[i]; k0 += u0[i]; k1 += u1[i]; }
#pragma unroll
                        for (int i = 0; i < 4; ++i) { a[i] = (a[i] - mu * k0[i]) * rstd; e[i] = (e[i] - mu * k1[i]) * rstd; }
                        st_sc1((u32x4){pk(a[0], a[1]), pk(a[2], a[3]), pk(e[0], e[1]), pk(e[2], e[3])}, rw, vl, OFF_KF + L * 65536 + tbg * 1024);
                        float n2 = (a[0] * a[0] + a[1] * a[1]) + (a[2] * a[2] + a[3] * a[3]) + (e[0] * e[0] + e[1] * e[1]) + (e[2] * e[2] + e[3] * e[3]);
                        n2 = wave_max(half_sum(n2));
                        if (lane == 0) *(LAS float*)(lds + F_N2) = n2;
                    } else {
                        const int g = kq - 1, lc = (h * 16 + (r & 15)) * 16;
                        float csv = 0.f, uc[4];
#pragma unroll
                        for (int i = 0; i < 4; ++i) { ta[i] = lds_ldf(lds, L_KVRED + i * 4096 + 2048 + (2 * g) * 512 + lc); te[i] = lds_ldf(lds, L_KVRED + i * 4096 + 2048 + (2 * g + 1) * 512 + lc);
                                                      uc[i] = cst[i * 96 + 64 + 16 + (r & 15)]; }
                        stat_sums<4>(lds, tb * 8, r, t1, t2); ln_from_sums(t1, t2, mu, rstd);
#pragma unroll
                        for (int i = 0; i < 4; ++i) { a += ta[i]; e += te[i]; csv += uc[i]; }
#pragma unroll
                        for (int i = 0; i < 8; ++i) {
                            const int tok = 16 * g + (i & 3) + 8 * (i >> 2) + 4 * h;
                            const float m2 = __uint_as_float(__builtin_amdgcn_ds_bpermute(tok * 4, __float_as_uint(mu))), r2 = __uint_as_float(__builtin_amdgcn_ds_bpermute(tok * 4, __float_as_uint(rstd)));
                            if (i < 4) a[i] = (a[i] - m2 * csv) * r2; else e[i - 4] = (e[i - 4] - m2 * csv) * r2;
                        }
                        if (r >= 16) st_sc1((u32x4){pk(a[0], a[1]), pk(a[2], a[3]), pk(e[0], e[1]), pk(e[2], e[3])}, rw, h * 256 + (r - 16) * 16, OFF_VF + L * 65536 + (2 * tbg + g) * 512);
                    }
                }
                if (kvteam && kq < 3) {
                    DRAIN();
                    unsigned old = 0;
                    if (lane == 0) old = __hip_atomic_fetch_add((LAS unsigned*)(lds + F_KVCNT), 1u, __ATOMIC_RELAXED, __HIP_MEMORY_SCOPE_WORKGROUP);
                    if (__builtin_amdgcn_readfirstlane(old) == 2u && lane < 32)
                        __hip_atomic_store((gu64*)(ws + OFF_KVFL) + (L * 32 + lane) * 64 + 2 * c + j,
                                           ((unsigned long long)(epoch + 1u + L) << 32) | __float_as_uint(*(volatile LAS float*)(lds + F_N2)), RLX_AGENT);
                }
                __builtin_amdgcn_s_setprio(0);
                STAMP(3 + L * 32);
                do_q();
                if (!kvteam) store_stats();
            } else {
                do_q(); store_stats();
                STAMP(3 + L * 32);
            }
            if (L == 0 && !kvm) {
#pragma unroll
                for (int i = 0; i < 4; ++i) {
                    const int d = prep_dst(i);
                    if (d < 0) continue;
                    const uint16_t* src; int ld, cb, s;
                    if (d >= OFF_W2) { const int g = (d - OFF_W2) >> 10; src = kp->w2; ld = D; cb = g / KSFP; s = min(g % KSFP, KSF - 1); }
                    else if (d >= OFF_W1) { const int g = (d - OFF_W1) >> 10; src = kp->w1; ld = F; cb = g >> 5; s = g & 31; }
                    else { const int g = (d - OFF_WO) >> 10; src = kp->wo; ld = D; cb = g >> 5; s = g & 31; }
                    glds16(src + (size_t)(16 * s + (lane >> 2)) * ld + 32 * cb + (lane & 3) * 8, lbase + L_PREP + (w * 4 + i) * 1024);
                }
            }
            STAMP(4 + L * 32);
            float q[8];
            reduce4(lds, kvm ? (kvteam ? 0 : 131072) : (TB == 0 ? 32768 : 131072), aA, aB, kq, lane, q);
            STAMP(5 + L * 32);
            float t1, t2;
            f32x4 cs0 = {}, cs1 = {}, u0[4], u1[4];
#pragma unroll
            for (int i = 0; i < 4; ++i) { u0[i] = lds_ldf(lds, L_CS + (i * 96 + 16 * kq + 4 * h) * 4); u1[i] = lds_ldf(lds, L_CS + (i * 96 + 16 * kq + 8 + 4 * h) * 4); }
            stat_sums<4>(lds, tb * 8, r, t1, t2);
#pragma unroll
            for (int i = 0; i < 4; ++i) { cs0 += u0[i]; cs1 += u1[i]; }
            float mu, rstd; ln_from_sums(t1, t2, mu, rstd);
            float n2 = 0.f;
#pragma unroll
            for (int i = 0; i < 4; ++i) { q[i] = (q[i] - mu * cs0[i]) * (rstd * QSCALE); q[4 + i] = (q[4 + i] - mu * cs1[i]) * (rstd * QSCALE); }
#pragma unroll
            for (int i = 0; i < 8; ++i) n2 += q[i] * q[i];
            q2own = half_sum(n2);
            qown = (u32x4){pk(q[0], q[1]), pk(q[2], q[3]), pk(q[4], q[5]), pk(q[6], q[7])};
            lds_st(lds, L_XCHQ + w * 1024 + vl, qown);
            *(LAS float*)(lds + L_STAT + (tb * 8 + 4 + kq) * 256 + lane * 4) = q2own;
            if (L == 0 && !kvm) {
                WAITVM(0);
                ColSum cp; cp.clear();
#pragma unroll
                for (int i = 0; i < 4; ++i) {
                    const int d = prep_dst(i);
                    if (d < 0) continue;
                    u32x4 fr = tr_frag(lds, L_PREP + (w * 4 + i) * 1024 + (8 * h + tq) * 64 + tg * 32 + tp * 8, 256);
                    if (d >= OFF_W2 && ((d - OFF_W2) >> 10) % KSFP >= KSF) fr = (u32x4){0u, 0u, 0u, 0u};
                    st_sc1(fr, rw, vl, d);
                    cp.add(fr);
                }
                const int u = c * 6 + (j - 2);
                if (u < 43) {
                    const float cs = half_sum(cp.sum());
                    LAS float* t = (LAS float*)(lds + L_WQ);
                    if (h == 0) t[w * 32 + r] = cs;
                    LDS_BARRIER();
                    if (tid < 32) { float v = 0.f, pv[8];
#pragma unroll
                        for (int i = 0; i < 8; ++i) pv[i] = t[i * 32 + tid];
                        SBAR();
#pragma unroll
                        for (int i = 0; i < 8; ++i) v += pv[i];
                        __hip_atomic_store((gu32*)(ws + OFF_CS1 + (32 * u + tid) * 4), __float_as_uint(v), RLX_AGENT); }
                }
                DRAIN();
            }
            STAMP(6 + L * 32);
        }
        u32x4 woA[8], woB[8];
        u32x4 w1f[32];
        {
            PHASE_IDS();
            const unsigned char* kvsrc = ws + (TB == 0 ? OFF_KF : OFF_VF) + L * 65536 + ((w >> 1) & 1) * 32768 + (w & 1) * 6144;
            unsigned kvdst = lbase + (TB == 0 ? L_K : L_V) + ((w >> 1) & 1) * 32768 + (w & 1) * 6144, kvoff = vl;
            asm volatile("" : "+s"(kvsrc), "+s"(kvdst)); asm volatile("" : "+v"(kvoff));
            const float km2 = wait_flags(tid, lds, (gu64*)(ws + OFF_KVFL) + (L * 32 + c) * 64, epoch + 1u + L, tmo);
            dma_run_sc1<6>(kvsrc, kvoff, kvdst);
            if (L == 0 && j >= 2 && tid == 0) __hip_atomic_fetch_add((gu32*)(ws + OFF_PREPC) + c * CW, 1u, RLX_AGENT);
            STAMP(7 + L * 32);
            { unsigned one2 = (!ZROWS || (tid & 31) == 0) ? 0x3f803f80u : 0u; asm volatile("" : "+v"(one2)); if (tid < 272) lds_st(lds, L_ONES + tid * 16, (u32x4){one2, one2, one2, one2}); }
            const u32x4 qpar = lds_ld(lds, L_XCHQ + (w ^ 1) * 1024 + vl);
            const float q2par = *(LAS float*)(lds + L_STAT + (tb * 8 + 4 + (kq ^ 1)) * 256 + lane * 4);
            float ma = __builtin_amdgcn_sqrtf(q2own * km2) * 1.02f, mb = __builtin_amdgcn_sqrtf(q2par * km2) * 1.02f;
            const bool unsafe = __any(!(ma <= SAFE_BOUND) || !(mb <= SAFE_BOUND));
            {
                LAS unsigned* fl = (LAS unsigned*)(lds + F_UNSAFE);
                if (lane == 0) fl[w] = unsafe ? 1u : 0u;
            }
            STAMP(8 + L * 32);
            WAITVM(0);
            LDS_BARRIER();
            {
                LAS unsigned* fl = (LAS unsigned*)(lds + F_UNSAFE);
                const u32x4 f0 = *(LAS u32x4*)fl, f1 = *(LAS u32x4*)(fl + 4);
                if (__builtin_amdgcn_readfirstlane(f0[0] | f0[1] | f0[2] | f0[3] | f1[0] | f1[1] | f1[2] | f1[3])) {
                    {
                        const unsigned char* src = ws + (TB == 0 ? OFF_KF : OFF_VF) + L * 65536 + ((w >> 1) & 1) * 32768 + (12 + (w & 1) * 10) * 1024 + vl;
                        const unsigned dst = lbase + (TB == 0 ? L_K : L_V) + ((w >> 1) & 1) * 32768 + (12 + (w & 1) * 10) * 1024;
#pragma unroll 1
                        for (int i = 0; i < 10; ++i) glds16_sc1(src + i * 1024, __builtin_amdgcn_readfirstlane(dst + i * 1024));
                    }
                    DRAIN();
                    LDS_BARRIER();
                    if (unsafe) {
                        float xa = -INFINITY, xb = -INFINITY;
#pragma unroll 1
                        for (int t = 0; t < 64; ++t) {
                            const u32x4 kf = lds_ld(lds, L_K + t * 1024 + vl);
                            const f32x16 sa = mfma(kf, qown, (f32x16){}), sb = mfma(kf, qpar, (f32x16){});
#pragma unroll
                            for (int i = 0; i < 16; ++i) { xa = fmaxf(xa, sa[i]); xb = fmaxf(xb, sb[i]); }
                        }
                        ma = half_max(xa); mb = half_max(xb);
                    }
                }
            }
            STAMP(9 + L * 32);
            const int kh = kq & 1;
            const bool real = r < 16;
            const unsigned kaddr = L_K + kh * 32768 + vl;
            const unsigned vaddr = real ? (unsigned)(L_V + kh * 32768 + h * 256 + r * 16) : (unsigned)(L_ONES + ((r >= 20 && r < 28) ? 64 : (ZROWS && r != 16) ? 16 : 0));
            if (PRIO >= 1 && TB == 1) __builtin_amdgcn_s_setprio(1);
            AttnState st;
            attn_begin(st, lds, qown, qpar, ma, mb, kaddr, vaddr, real ? 4096u : 0u);
            attn_first(st, (unsigned long long)(uintptr_t)(ws + (TB == 0 ? OFF_KF : OFF_VF) + L * 65536 + ((w >> 1) & 1) * 32768 + (12 + (w & 1) * 10) * 1024),
                       lbase + (TB == 0 ? L_K : L_V) + ((w >> 1) & 1) * 32768 + (12 + (w & 1) * 10) * 1024, (unsigned)vl);
            STAMP(10 + L * 32);
            if (L == 0) {
                unsigned spins = 0;
                while (!__all(__hip_atomic_load((gu32*)(ws + OFF_PREPC) + (lane & 31) * CW, RLX_AGENT) >= 6u)) {
                    if (++spins > SPIN_CAP) { if (lane == 0) __hip_atomic_store(tmo, 1u, RLX_AGENT); break; }
                }
                __builtin_amdgcn_fence(__ATOMIC_ACQUIRE, "wavefront");
            }
            STAMP(11 + L * 32);
            Prefetch pf;
            { W1_IDS();
              const unsigned long long wsa = (unsigned long long)(uintptr_t)ws;
              const u32x4 rs = {(unsigned)wsa, (unsigned)(wsa >> 32) & 0xffffu, (unsigned)WS_TOTAL, 0x00020000u};
              attn_second<TB>(st, pf, rs, (unsigned)vw, (unsigned)vl, OFF_WO + (cbA * KSD + kq * 8) * 1024, OFF_WO + (cbB * KSD + kq * 8) * 1024, OFF_W1 + (cbg1 * KSD + par) * 1024); }
            STAMP(12 + L * 32);
            if (TB == 1) __builtin_amdgcn_s_setprio(0);
            const f32x16 oa = st.oa, ob = st.ob;
            const int xo = L_XCHO + w * 2304;
            lds_stf(lds, xo + vl, (f32x4){ob[0], ob[1], ob[2], ob[3]}); lds_stf(lds, xo + 1024 + vl, (f32x4){ob[4], ob[5], ob[6], ob[7]});
            *(LAS float*)(lds + xo + 2048 + lane * 4) = ZROWS ? half_sum(ob[8]) : ob[8];
            asm volatile("s_waitcnt lgkmcnt(0)" ::: "memory");
            *(volatile LAS unsigned*)(lds + F_PAIR + w * 4) = L + 1u;
            while (*(volatile LAS unsigned*)(lds + F_PAIR + (w ^ 1) * 4) != L + 1u) { }
            asm volatile("" ::: "memory");
            const int xi = L_XCHO + (w ^ 1) * 2304;
            const f32x4 e0 = lds_ldf(lds, xi + vl), e1 = lds_ldf(lds, xi + 1024 + vl);
            const float lpar = *(LAS float*)(lds + xi + 2048 + lane * 4);
            SBAR();
            const float rl = __builtin_amdgcn_rcpf((ZROWS ? half_sum(oa[8]) : oa[8]) + lpar);
            const u32x4 of = {pk((oa[0] + e0[0]) * rl, (oa[1] + e0[1]) * rl), pk((oa[2] + e0[2]) * rl, (oa[3] + e0[3]) * rl),
                              pk((oa[4] + e1[0]) * rl, (oa[5] + e1[1]) * rl), pk((oa[6] + e1[2]) * rl, (oa[7] + e1[3]) * rl)};
            st_sc1(swap_order(of), rw, vl, OFF_O + L * ACT_BYTES + (tbg * KSD + sown) * 1024);
            if (TB == 0) asm volatile("s_waitcnt vmcnt(0)" : "+{v[152:167]}"(pf.a0), "+{v[168:183]}"(pf.a1), "+{v[184:199]}"(pf.b0), "+{v[200:215]}"(pf.b1) :: "memory");
            else asm volatile("s_waitcnt vmcnt(0)" : "+{v[152:167]}"(pf.a0), "+{v[168:183]}"(pf.a1), "+{v[184:199]}"(pf.b0), "+{v[200:215]}"(pf.b1), "+{v[216:231]}"(pf.w0), "+{v[232:247]}"(pf.w1) :: "memory");
            unsigned old = 0;
            if (lane == 0) old = __hip_atomic_fetch_add((LAS unsigned*)(lds + F_OCNT), 1u, __ATOMIC_RELAXED, __HIP_MEMORY_SCOPE_WORKGROUP);
            if (__builtin_amdgcn_readfirstlane(old) == 8u * L + 7u && lane == 0) __hip_atomic_fetch_add(cnt + cnt_word(L, CNT_O, c), 1u, RLX_AGENT);
            STAMP(13 + L * 32);
            woA[0] = quad<0>(pf.a0); woA[1] = quad<1>(pf.a0); woA[2] = quad<2>(pf.a0); woA[3] = quad<3>(pf.a0);
            woA[4] = quad<0>(pf.a1); woA[5] = quad<1>(pf.a1); woA[6] = quad<2>(pf.a1); woA[7] = quad<3>(pf.a1);
            woB[0] = quad<0>(pf.b0); woB[1] = quad<1>(pf.b0); woB[2] = quad<2>(pf.b0); woB[3] = quad<3>(pf.b0);
            woB[4] = quad<0>(pf.b1); woB[5] = quad<1>(pf.b1); woB[6] = quad<2>(pf.b1); woB[7] = quad<3>(pf.b1);
            if (TB == 1) {
                w1f[0] = quad<0>(pf.w0); w1f[1] = quad<1>(pf.w0); w1f[2] = quad<2>(pf.w0); w1f[3] = quad<3>(pf.w0);
                w1f[4] = quad<0>(pf.w1); w1f[5] = quad<1>(pf.w1); w1f[6] = quad<2>(pf.w1); w1f[7] = quad<3>(pf.w1);
            }
            if (TB == 0) { W1_IDS();
#pragma unroll
                for (int i = 0; i < W1_EARLY; ++i) { w1f[i] = LR_W ? ld_run(rw, vl, OFF_W1 + cbg1 * KSD * 1024, i) : ld_sc1(rw, vl, OFF_W1 + (cbg1 * KSD + i) * 1024); SBAR(); __builtin_amdgcn_s_sleep(W1_SLEEP); SBAR(); }
            }
        }
        u32x4 resid;
        {
            PHASE_IDS();
            if (L == 0) { const rsrc_t rx = __builtin_amdgcn_make_buffer_rsrc((void*)kp->x, 0, S * D * 2, 0x00020000);
                          resid = ld_plain(rx, r * 1024 + h * 16, (64 * c + 32 * tb) * 1024 + sown * 32); }
            else resid = ld_sc1(rw, vl, OFF_X2 + (tbg * KSD + sown) * 1024);
            int oso = OFF_O + L * ACT_BYTES + (tbg * KSD + kq * 8) * 1024, ovo = vl;
            asm volatile("" : "+s"(oso)); asm volatile("" : "+v"(ovo));
            STAMP(14 + L * 32);
            wait_cnt(tid, cnt + cnt_word(L, CNT_O, c), 8u, tmo);
            STAMP(15 + L * 32);
            u32x4 xa[8];
#pragma unroll
            for (int i = 0; i < 8; ++i) xa[i] = LR_C ? ld_run(rw, ovo, oso, i) : ld_sc1(rw, vl, OFF_O + L * ACT_BYTES + (tbg * KSD + kq * 8 + i) * 1024);
            SBAR();
            f32x16 aA = {}, aB = {};
#pragma unroll
            for (int i = 0; i < 8; ++i) { aA = mfma(woA[i], xa[i], aA); aB = mfma(woB[i], xa[i], aB); }
            PIN2(aA, aB);
            SBAR();
            STAMP(16 + L * 32);
            {
                auto run_src = [&](int run) { return ws + OFF_W2 + ((2 * j + (run & 1)) * KSFP + (run >> 1) * 22 + W2_REG) * 1024; };
                if (TB == 0) { dma_run_sc1<6>(run_src(kq), vl, lbase + L_W2A + kq * W2_A * 1024); dma_run_sc1<3>(run_src(kq + 4), vl, lbase + L_W2A + (kq + 4) * W2_A * 1024); }
                else dma_run_sc1<3>(run_src(w) + 3072, vl, lbase + L_W2A + (w * W2_A + 3) * 1024);
            }
            STAMP(17 + L * 32);
            float o[8];
            reduce4(lds, tb * 24576, aA, aB, kq, lane, o);
            resid = swap_order(resid);
#pragma unroll
            for (int i = 0; i < 4; ++i) resid[i] = pk(bflo(resid[i]) + o[2 * i], bfhi(resid[i]) + o[2 * i + 1]);
            st_sc1(swap_order(resid), rw, vl, OFF_X1 + L * ACT_BYTES + (tbg * KSD + sown) * 1024);
            STAMP(18 + L * 32);
            publish(tid, cnt + cnt_word(L, CNT_X1, c));
            STAMP(19 + L * 32);
        }
        u32x4 w2A[W2_REG], w2B[W2_REG];
        {
            PHASE_IDS(); W1_IDS();
#pragma unroll
            for (int n = 0; n < 8; ++n) {
                if (TB == 0) w1f[W1_EARLY + n] = LR_W ? ld_run(rw, vl, OFF_W1 + (cbg1 * KSD + W1_EARLY) * 1024, n) : ld_sc1(rw, vl, OFF_W1 + (cbg1 * KSD + W1_EARLY + n) * 1024);
                else w1f[16 + n] = LR_W ? ld_run(rw, vl + (n & 1) * 2048, OFF_W1 + (cbg1 * KSD + 16 + par) * 1024 + (n >> 1) * 4096, 0) : ld_sc1(rw, vl, OFF_W1 + (cbg1 * KSD + 16 + 2 * n + par) * 1024);
            }
            unsigned tab_cs = 0; unsigned short tab_b = 0;
            if (w >= 1 && w <= 3) { const int col = min(32 * cb0 + tid - 64, F - 1);
                tab_cs = __builtin_amdgcn_raw_buffer_load_b32(rw, col * 4, OFF_CS1, AUX_SC1); tab_b = kp->b1[col]; }
            SBAR();
            const unsigned char* xsrc = ws + OFF_X1 + L * ACT_BYTES + tbg * KSD * 1024;
            unsigned xdst = lbase + L_ACT + tb * KSD * 1024, xoff = vl;
            asm volatile("" : "+s"(xsrc), "+s"(xdst)); asm volatile("" : "+v"(xoff));
            STAMP(20 + L * 32);
            wait_cnt(tid, cnt + cnt_word(L, CNT_X1, c), 8u, tmo);
            STAMP(21 + L * 32);
            if (w >= 1 && w <= 3) { *(LAS float*)(lds + L_TAB + (tid - 64) * 4) = __uint_as_float(tab_cs); *(LAS float*)(lds + L_TAB + 768 + (tid - 64) * 4) = bf2f(tab_b); }
            if (TB == 0) asm volatile("" : "+v"(w1f[24]), "+v"(w1f[25]), "+v"(w1f[26]), "+v"(w1f[27]), "+v"(w1f[28]), "+v"(w1f[29]), "+v"(w1f[30]), "+v"(w1f[31]));
            else asm volatile("" : "+v"(w1f[16]), "+v"(w1f[17]), "+v"(w1f[18]), "+v"(w1f[19]), "+v"(w1f[20]), "+v"(w1f[21]), "+v"(w1f[22]), "+v"(w1f[23]));
            dma_run_sc1<2>(xsrc + 2 * kq * 1024, xoff, xdst + 2 * kq * 1024);
            asm volatile("s_barrier" ::: "memory");
            dma_run_sc1<6>(xsrc + (8 + 6 * kq) * 1024, xoff, xdst + (8 + 6 * kq) * 1024);
            WAITVM(6);
            LDS_BARRIER();
            STAMP(22 + L * 32);
            f32x16 a0 = {}, a1 = {};
            Sums sm; sm.clear();
            const unsigned char* w2a_src = ws + OFF_W2 + ((2 * j + (w & 1)) * KSFP + (w >> 1) * 22 + W2_REG) * 1024;
            auto req = [&](int n) {
                if (n < 5) dma4_sc1<1>(w2a_src + (6 + n) * 1024, vl, lbase + L_W2A + (w * W2_A + 6 + n) * 1024);
                else if (n < 5 + W2_REG) w2A[n - 5] = ld_sc1(rw, vw, OFF_W2 + (cbA * KSFP + kq * 22 + n - 5) * 1024);
                else if (n < 5 + 2 * W2_REG) w2B[n - 5 - W2_REG] = ld_sc1(rw, vw, OFF_W2 + (cbB * KSFP + kq * 22 + n - 5 - W2_REG) * 1024);
            };
            u32x4 sv[6];
            auto stat_read = [&](int k0, int n) {
#pragma unroll
                for (int i = 0; i < 6; ++i) if (i < n) sv[i] = lds_ld(lds, L_ACT + (tb * KSD + k0 + i) * 1024 + vl);
            };
            auto stat_step = [&](int first, int n, int s) { if (s >= first && s < first + n) sm.add(sv[s - first]); };
            const int act = L_ACT + vl;
            {
                constexpr int N = TB == 0 ? 8 : 4, ST = TB == 0 ? 1 : 2;
                const int ab = act + par * 1024;
                constexpr int PD = 4;
                u32x4 b0[PD + 1], b1[PD + 1];
                stat_read(2 * kq, 2);
#pragma unroll
                for (int i = 0; i < PD; ++i) { b0[i] = lds_ld(lds, ab + ST * i * 1024); b1[i] = lds_ld(lds, ab + (KSD + ST * i) * 1024); }
#pragma unroll
                for (int s = 0; s < N; ++s) {
                    if (s + PD < N) { b0[(s + PD) % (PD + 1)] = lds_ld(lds, ab + ST * (s + PD) * 1024); b1[(s + PD) % (PD + 1)] = lds_ld(lds, ab + (KSD + ST * (s + PD)) * 1024); }
                    SBAR();
                    a0 = mfma(w1f[s], b0[s % (PD + 1)], a0); a1 = mfma(w1f[s], b1[s % (PD + 1)], a1);
                    PIN2(a0, a1);
                    stat_step(N - 2, 2, s);
                    SBAR();
                }
            }
            STAMP(23 + L * 32);
            WAITVM(0);
            LDS_BARRIER();
            if (TB == 0) {
                constexpr int PD = 7;
                u32x4 b0[PD + 1];
                stat_read(8 + 6 * kq, 6);
#pragma unroll
                for (int i = 0; i < PD; ++i) b0[i] = lds_ld(lds, act + (8 + i) * 1024);
#pragma unroll
                for (int s = 0; s < 24; ++s) {
                    if (s + PD < 24) b0[(s + PD) % (PD + 1)] = lds_ld(lds, act + (8 + s + PD) * 1024);
                    SBAR();
                    a0 = mfma(w1f[8 + s], b0[s % (PD + 1)], a0);
                    asm volatile("" : "+v"(a0));
                    if ((s & 1) == 0 && s / 2 < 11) req(s / 2);
                    stat_step(10, 6, s);
                    SBAR();
                }
            } else {
                const int ab = act + (8 + par) * 1024;
                constexpr int PD = 4;
                u32x4 b0[PD + 1], b1[PD + 1];
                stat_read(8 + 6 * kq, 6);
#pragma unroll
                for (int i = 0; i < PD; ++i) { b0[i] = lds_ld(lds, ab + 2 * i * 1024); b1[i] = lds_ld(lds, ab + (KSD + 2 * i) * 1024); }
#pragma unroll
                for (int s = 0; s < 12; ++s) {
                    if (s + PD < 12) { b0[(s + PD) % (PD + 1)] = lds_ld(lds, ab + 2 * (s + PD) * 1024); b1[(s + PD) % (PD + 1)] = lds_ld(lds, ab + (KSD + 2 * (s + PD)) * 1024); }
                    SBAR();
                    const int f = 4 + s < 8 ? 4 + s : 12 + s;
                    a0 = mfma(w1f[f], b0[s % (PD + 1)], a0); a1 = mfma(w1f[f], b1[s % (PD + 1)], a1);
                    PIN2(a0, a1);
                    stat_step(5, 6, s);
                    SBAR();
                }
            }
            { const float u1 = half_sum(sm.sum()), u2 = half_sum(sm.sumsq()); if (h == 0) ((LAS f32x2*)(lds + L_STAT))[(tb * 8 + kq) * 32 + r] = (f32x2){u1, u2}; }
            STAMP(24 + L * 32);
            const int xslot = L_ACT + (kq >> 1) * KSD * 1024 + (kq & 1) * 4096 + vl, xslot_p = L_ACT + (kq >> 1) * KSD * 1024 + ((kq & 1) ^ 1) * 4096 + vl;
            if (TB == 1) {
#pragma unroll
                for (int g = 0; g < 4; ++g) {
                    const f32x4 v = par ? (f32x4){a0[4 * g], a0[4 * g + 1], a0[4 * g + 2], a0[4 * g + 3]} : (f32x4){a1[4 * g], a1[4 * g + 1], a1[4 * g + 2], a1[4 * g + 3]};
                    lds_stf(lds, xslot + g * 1024, v);
                }
            }
            LDS_BARRIER();
            STAMP(25 + L * 32);
            constexpr float G1 = -2.f * 0.7978845608028654f * 1.4426950408889634f, G3 = G1 * 0.044715f;
            f32x4 tcs[4], tbb[4];
#pragma unroll
            for (int g = 0; g < 4; ++g) { tcs[g] = lds_ldf(lds, L_TAB + (32 * cb1 + 8 * g + 4 * h) * 4); tbb[g] = lds_ldf(lds, L_TAB + 768 + (32 * cb1 + 8 * g + 4 * h) * 4); }
            f32x2 gx[8], ge[8];
            float g_rstd = 0.f, g_nmr = 0.f;
            auto gelu_slice = [&](const f32x16& acc, int t, int n) {
                if (n == 0) { float t1, t2; stat_sums<4>(lds, t * 8, r, t1, t2); float mu; ln_from_sums(t1, t2, mu, g_rstd); g_nmr = -mu * g_rstd; }
                else if (n <= 3) {
#pragma unroll
                    for (int i = (n - 1) * 3; i < (n == 3 ? 8 : n * 3); ++i) {
                        const f32x2 a2 = {acc[2 * i], acc[2 * i + 1]}, c2 = {tcs[i >> 1][2 * (i & 1)], tcs[i >> 1][2 * (i & 1) + 1]}, b2 = {tbb[i >> 1][2 * (i & 1)], tbb[i >> 1][2 * (i & 1) + 1]};
                        gx[i] = __builtin_elementwise_fma(a2, (f32x2){g_rstd, g_rstd}, __builtin_elementwise_fma(c2, (f32x2){g_nmr, g_nmr}, b2));
                        asm volatile("" : "+v"(gx[i]));
                    }
                } else if (n <= 7) {
#pragma unroll
                    for (int i = (n - 4) * 2; i < (n - 4) * 2 + 2; ++i) { ge[i] = gx[i] * __builtin_elementwise_fma(gx[i] * gx[i], (f32x2){G3, G3}, (f32x2){G1, G1}); asm volatile("" : "+v"(ge[i])); }
                } else if (n <= 13) {
#pragma unroll
                    for (int i = (n - 8) * 3; i < min((n - 8) * 3 + 3, 16); ++i) ge[i >> 1][i & 1] = __builtin_amdgcn_exp2f(ge[i >> 1][i & 1]);
                    asm volatile("" : "+v"(ge[min((n - 8) * 3, 15) >> 1]), "+v"(ge[min((n - 8) * 3 + 2, 15) >> 1]));
                } else if (n == 14) {
#pragma unroll
                    for (int i = 0; i < 8; ++i) ge[i] = ge[i] + (f32x2){1.f, 1.f};
                    PIN8(ge);
                } else if (n <= 20) {
#pragma unroll
                    for (int i = (n - 15) * 3; i < min((n - 15) * 3 + 3, 16); ++i) ge[i >> 1][i & 1] = __builtin_amdgcn_rcpf(ge[i >> 1][i & 1]);
                    asm volatile("" : "+v"(ge[min((n - 15) * 3, 15) >> 1]), "+v"(ge[min((n - 15) * 3 + 2, 15) >> 1]));
                } else if (n == 21) {
#pragma unroll
                    for (int i = 0; i < 8; ++i) gx[i] = gx[i] * ge[i];
                    PIN8(gx);
                } else if (n == 22 && d_active) {
                    const int so = OFF_H + L * H_BYTES + ((2 * c + t) * KSF + 2 * cbg1) * 1024;
                    st_sc1(swap_order((u32x4){pk(gx[0][0], gx[0][1]), pk(gx[1][0], gx[1][1]), pk(gx[2][0], gx[2][1]), pk(gx[3][0], gx[3][1])}), rw, vl, so);
                    st_sc1(swap_order((u32x4){pk(gx[4][0], gx[4][1]), pk(gx[5][0], gx[5][1]), pk(gx[6][0], gx[6][1]), pk(gx[7][0], gx[7][1])}), rw, vl, so + 1024);
                }
            };
            if (TB == 0) {
                constexpr int PD = 5;
                u32x4 b1[PD + 1];
#pragma unroll
                for (int i = 0; i < PD; ++i) b1[i] = lds_ld(lds, act + (KSD + 8 + i) * 1024);
#pragma unroll
                for (int s = 0; s < 24; ++s) {
                    if (s + PD < 24) b1[(s + PD) % (PD + 1)] = lds_ld(lds, act + (KSD + 8 + s + PD) * 1024);
                    SBAR();
                    a1 = mfma(w1f[8 + s], b1[s % (PD + 1)], a1);
                    asm volatile("" : "+v"(a1));
                    gelu_slice(a0, 0, s);
                    SBAR();
                }
#pragma unroll
                for (int n = 0; n < 23; ++n) gelu_slice(a1, 1, n);
            } else {
                f32x4 v[4];
#pragma unroll
                for (int g = 0; g < 4; ++g) v[g] = lds_ldf(lds, xslot_p + g * 1024);
                SBAR();
#pragma unroll
                for (int g = 0; g < 4; ++g)
#pragma unroll
                    for (int i = 0; i < 4; ++i) a0[4 * g + i] = (par ? a1[4 * g + i] : a0[4 * g + i]) + v[g][i];
#pragma unroll
                for (int n = 0; n < 23; ++n) { if (n < 11) req(n); gelu_slice(a0, par, n); SBAR(); }
            }
            STAMP(26 + L * 32);
            publish(tid, cnt + cnt_word(L, CNT_H, c));
            STAMP(27 + L * 32);
        }
        {
            PHASE_IDS();
            dma_run_sc1<8>(ws + OFF_W2 + ((2 * j + (w & 1)) * KSFP + (w >> 1) * 22 + W2_REG + W2_A) * 1024, vl, lbase + L_W2B + 8 * w * 1024);
            const u32x2 bb0 = *(const GAS u32x2*)(kp->b2 + 16 * sown + 4 * h), bb1 = *(const GAS u32x2*)(kp->b2 + 16 * sown + 8 + 4 * h);
            int hso = OFF_H + L * H_BYTES + (tbg * KSF + kq * 22) * 1024, hso2 = hso + (kq == 3 ? 18 : 20) * 1024, hvo = vl;
            asm volatile("" : "+s"(hso), "+s"(hso2)); asm volatile("" : "+v"(hvo));
            auto ld_h = [&](int i) { return !LR_E ? ld_sc1(rw, vl, OFF_H + L * H_BYTES + tbg * KSF * 1024 + min(kq * 22 + i, KSF - 1) * 1024) : i < 20 ? ld_run(rw, hvo, hso, i) : ld_run(rw, hvo, hso2, i - 20); };
            if (tid == 0) poll_ge(cnt + cnt_word(L, CNT_H, c), 8u, tmo);
            __builtin_amdgcn_fence(__ATOMIC_ACQUIRE, "wavefront");
            LDS_BARRIER();
            STAMP(28 + L * 32);
            u32x4 ha[22];
            constexpr int EWIN = EWIN_;
#pragma unroll
            for (int i = 0; i < EWIN; ++i) ha[i] = ld_h(i);
            SBAR();
            f32x16 aA = {}, aB = {};
            const int aA0 = L_W2A + ((kq * 2 + (kq >> 1)) * W2_A) * 1024 + vw, aB0 = L_W2A + ((kq * 2 + ((kq >> 1) ^ 1)) * W2_A) * 1024 + vw;
            const int bA0 = L_W2B + ((kq * 2 + (kq >> 1)) * W2_B) * 1024 + vw, bB0 = L_W2B + ((kq * 2 + ((kq >> 1) ^ 1)) * W2_B) * 1024 + vw;
            constexpr int PD = 4;
            u32x4 la[PD + 1], lb[PD + 1];
            auto w_lds = [&](int n) {
                if (n >= W2_REG && n < W2_REG + W2_A) { la[n % (PD + 1)] = lds_ld(lds, aA0 + (n - W2_REG) * 1024); lb[n % (PD + 1)] = lds_ld(lds, aB0 + (n - W2_REG) * 1024); }
                else if (n >= W2_REG + W2_A && n < 22) { la[n % (PD + 1)] = lds_ld(lds, bA0 + (n - W2_REG - W2_A) * 1024); lb[n % (PD + 1)] = lds_ld(lds, bB0 + (n - W2_REG - W2_A) * 1024); }
            };
#pragma unroll
            for (int n = 0; n < PD; ++n) w_lds(n);
#pragma unroll
            for (int i = 0; i < 22; ++i) {
                if (i + EWIN < 22) ha[i + EWIN] = ld_h(i + EWIN);
                if (i + PD == W2_REG + W2_A) asm volatile("s_barrier" ::: "memory");
                w_lds(i + PD);
                SBAR();
                if (i < W2_REG) { aA = mfma(w2A[i], ha[i], aA); aB = mfma(w2B[i], ha[i], aB); }
                else { aA = mfma(la[i % (PD + 1)], ha[i], aA); aB = mfma(lb[i % (PD + 1)], ha[i], aB); }
                PIN2(aA, aB);
                SBAR();
            }
            STAMP(29 + L * 32);
            LDS_BARRIER();
            if (L == 0) {
                const int row = lane >> 3, pc = lane & 7;
                const uint16_t* src = kp->wq + (size_t)(64 * w + row) * D + 64 * j + (((pc >> 2) ^ ((row >> 1) & 1)) * 32 + (pc & 3) * 8);
                const bool mine = !(j == tb);
                const uint16_t* src2 = kp->wq + (size_t)(64 * (w ^ 4) + row) * D + 64 * j + (((pc >> 2) ^ ((row >> 1) & 1)) * 32 + (pc & 3) * 8);
                if (!mine) REQ_WKV();
                if (mine) {
#pragma unroll
                    for (int i = 0; i < 4; ++i) glds16(src + i * 8 * D, lbase + L_WQ + (w * 8 + i) * 1024);
                    if (j < 2) {
#pragma unroll
                        for (int i = 0; i < 4; ++i) glds16(src2 + i * 8 * D, lbase + L_WQ + ((w ^ 4) * 8 + i) * 1024);
                    }
                }
            }
            float o[8];
            reduce4(lds, tb * 24576, aA, aB, kq, lane, o);
#pragma unroll
            for (int i = 0; i < 2; ++i) { o[2 * i] += bflo(bb0[i]); o[2 * i + 1] += bfhi(bb0[i]); o[4 + 2 * i] += bflo(bb1[i]); o[4 + 2 * i + 1] += bfhi(bb1[i]); }
#pragma unroll
            for (int i = 0; i < 4; ++i) resid[i] = pk(bflo(resid[i]) + o[2 * i], bfhi(resid[i]) + o[2 * i + 1]);
            STAMP(30 + L * 32);
            const u32x4 xo = swap_order(resid);
            if (L == 0) {
                st_sc1(xo, rw, vl, OFF_X2 + (tbg * KSD + sown) * 1024);
                publish(tid, cnt + cnt_word(0, CNT_X2, c));
            } else {
                *(GAS u32x4*)(kp->out + (size_t)(64 * c + 32 * tb + r) * D + 16 * sown + 8 * h) = xo;
            }
            STAMP(31 + L * 32);
        }
    }
    { unsigned e = epoch0; asm volatile("" : "+s"(e)); if (blockIdx.x == 0 && threadIdx.x == 0) __hip_atomic_store((gu32*)(ws0 + OFF_SYNC), e + 2u, RLX_AGENT); }
}

__global__ void __launch_bounds__(512, 2) main_kernel(Params p) {
    extern __shared__ __attribute__((aligned(16))) char lds_dyn[];
    LAS char* lds = (LAS char*)lds_dyn;
    asm volatile("" :: "s"(p.x), "s"(p.wq), "s"(p.wk), "s"(p.wv), "s"(p.w1), "s"(p.b2), "s"(p.out));
    if (__builtin_amdgcn_readfirstlane(threadIdx.x >> 8) == 0) run_layers<0>(p.ws, lds); else run_layers<1>(p.ws, lds);
}

}

extern "C" void kernel_launch(void* const* d_in, const int* in_sizes, int n_in, void* d_out, int out_size, void* d_ws, size_t ws_size, hipStream_t stream) {
    Params p;
    p.x = (const uint16_t*)d_in[0]; p.wq = (const uint16_t*)d_in[1]; p.wk = (const uint16_t*)d_in[2]; p.wv = (const uint16_t*)d_in[3]; p.wo = (const uint16_t*)d_in[4];
    p.w1 = (const uint16_t*)d_in[6]; p.b1 = (const uint16_t*)d_in[7]; p.w2 = (const uint16_t*)d_in[8]; p.b2 = (const uint16_t*)d_in[9];
    p.out = (uint16_t*)d_out; p.ws = (unsigned char*)d_ws;
    (void)hipFuncSetAttribute((const void*)main_kernel, hipFuncAttributeMaxDynamicSharedMemorySize, LDS_BYTES);
    hipLaunchKernelGGL(main_kernel, dim3(256), dim3(512), LDS_BYTES, stream, p);
}
```

```cpp
#include <hip/hip_runtime.h>
#include <hip/hip_bf16.h>
#include <stdint.h>

#define GAS __attribute__((address_space(1)))
#define LAS __attribute__((address_space(3)))
typedef __bf16 bf16x8 __attribute__((ext_vector_type(8)));
typedef __bf16 bf16x2 __attribute__((ext_vector_type(2)));
typedef float f32x16 __attribute__((ext_vector_type(16)));
typedef float f32x4 __attribute__((ext_vector_type(4)));
typedef float f32x2 __attribute__((ext_vector_type(2)));
typedef unsigned u32x4 __attribute__((ext_vector_type(4)));
typedef GAS unsigned gu32;
#define RLX_AGENT __ATOMIC_RELAXED, __HIP_MEMORY_SCOPE_AGENT

namespace {

constexpr int S = 2048, D = 512, F = 1376;
constexpr int KSD = 32;
constexpr int KSF = 86;
constexpr int KSFP = 88;
constexpr float LN_EPS = 1e-5f;
constexpr float QSCALE = 0.25f * 1.4426950408889634f;
constexpr float SAFE_BOUND = 50.f;

constexpr int CW = 64;
constexpr int OFF_CNT = 0;
constexpr int CNT_O = 0, CNT_X1 = 1, CNT_H = 2, CNT_X2 = 3;
__device__ __forceinline__ int cnt_word(int layer, int kind, int c) { return ((layer * 4 + kind) * 32 + c) * CW; }
constexpr int OFF_PREPC = 65536;
constexpr int OFF_SYNC = OFF_PREPC + 32 * 256;
constexpr int OFF_KVFL = OFF_SYNC + 1024;
constexpr int OFF_WO = OFF_KVFL + 2 * 32 * 512;
constexpr int OFF_W1 = OFF_WO + 16 * KSD * 1024;
constexpr int OFF_W2 = OFF_W1 + 43 * KSD * 1024;
constexpr int OFF_CS1 = OFF_W2 + 16 * KSFP * 1024;
constexpr int OFF_KF = (OFF_CS1 + 1408 * 4 + 1023) / 1024 * 1024;
constexpr int OFF_VF = OFF_KF + 2 * 65536;
constexpr int OFF_O = OFF_VF + 2 * 65536;
constexpr int ACT_BYTES = 64 * KSD * 1024;
constexpr int OFF_X1 = OFF_O + 2 * ACT_BYTES;
constexpr int OFF_X2 = OFF_X1 + 2 * ACT_BYTES;
constexpr int H_BYTES = 64 * KSF * 1024;
constexpr int OFF_H = OFF_X2 + ACT_BYTES;
constexpr int WS_TOTAL = OFF_H + 2 * H_BYTES;
constexpr unsigned SPIN_CAP = 1u << 15;

constexpr int L_K = 0, L_V = 65536, L_ONES = 131072, L_XCHO = 135424;
constexpr int L_WQ = 65536, L_WKV0 = 49152, L_WKV1 = 131072, L_PREP = 0, L_KVRED = 32768, L_XCHQ = 57344;
constexpr int L_ACT = 0, L_W2A = 65536, L_W2B = 0;
constexpr int L_STAT = 155648, L_TAB = 159744, L_CS = L_TAB, L_FLAG = 163584;
constexpr int F_UNSAFE = L_FLAG, F_KM2 = L_FLAG + 32, F_N2 = L_FLAG + 36, F_KVCNT = L_FLAG + 40, F_OCNT = L_FLAG + 44, F_PAIR = L_FLAG + 64;
constexpr int LDS_BYTES = 163840;
constexpr int W2_REG = 3, W2_A = 11, W2_B = 8;
constexpr int W1_EARLY = 24;

constexpr int EWIN = 5;
constexpr int W1_PAUSE = 3;
constexpr int NCH = 4;

struct Params {
    const uint16_t *x, *wq, *wk, *wv, *wo, *w1, *b1, *w2, *b2;
    uint16_t* out;
    unsigned char* ws;
};

__device__ __forceinline__ f32x16 mfma(u32x4 a, u32x4 b, f32x16 c) {
    return __builtin_amdgcn_mfma_f32_32x32x16_bf16(__builtin_bit_cast(bf16x8, a), __builtin_bit_cast(bf16x8, b), c, 0, 0, 0);
}
__device__ __forceinline__ unsigned pk(float a, float b) { f32x2 v = {a, b}; return __builtin_bit_cast(unsigned, __builtin_convertvector(v, bf16x2)); }
__device__ __forceinline__ float bflo(unsigned u) { return __uint_as_float(u << 16); }
__device__ __forceinline__ float bfhi(unsigned u) { return __uint_as_float(u & 0xffff0000u); }
__device__ __forceinline__ float bf2f(uint16_t v) { return __uint_as_float((unsigned)v << 16); }
__device__ __forceinline__ float dot2(unsigned a, unsigned b, float c) {
    return __builtin_amdgcn_fdot2_f32_bf16(__builtin_bit_cast(bf16x2, a), __builtin_bit_cast(bf16x2, b), c, false);
}
__device__ __forceinline__ float half_sum(float v) {
    auto r = __builtin_amdgcn_permlane32_swap(__float_as_uint(v), __float_as_uint(v), false, false);
    return __uint_as_float(r[0]) + __uint_as_float(r[1]);
}
__device__ __forceinline__ float half_max(float v) {
    auto r = __builtin_amdgcn_permlane32_swap(__float_as_uint(v), __float_as_uint(v), false, false);
    return fmaxf(__uint_as_float(r[0]), __uint_as_float(r[1]));
}
__device__ __forceinline__ u32x4 swap_order(u32x4 a) {
    auto r0 = __builtin_amdgcn_permlane32_swap(a[0], a[2], false, false);
    auto r1 = __builtin_amdgcn_permlane32_swap(a[1], a[3], false, false);
    return (u32x4){r0[0], r1[0], r0[1], r1[1]};
}
template <int CTRL> __device__ __forceinline__ float dpp(float v) {
    return __uint_as_float(__builtin_amdgcn_update_dpp(0u, __float_as_uint(v), CTRL, 0xf, 0xf, true));
}
__device__ __forceinline__ float wave_max(float v) {
    v = fmaxf(v, dpp<0xB1>(v));
    v = fmaxf(v, dpp<0x4E>(v));
    v = fmaxf(v, dpp<0x141>(v));
    v = fmaxf(v, dpp<0x140>(v));
    auto r = __builtin_amdgcn_permlane16_swap(__float_as_uint(v), __float_as_uint(v), false, false);
    return half_max(fmaxf(__uint_as_float(r[0]), __uint_as_float(r[1])));
}
#define SBAR() __builtin_amdgcn_sched_barrier(0)
#define PIN8(x) asm volatile("" : "+v"(x[0]), "+v"(x[1]), "+v"(x[2]), "+v"(x[3]), "+v"(x[4]), "+v"(x[5]), "+v"(x[6]), "+v"(x[7]))
#define PIN2(a, b) asm volatile("" : "+v"(a), "+v"(b))
#define LDS_BARRIER() asm volatile("s_waitcnt lgkmcnt(0)\n\ts_barrier" ::: "memory")
#define DRAIN() asm volatile("s_waitcnt vmcnt(0)" ::: "memory")
constexpr int AUX_SC1 = 16;
typedef __amdgpu_buffer_rsrc_t rsrc_t;
__device__ __forceinline__ u32x4 ld_sc1(rsrc_t r, int voff, int soff) { return __builtin_amdgcn_raw_buffer_load_b128(r, voff, soff, AUX_SC1); }
__device__ __forceinline__ u32x4 ld_plain(rsrc_t r, int voff, int soff) { return __builtin_amdgcn_raw_buffer_load_b128(r, voff, soff, 0); }
__device__ __forceinline__ void st_sc1(u32x4 v, rsrc_t r, int voff, int soff) {
    __builtin_amdgcn_raw_buffer_store_b128(v, r, voff, soff, AUX_SC1);
    asm volatile("s_nop 1" :: "v"(v) : "memory");
}
__device__ __forceinline__ u32x4 lds_ld(LAS char* l, int off) { return *(LAS u32x4*)(l + off); }
__device__ __forceinline__ void lds_st(LAS char* l, int off, u32x4 v) { *(LAS u32x4*)(l + off) = v; }
__device__ __forceinline__ f32x4 lds_ldf(LAS char* l, int off) { return *(LAS f32x4*)(l + off); }
__device__ __forceinline__ void lds_stf(LAS char* l, int off, f32x4 v) { *(LAS f32x4*)(l + off) = v; }

__device__ __forceinline__ void publish(int tid, gu32* cnt) {
    DRAIN();
    LDS_BARRIER();
    if (tid == 0) __hip_atomic_fetch_add(cnt, 1u, RLX_AGENT);
}
__device__ __forceinline__ void poll_ge(gu32* cnt, unsigned want, gu32* tmo) {
    unsigned spins = 0;
    while (__hip_atomic_load(cnt, RLX_AGENT) < want) {
        if (++spins > SPIN_CAP) { __hip_atomic_store(tmo, 1u, RLX_AGENT); break; }
    }
}
__device__ __forceinline__ void wait_cnt(int tid, gu32* cnt, unsigned want, gu32* tmo) {
    if (tid == 0) poll_ge(cnt, want, tmo);
    __builtin_amdgcn_fence(__ATOMIC_ACQUIRE, "wavefront");
    LDS_BARRIER();
}
typedef GAS unsigned long long gu64;
__device__ __forceinline__ float wait_flags(int tid, LAS char* lds, gu64* fl, unsigned tag, gu32* tmo) {
    if (tid < 64) {
        unsigned spins = 0; unsigned long long v;
        while (v = __hip_atomic_load(fl + tid, RLX_AGENT), !__all((unsigned)(v >> 32) == tag)) {
            if (++spins > SPIN_CAP) { if (tid == 0) __hip_atomic_store(tmo, 1u, RLX_AGENT); break; }
        }
        const float m = wave_max(__uint_as_float((unsigned)v));
        if (tid == 0) *(LAS float*)(lds + F_KM2) = m;
    }
    __builtin_amdgcn_fence(__ATOMIC_ACQUIRE, "wavefront");
    LDS_BARRIER();
    return *(LAS float*)(lds + F_KM2);
}
__device__ __forceinline__ void glds16(const void* g, unsigned lds_dst) {
    unsigned keep;
    asm volatile("s_mov_b32 %0, m0\n\ts_mov_b32 m0, %2\n\ts_nop 0\n\tglobal_load_lds_dwordx4 %1, off\n\ts_mov_b32 m0, %0" : "=&s"(keep) : "v"(g), "s"(lds_dst) : "memory");
}
__device__ __forceinline__ void glds16_sc1(const void* g, unsigned lds_dst) {
    unsigned keep;
    asm volatile("s_mov_b32 %0, m0\n\ts_mov_b32 m0, %2\n\ts_nop 0\n\tglobal_load_lds_dwordx4 %1, off sc1\n\ts_mov_b32 m0, %0" : "=&s"(keep) : "v"(g), "s"(lds_dst) : "memory");
}
template <int N>
__device__ __forceinline__ void dma4_sc1(const unsigned char* g, unsigned voff, unsigned lds_dst) {
    if constexpr (N == 1) asm volatile("s_mov_b32 m0, %2\n\ts_nop 0\n\tglobal_load_lds_dwordx4 %0, %1 sc1" :: "v"(voff), "s"(g), "s"(lds_dst) : "memory");
    if constexpr (N == 2) asm volatile("s_mov_b32 m0, %2\n\ts_nop 0\n\tglobal_load_lds_dwordx4 %0, %1 sc1\n\tglobal_load_lds_dwordx4 %0, %1 offset:1024 sc1" :: "v"(voff), "s"(g), "s"(lds_dst) : "memory");
    if constexpr (N == 3) asm volatile("s_mov_b32 m0, %2\n\ts_nop 0\n\tglobal_load_lds_dwordx4 %0, %1 sc1\n\tglobal_load_lds_dwordx4 %0, %1 offset:1024 sc1\n\t"
                                       "global_load_lds_dwordx4 %0, %1 offset:2048 sc1" :: "v"(voff), "s"(g), "s"(lds_dst) : "memory");
    if constexpr (N == 4) asm volatile("s_mov_b32 m0, %2\n\ts_nop 0\n\tglobal_load_lds_dwordx4 %0, %1 sc1\n\tglobal_load_lds_dwordx4 %0, %1 offset:1024 sc1\n\t"
                                       "global_load_lds_dwordx4 %0, %1 offset:2048 sc1\n\tglobal_load_lds_dwordx4 %0, %1 offset:3072 sc1" :: "v"(voff), "s"(g), "s"(lds_dst) : "memory");
}
template <int N>
__device__ __forceinline__ void dma_run_sc1(const unsigned char* g, unsigned voff, unsigned lds_dst) {
    if constexpr (N <= 4) dma4_sc1<N>(g, voff, lds_dst);
    else { dma4_sc1<4>(g, voff, lds_dst); dma_run_sc1<N - 4>(g + 4096, voff, lds_dst + 4096); }
}
#define WAITVM(n) asm volatile("s_waitcnt vmcnt(" #n ")" ::: "memory")

__device__ __forceinline__ void reduce4(LAS char* lds, int region, const f32x16& aA, const f32x16& aB, int kq, int lane, float (&o)[8]) {
    const int base = region + lane * 16;
    { const int a = base + ((kq ^ 1) * 3 + 0) * 2048;
      lds_stf(lds, a, (f32x4){aA[8], aA[9], aA[10], aA[11]}); lds_stf(lds, a + 1024, (f32x4){aA[12], aA[13], aA[14], aA[15]}); }
    { const int a = base + ((kq ^ 2) * 3 + 1) * 2048;
      lds_stf(lds, a, (f32x4){aB[0], aB[1], aB[2], aB[3]}); lds_stf(lds, a + 1024, (f32x4){aB[4], aB[5], aB[6], aB[7]}); }
    { const int a = base + ((kq ^ 3) * 3 + 2) * 2048;
      lds_stf(lds, a, (f32x4){aB[8], aB[9], aB[10], aB[11]}); lds_stf(lds, a + 1024, (f32x4){aB[12], aB[13], aB[14], aB[15]}); }
    LDS_BARRIER();
    f32x4 lo = {aA[0], aA[1], aA[2], aA[3]}, hi = {aA[4], aA[5], aA[6], aA[7]}, t[6];
#pragma unroll
    for (int x = 0; x < 6; ++x) t[x] = lds_ldf(lds, base + kq * 3 * 2048 + x * 1024);
    SBAR();
#pragma unroll
    for (int x = 0; x < 3; ++x) { lo += t[2 * x]; hi += t[2 * x + 1]; }
#pragma unroll
    for (int i = 0; i < 4; ++i) { o[i] = lo[i]; o[4 + i] = hi[i]; }
}
typedef short v4i16 __attribute__((ext_vector_type(4)));
typedef unsigned u32x2 __attribute__((ext_vector_type(2)));
__device__ __forceinline__ u32x4 tr_frag(LAS char* lds, int a, int step) {
    const u32x2 lo = __builtin_bit_cast(u32x2, __builtin_amdgcn_ds_read_tr16_b64_v4i16((LAS v4i16*)(lds + a)));
    const u32x2 hi = __builtin_bit_cast(u32x2, __builtin_amdgcn_ds_read_tr16_b64_v4i16((LAS v4i16*)(lds + a + step)));
    return (u32x4){lo[0], lo[1], hi[0], hi[1]};
}
struct ColSum { float a[4];
    __device__ __forceinline__ void clear() { _Pragma("unroll") for (int d = 0; d < 4; ++d) a[d] = 0.f; }
    __device__ __forceinline__ void add(u32x4 f) { _Pragma("unroll") for (int d = 0; d < 4; ++d) a[d % NCH] = dot2(f[d], 0x3f803f80u, a[d % NCH]); }
    __device__ __forceinline__ float sum() const { return (a[0] + a[1]) + (a[2] + a[3]); }
};

#define A_ALIGN ".p2align 5\n\t"
#define A_E1(a) "v_exp_f32 v" #a ", v" #a "\n\t"
#define A_E4(a, b, c, d) A_E1(a) A_E1(b) A_E1(c) A_E1(d)
#define A_CVT(d, a, b) "v_cvt_pk_bf16_f32 v" #d ", v" #a ", v" #b "\n\t"
#define A_DSK(off) "ds_read_b128 v[92:95], v112 offset:" #off "\n\t"
#define A_DSV0(off) "ds_read_b128 v[96:99], v113 offset:" #off "\n\t"
#define A_DSV1(off) "ds_read_b128 v[100:103], v113 offset:" #off "\n\t"
#define A_WAIT(n) "s_waitcnt lgkmcnt(" #n ")\n\t"
#define A_MSB "v_mfma_f32_32x32x16_bf16 v[40:55], v[92:95], v[148:151], v[116:131]\n\t"
#define A_MSA "v_mfma_f32_32x32x16_bf16 v[24:39], v[92:95], v[88:91], v[56:71]\n\t"
#define A_MPV(O, V, P) "v_mfma_f32_32x32x16_bf16 " O ", " V ", " P ", " O "\n\t"
#define A_OA "v[72:87]"
#define A_OB "v[132:147]"
#define A_V0 "v[96:99]"
#define A_V1 "v[100:103]"
#define A_P0 "v[104:107]"
#define A_P1 "v[108:111]"
#define A_STEP(koff, v1off, v0off) \
    A_MSB A_DSK(koff) A_E4(24, 25, 26, 27) \
    A_MPV(A_OB, A_V1, A_P1) A_DSV1(v1off) A_E4(28, 29, 30, 31) \
    A_CVT(104, 24, 25) A_CVT(105, 26, 27) A_CVT(106, 28, 29) A_CVT(107, 30, 31) \
    A_E4(32, 33, 34, 35) \
    A_WAIT(2) A_MPV(A_OA, A_V0, A_P0) A_E4(36, 37, 38, 39) \
    A_CVT(108, 32, 33) A_CVT(109, 34, 35) A_CVT(110, 36, 37) A_CVT(111, 38, 39) \
    A_WAIT(1) A_MSA A_E4(40, 41, 42, 43) \
    A_WAIT(0) A_MPV(A_OA, A_V1, A_P1) A_E4(44, 45, 46, 47) \
    A_CVT(104, 40, 41) A_CVT(105, 42, 43) A_CVT(106, 44, 45) A_CVT(107, 46, 47) \
    A_E4(48, 49, 50, 51) \
    A_MPV(A_OB, A_V0, A_P0) A_DSV0(v0off) A_E4(52, 53, 54, 55) \
    A_CVT(108, 48, 49) A_CVT(109, 50, 51) A_CVT(110, 52, 53) A_CVT(111, 54, 55)

struct AttnState { f32x16 sa, sb, oa, ob, negma, negmb; u32x4 qa, qb, kf, v0, v1, p1; unsigned kaddr, vaddr, vstride; };
#define A_ITER \
        A_STEP(1024, 512, 1024) \
        A_STEP(2048, 1536, 2048) \
        A_STEP(3072, 2560, 3072) \
        A_STEP(4096, 3584, 4096) \
        "v_add_u32 v112, 4096, v112\n\t" \
        "v_add_u32 v113, v114, v113\n\t"
__device__ __forceinline__ void attn_begin(AttnState& st, LAS char* lds, u32x4 qa, u32x4 qb, float ma, float mb, unsigned kaddr, unsigned vaddr, unsigned vstride) {
#pragma unroll
    for (int i = 0; i < 16; ++i) { st.negma[i] = -ma; st.negmb[i] = -mb; }
    st.qa = qa; st.qb = qb;
    st.kf = lds_ld(lds, kaddr); st.v0 = lds_ld(lds, vaddr); st.v1 = st.v0; st.p1 = (u32x4){0u, 0u, 0u, 0u};
    st.sa = mfma(st.kf, qa, st.negma); st.sb = st.sa;
    st.oa = (f32x16){}; st.ob = (f32x16){};
    const unsigned lbase = (unsigned)(uintptr_t)lds;
    st.kaddr = kaddr + lbase; st.vaddr = vaddr + lbase; st.vstride = vstride;
}
#define A_DMA \
    "s_mov_b32 m0, s92\n\t" \
    "s_add_u32 s92, s92, 1024\n\t" \
    "global_load_lds_dwordx4 v115, s[90:91] sc1\n\t" \
    "s_add_u32 s90, s90, 1024\n\t" \
    "s_addc_u32 s91, s91, 0\n\t"
#define A_ITER_DMA \
        A_DMA A_DMA A_STEP(1024, 512, 1024) \
        A_DMA A_STEP(2048, 1536, 2048) \
        A_DMA A_STEP(3072, 2560, 3072) \
        A_DMA A_STEP(4096, 3584, 4096) \
        "v_add_u32 v112, 4096, v112\n\t" \
        "v_add_u32 v113, v114, v113\n\t"
__device__ __forceinline__ void attn_first(AttnState& st, unsigned long long src, unsigned dst, unsigned voff) {
    unsigned cnt = 2, keep; u32x4 p0;
    asm volatile(
        "s_mov_b32 %[keep], m0\n\t"
        "s_nop 15\n\t"
        A_ALIGN
        A_ITER_DMA
        A_ITER_DMA
        "s_mov_b32 m0, %[keep]\n\t"
        "s_waitcnt vmcnt(0)\n\t"
        "s_barrier\n\t"
        A_ALIGN
        "ATT1_LOOP_%=:\n\t"
        A_ITER
        "s_sub_u32 %[n], %[n], 1\n\t"
        "s_cmp_lg_u32 %[n], 0\n\t"
        "s_cbranch_scc1 ATT1_LOOP_%=\n\t"
        : "+{v[24:39]}"(st.sa), "+{v[40:55]}"(st.sb), "+{v[72:87]}"(st.oa), "+{v[132:147]}"(st.ob), "+{v[92:95]}"(st.kf), "+{v[96:99]}"(st.v0), "+{v[100:103]}"(st.v1),
          "={v[104:107]}"(p0), "+{v[108:111]}"(st.p1), "+{v112}"(st.kaddr), "+{v113}"(st.vaddr), [n] "+s"(cnt), [keep] "=&s"(keep), "+{s[90:91]}"(src), "+{s92}"(dst)
        : "{v[56:71]}"(st.negma), "{v[116:131]}"(st.negmb), "{v[88:91]}"(st.qa), "{v[148:151]}"(st.qb), "{v114}"(st.vstride), "{v115}"(voff)
        : "memory", "scc");
}
typedef unsigned u32x16 __attribute__((ext_vector_type(16)));
struct Prefetch { u32x16 a0, a1, b0, b1, w0, w1; };
template <int TB>
__device__ __forceinline__ void attn_second(AttnState& st, Prefetch& pf, u32x4 rs, unsigned voffw, unsigned voffl, unsigned soa, unsigned sob, unsigned sow) {
    u32x4 p0;
    if constexpr (TB == 0) {
    asm volatile(
        A_ALIGN
        "buffer_load_dwordx4 v[152:155], v115, %[rs], s93 offen offset:0 sc1\n\t" A_STEP(1024, 512, 1024)
        "buffer_load_dwordx4 v[156:159], v115, %[rs], s93 offen offset:1024 sc1\n\t" A_STEP(2048, 1536, 2048)
        "buffer_load_dwordx4 v[160:163], v115, %[rs], s93 offen offset:2048 sc1\n\t" A_STEP(3072, 2560, 3072)
        "buffer_load_dwordx4 v[164:167], v115, %[rs], s93 offen offset:3072 sc1\n\t" "s_add_u32 s93, s93, 4096\n\t" A_STEP(4096, 3584, 4096)
        "v_add_u32 v112, 4096, v112\n\t" "v_add_u32 v113, v114, v113\n\t"
        "buffer_load_dwordx4 v[168:171], v115, %[rs], s93 offen offset:0 sc1\n\t" A_STEP(1024, 512, 1024)
        "buffer_load_dwordx4 v[172:175], v115, %[rs], s93 offen offset:1024 sc1\n\t" A_STEP(2048, 1536, 2048)
        "buffer_load_dwordx4 v[176:179], v115, %[rs], s93 offen offset:2048 sc1\n\t" A_STEP(3072, 2560, 3072)
        "buffer_load_dwordx4 v[180:183], v115, %[rs], s93 offen offset:3072 sc1\n\t" "s_add_u32 s93, s93, 4096\n\t" A_STEP(4096, 3584, 4096)
        "v_add_u32 v112, 4096, v112\n\t" "v_add_u32 v113, v114, v113\n\t"
        "buffer_load_dwordx4 v[184:187], v115, %[rs], s94 offen offset:0 sc1\n\t" A_STEP(1024, 512, 1024)
        "buffer_load_dwordx4 v[188:191], v115, %[rs], s94 offen offset:1024 sc1\n\t" A_STEP(2048, 1536, 2048)
        "buffer_load_dwordx4 v[192:195], v115, %[rs], s94 offen offset:2048 sc1\n\t" A_STEP(3072, 2560, 3072)
        "buffer_load_dwordx4 v[196:199], v115, %[rs], s94 offen offset:3072 sc1\n\t" "s_add_u32 s94, s94, 4096\n\t" A_STEP(4096, 3584, 4096)
        "v_add_u32 v112, 4096, v112\n\t" "v_add_u32 v113, v114, v113\n\t"
        "buffer_load_dwordx4 v[200:203], v115, %[rs], s94 offen offset:0 sc1\n\t" A_STEP(1024, 512, 1024)
        "buffer_load_dwordx4 v[204:207], v115, %[rs], s94 offen offset:1024 sc1\n\t" A_STEP(2048, 1536, 2048)
        "buffer_load_dwordx4 v[208:211], v115, %[rs], s94 offen offset:2048 sc1\n\t" A_STEP(3072, 2560, 3072)
        "buffer_load_dwordx4 v[212:215], v115, %[rs], s94 offen offset:3072 sc1\n\t" "s_add_u32 s94, s94, 4096\n\t" A_STEP(4096, 3584, 4096)
        "v_add_u32 v112, 4096, v112\n\t" "v_add_u32 v113, v114, v113\n\t"
        "s_waitcnt lgkmcnt(0)\n\t"
        A_MPV(A_OB, A_V1, A_P1)
        "s_nop 15\n\t"
        : "+{v[24:39]}"(st.sa), "+{v[40:55]}"(st.sb), "+{v[72:87]}"(st.oa), "+{v[132:147]}"(st.ob), "+{v[92:95]}"(st.kf), "+{v[96:99]}"(st.v0), "+{v[100:103]}"(st.v1),
          "={v[104:107]}"(p0), "+{v[108:111]}"(st.p1), "+{v112}"(st.kaddr), "+{v113}"(st.vaddr), "+{s93}"(soa), "+{s94}"(sob),
          "=&{v[152:167]}"(pf.a0), "=&{v[168:183]}"(pf.a1), "=&{v[184:199]}"(pf.b0), "=&{v[200:215]}"(pf.b1)
        : "{v[56:71]}"(st.negma), "{v[116:131]}"(st.negmb), "{v[88:91]}"(st.qa), "{v[148:151]}"(st.qb), "{v114}"(st.vstride), "{v115}"(voffw), [rs] "s"(rs)
        : "memory", "scc");
    } else {
    asm volatile(
        A_ALIGN
        "buffer_load_dwordx4 v[152:155], v115, %[rs], s93 offen offset:0 sc1\n\t" "buffer_load_dwordx4 v[216:219], %[vl], %[rs], s95 offen offset:0 sc1\n\t" A_STEP(1024, 512, 1024)
        "buffer_load_dwordx4 v[156:159], v115, %[rs], s93 offen offset:1024 sc1\n\t" "buffer_load_dwordx4 v[220:223], %[vl], %[rs], s95 offen offset:2048 sc1\n\t" "s_add_u32 s95, s95, 4096\n\t" A_STEP(2048, 1536, 2048)
        "buffer_load_dwordx4 v[160:163], v115, %[rs], s93 offen offset:2048 sc1\n\t" "buffer_load_dwordx4 v[224:227], %[vl], %[rs], s95 offen offset:0 sc1\n\t" A_STEP(3072, 2560, 3072)
        "buffer_load_dwordx4 v[164:167], v115, %[rs], s93 offen offset:3072 sc1\n\t" "s_add_u32 s93, s93, 4096\n\t" "buffer_load_dwordx4 v[228:231], %[vl], %[rs], s95 offen offset:2048 sc1\n\t" "s_add_u32 s95, s95, 4096\n\t" A_STEP(4096, 3584, 4096)
        "v_add_u32 v112, 4096, v112\n\t" "v_add_u32 v113, v114, v113\n\t"
        "s_setprio 0\n\t"
        "buffer_load_dwordx4 v[168:171], v115, %[rs], s93 offen offset:0 sc1\n\t" "buffer_load_dwordx4 v[232:235], %[vl], %[rs], s95 offen offset:0 sc1\n\t" A_STEP(1024, 512, 1024)
        "buffer_load_dwordx4 v[172:175], v115, %[rs], s93 offen offset:1024 sc1\n\t" "buffer_load_dwordx4 v[236:239], %[vl], %[rs], s95 offen offset:2048 sc1\n\t" "s_add_u32 s95, s95, 4096\n\t" A_STEP(2048, 1536, 2048)
        "buffer_load_dwordx4 v[176:179], v115, %[rs], s93 offen offset:2048 sc1\n\t" "buffer_load_dwordx4 v[240:243], %[vl], %[rs], s95 offen offset:0 sc1\n\t" A_STEP(3072, 2560, 3072)
        "buffer_load_dwordx4 v[180:183], v115, %[rs], s93 offen offset:3072 sc1\n\t" "s_add_u32 s93, s93, 4096\n\t" "buffer_load_dwordx4 v[244:247], %[vl], %[rs], s95 offen offset:2048 sc1\n\t" "s_add_u32 s95, s95, 4096\n\t" A_STEP(4096, 3584, 4096)
        "v_add_u32 v112, 4096, v112\n\t" "v_add_u32 v113, v114, v113\n\t"
        "buffer_load_dwordx4 v[184:187], v115, %[rs], s94 offen offset:0 sc1\n\t" A_STEP(1024, 512, 1024)
        "buffer_load_dwordx4 v[188:191], v115, %[rs], s94 offen offset:1024 sc1\n\t" A_STEP(2048, 1536, 2048)
        "buffer_load_dwordx4 v[192:195], v115, %[rs], s94 offen offset:2048 sc1\n\t" A_STEP(3072, 2560, 3072)
        "buffer_load_dwordx4 v[196:199], v115, %[rs], s94 offen offset:3072 sc1\n\t" "s_add_u32 s94, s94, 4096\n\t" A_STEP(4096, 3584, 4096)
        "v_add_u32 v112, 4096, v112\n\t" "v_add_u32 v113, v114, v113\n\t"
        "buffer_load_dwordx4 v[200:203], v115, %[rs], s94 offen offset:0 sc1\n\t" A_STEP(1024, 512, 1024)
        "buffer_load_dwordx4 v[204:207], v115, %[rs], s94 offen offset:1024 sc1\n\t" A_STEP(2048, 1536, 2048)
        "buffer_load_dwordx4 v[208:211], v115, %[rs], s94 offen offset:2048 sc1\n\t" A_STEP(3072, 2560, 3072)
        "buffer_load_dwordx4 v[212:215], v115, %[rs], s94 offen offset:3072 sc1\n\t" "s_add_u32 s94, s94, 4096\n\t" A_STEP(4096, 3584, 4096)
        "v_add_u32 v112, 4096, v112\n\t" "v_add_u32 v113, v114, v113\n\t"
        "s_waitcnt lgkmcnt(0)\n\t"
        A_MPV(A_OB, A_V1, A_P1)
        "s_nop 15\n\t"
        : "+{v[24:39]}"(st.sa), "+{v[40:55]}"(st.sb), "+{v[72:87]}"(st.oa), "+{v[132:147]}"(st.ob), "+{v[92:95]}"(st.kf), "+{v[96:99]}"(st.v0), "+{v[100:103]}"(st.v1),
          "={v[104:107]}"(p0), "+{v[108:111]}"(st.p1), "+{v112}"(st.kaddr), "+{v113}"(st.vaddr), "+{s93}"(soa), "+{s94}"(sob), "+{s95}"(sow),
          "=&{v[152:167]}"(pf.a0), "=&{v[168:183]}"(pf.a1), "=&{v[184:199]}"(pf.b0), "=&{v[200:215]}"(pf.b1), "=&{v[216:231]}"(pf.w0), "=&{v[232:247]}"(pf.w1)
        : "{v[56:71]}"(st.negma), "{v[116:131]}"(st.negmb), "{v[88:91]}"(st.qa), "{v[148:151]}"(st.qb), "{v114}"(st.vstride), "{v115}"(voffw), [rs] "s"(rs), [vl] "v"(voffl)
        : "memory", "scc");
    }
}
template <int I> __device__ __forceinline__ u32x4 quad(const u32x16& v) { return (u32x4){v[4 * I], v[4 * I + 1], v[4 * I + 2], v[4 * I + 3]}; }

struct Sums { float a[4], b[4];
    __device__ __forceinline__ void clear() { _Pragma("unroll") for (int d = 0; d < 4; ++d) { a[d] = 0.f; b[d] = 0.f; } }
    __device__ __forceinline__ void add(u32x4 f) { _Pragma("unroll") for (int d = 0; d < 4; ++d) { a[d % NCH] = dot2(f[d], 0x3f803f80u, a[d % NCH]); b[d % NCH] = dot2(f[d], f[d], b[d % NCH]); } }
    __device__ __forceinline__ float sum() const { return (a[0] + a[1]) + (a[2] + a[3]); }
    __device__ __forceinline__ float sumsq() const { return (b[0] + b[1]) + (b[2] + b[3]); }
};
template <int N>
__device__ __forceinline__ void stat_sums(LAS char* lds, int slot0, int tok, float& t1, float& t2) {
    f32x2 v[N];
#pragma unroll
    for (int i = 0; i < N; ++i) v[i] = *(LAS f32x2*)(lds + L_STAT + ((slot0 + i) * 32 + tok) * 8);
    SBAR();
    t1 = 0.f; t2 = 0.f;
#pragma unroll
    for (int i = 0; i < N; ++i) { t1 += v[i][0]; t2 += v[i][1]; }
}
__device__ __forceinline__ void ln_from_sums(float t1, float t2, float& mu, float& rstd) {
    mu = t1 * (1.f / D);
    rstd = __builtin_amdgcn_rsqf(fmaxf(t2 * (1.f / D) - mu * mu, 0.f) + LN_EPS);
}

template <int TB>
__device__ __forceinline__ void run_layers(unsigned char* ws0, LAS char* lds) {
    const unsigned lbase = (unsigned)(uintptr_t)lds;
    const int tid0 = threadIdx.x, w0 = __builtin_amdgcn_readfirstlane(tid0 >> 6);
    const int kk0 = blockIdx.x >> 3, c0 = (blockIdx.x & 7) * 4 + (kk0 >> 3), j0 = kk0 & 7;
    typedef const __attribute__((address_space(4))) Params* kargs_t;
#define PHASE_IDS() \
    int tid = tid0, w = w0, c = c0, j = j0; unsigned epoch = epoch0; unsigned char* ws = ws0; \
    kargs_t kp = (kargs_t)__builtin_amdgcn_kernarg_segment_ptr();        \
    asm volatile("" : "+v"(tid)); asm volatile("" : "+s"(w), "+s"(c), "+s"(j), "+s"(epoch), "+s"(ws), "+s"(kp)); \
    const rsrc_t rw = __builtin_amdgcn_make_buffer_rsrc(ws, 0, WS_TOTAL, 0x00020000); \
    gu32* cnt = (gu32*)(ws + OFF_CNT); gu32* sync = (gu32*)(ws + OFF_SYNC); gu32* tmo = sync + 2 * CW; \
    (void)rw; (void)cnt; (void)tmo; (void)kp; \
    constexpr int tb = TB; const int lane = tid & 63, r = lane & 31, h = lane >> 5, kq = w & 3, tbg = 2 * c + tb; \
    const int vl = lane * 16;                                     \
    const int vw = (lane ^ ((kq & 1) << 4)) * 16;                 \
    const int cbA = 2 * j + (kq >> 1), cbB = 2 * j + ((kq >> 1) ^ 1); \
    const int sown = 4 * j + kq;                                  \
    (void)epoch; (void)r; (void)h; (void)tbg; (void)vl; (void)vw; (void)cbA; (void)cbB; (void)sown; (void)tb;
#define W1_IDS() \
    const int ncb = j < 3 ? 6 : 5, cb0 = j < 3 ? 6 * j : 18 + 5 * (j - 3);     \
    const int cb1 = TB == 0 ? kq : 4 + (kq >> 1);     \
    const bool d_active = cb1 < ncb; \
    const int cbg1 = cb0 + (d_active ? cb1 : 0); (void)cbg1; \
    const int par = TB == 0 ? 0 : (kq & 1); (void)par;

#define REQ_WKV() do { \
        const uint16_t* src_ = ((lane & 2) ? kp->wv : kp->wk) + (128 * kq + (lane >> 2)) * 16 + (lane & 1) * 8; \
        _Pragma("unroll") for (int i = 0; i < 8; ++i) glds16(src_ + i * 256, lbase + ((kq & 2) ? L_WKV1 : L_WKV0) + ((kq & 1) * 8 + i) * 1024); } while (0)

    unsigned epoch_v, epoch0 = 0;
    { gu32* e = (gu32*)(ws0 + OFF_SYNC); asm volatile("" : "+s"(e)); epoch_v = __hip_atomic_load(e, RLX_AGENT); }
    {
        PHASE_IDS();
        if (tid < 16) *(LAS unsigned*)(lds + (tid < 8 ? F_PAIR + tid * 4 : F_OCNT)) = 0u;
        if (j == tb && kq == 0 && lane < 4) __hip_atomic_store(cnt + cnt_word(j, lane, c), 0u, RLX_AGENT);
        if (j == 0 && tid == 4) __hip_atomic_store((gu32*)(ws + OFF_PREPC) + c * CW, 0u, RLX_AGENT);
        if (c == 0 && j == 0 && tid == 5) __hip_atomic_store(tmo, 0u, RLX_AGENT);
    }

#pragma unroll 1
    for (int L = 0; L < 2; ++L) {
        u32x4 qown; float q2own;
        {
            PHASE_IDS();
            const bool kvm = j < 2;
            const bool kvteam = j == tb;
            if (kvm && kvteam) {
                __builtin_amdgcn_s_setprio(2);
                if (L == 0) REQ_WKV();
            } else {
                const int row = lane >> 3, pc = lane & 7;
                const uint16_t* src = kp->wq + (size_t)(64 * w + row) * D + 64 * j + (((pc >> 2) ^ ((row >> 1) & 1)) * 32 + (pc & 3) * 8);
                if (L == 0) {
#pragma unroll
                    for (int i = 0; i < 4; ++i) glds16(src + i * 8 * D, lbase + L_WQ + (w * 8 + i) * 1024);
                }
#pragma unroll
                for (int i = 4; i < 8; ++i) glds16(src + i * 8 * D, lbase + L_WQ + (w * 8 + i) * 1024);
                if (kvm) {
                    const int w2 = w ^ 4;
                    const uint16_t* src2 = kp->wq + (size_t)(64 * w2 + row) * D + 64 * j + (((pc >> 2) ^ ((row >> 1) & 1)) * 32 + (pc & 3) * 8);
                    if (L == 0) {
#pragma unroll
                        for (int i = 0; i < 4; ++i) glds16(src2 + i * 8 * D, lbase + L_WQ + (w2 * 8 + i) * 1024);
                    }
#pragma unroll
                    for (int i = 4; i < 8; ++i) glds16(src2 + i * 8 * D, lbase + L_WQ + (w2 * 8 + i) * 1024);
                }
            }
            auto prep_dst = [&](int i) -> int {
                const int u = c * 6 + (j - 2);
                if (u < 43) return OFF_W1 + (u * KSD + 4 * w + i) * 1024;
                const int f = ((u - 43) * 8 + w) + i * 1192;
                if (i >= 2 || f >= 1920) return -1;
                return f < 512 ? OFF_WO + f * 1024 : OFF_W2 + (f - 512) * 1024;
            };
            u32x4 xa[8] = {};
            if (L == 0) {
                const rsrc_t rx = __builtin_amdgcn_make_buffer_rsrc((void*)kp->x, 0, S * D * 2, 0x00020000);
#pragma unroll
                for (int i = 0; i < 8; ++i) xa[i] = ld_plain(rx, r * 1024 + h * 16, (64 * c + 32 * tb) * 1024 + (kq * 8 + i) * 32);
                SBAR();
                epoch0 = __builtin_amdgcn_readfirstlane(epoch_v); asm volatile("" : "+s"(epoch0)); epoch = epoch0;
                if (!kvm || kvteam) WAITVM(8);
            } else {
                if (tid == (kvm ? 256 * j : 0)) poll_ge(cnt + cnt_word(0, CNT_X2, c), 8u, tmo);
                __builtin_amdgcn_fence(__ATOMIC_ACQUIRE, "wavefront");
                if (!kvm || kvteam) WAITVM(0);
            }
            if (!(L == 0 && kvm)) LDS_BARRIER();
            if (L == 1) {
#pragma unroll
                for (int i = 0; i < 8; ++i) xa[i] = ld_sc1(rw, vl, OFF_X2 + (tbg * KSD + kq * 8 + i) * 1024);
                SBAR();
            }
            LAS f32x2* st = (LAS f32x2*)(lds + L_STAT);
            LAS float* cst = (LAS float*)(lds + L_CS);
            const int tq = (lane & 15) >> 2, tp = lane & 3, tg = (lane >> 4) & 1;
            f32x16 aA = {}, aB = {};
            auto store_stats = [&]() {
                Sums sm; sm.clear();
#pragma unroll
                for (int i = 0; i < 8; ++i) sm.add(xa[i]);
                const float s1 = half_sum(sm.sum()), s2 = half_sum(sm.sumsq());
                if (h == 0) st[(tb * 8 + kq) * 32 + r] = (f32x2){s1, s2};
            };
            auto do_q = [&]() {
                u32x4 fa[8], fb[8];
                ColSum cA, cB; cA.clear(); cB.clear();
                const int sw = (tq >> 1) & 1, col = (tg ^ (kq & 1)) * 32 + tp * 8;
                const int a0 = L_WQ + (kq * 128 + 8 * h + tq) * 128 + col;
                const int aa = a0 + (((kq >> 1) ^ sw) * 64), ab = a0 + (((kq >> 1) ^ 1 ^ sw) * 64);
#pragma unroll
                for (int i = 0; i < 8; ++i) { fa[i] = tr_frag(lds, aa + i * 2048, 512); fb[i] = tr_frag(lds, ab + i * 2048, 512); }
                SBAR();
#pragma unroll
                for (int i = 0; i < 8; ++i) { cA.add(fa[i]); cB.add(fb[i]); }
                const float csA = half_sum(cA.sum()), csB = half_sum(cB.sum());
                if (TB == 0 && h == 0) { const int n = r ^ ((kq & 1) << 4); cst[kq * 96 + (kq >> 1) * 32 + n] = csA; cst[kq * 96 + ((kq >> 1) ^ 1) * 32 + n] = csB; }
                SBAR();
#pragma unroll
                for (int i = 0; i < 8; ++i) { aA = mfma(fa[i], xa[i], aA); aB = mfma(fb[i], xa[i], aB); }
                PIN2(aA, aB);
            };
            if (kvm) {
                if (kvteam) {
                    u32x4 wk[8];
                    const int a = ((kq & 2) ? L_WKV1 : L_WKV0) + ((kq & 1) * 128 + 8 * h + tq) * 64 + tg * 32 + tp * 8;
#pragma unroll
                    for (int i = 0; i < 8; ++i) wk[i] = tr_frag(lds, a + i * 1024, 256);
                    SBAR();
                    ColSum ck; ck.clear();
#pragma unroll
                    for (int i = 0; i < 8; ++i) ck.add(wk[i]);
                    const float cs = half_sum(ck.sum());
                    if (h == 0) cst[kq * 96 + 64 + r] = cs;
                    SBAR();
                    f32x16 ak = {}, av = {};
#pragma unroll
                    for (int i = 0; i < 8; ++i) { ak = mfma(wk[i], xa[i], ak); av = mfma(xa[i], wk[i], av); }
                    store_stats();
                    if (kq == 3 && lane == 0) *(LAS unsigned*)(lds + F_KVCNT) = 0u;
                    const int o = L_KVRED + kq * 4096;
                    lds_stf(lds, o + vl, (f32x4){ak[0], ak[1], ak[2], ak[3]}); lds_stf(lds, o + 1024 + vl, (f32x4){ak[4], ak[5], ak[6], ak[7]});
                    if (r >= 16) {
#pragma unroll
                        for (int g = 0; g < 4; ++g) lds_stf(lds, o + 2048 + g * 512 + (h * 16 + r - 16) * 16, (f32x4){av[4 * g], av[4 * g + 1], av[4 * g + 2], av[4 * g + 3]});
                    }
                } else WAITVM(8);
                LDS_BARRIER();
                if (kvteam && kq < 3) {
                    f32x4 a = {}, e = {}, ta[4], te[4];
                    float t1, t2, mu, rstd;
                    if (kq == 0) {
                        f32x4 k0 = {}, k1 = {}, u0[4], u1[4];
#pragma unroll
                        for (int i = 0; i < 4; ++i) { ta[i] = lds_ldf(lds, L_KVRED + i * 4096 + vl); te[i] = lds_ldf(lds, L_KVRED + i * 4096 + 1024 + vl);
                                                      u0[i] = lds_ldf(lds, L_CS + (i * 96 + 64 + 4 * h) * 4); u1[i] = lds_ldf(lds, L_CS + (i * 96 + 64 + 8 + 4 * h) * 4); }
                        stat_sums<4>(lds, tb * 8, r, t1, t2); ln_from_sums(t1, t2, mu, rstd);
#pragma unroll
                        for (int i = 0; i < 4; ++i) { a += ta[i]; e += te[i]; k0 += u0[i]; k1 += u1[i]; }
#pragma unroll
                        for (int i = 0; i < 4; ++i) { a[i] = (a[i] - mu * k0[i]) * rstd; e[i] = (e[i] - mu * k1[i]) * rstd; }
                        st_sc1((u32x4){pk(a[0], a[1]), pk(a[2], a[3]), pk(e[0], e[1]), pk(e[2], e[3])}, rw, vl, OFF_KF + L * 65536 + tbg * 1024);
                        float n2 = (a[0] * a[0] + a[1] * a[1]) + (a[2] * a[2] + a[3] * a[3]) + (e[0] * e[0] + e[1] * e[1]) + (e[2] * e[2] + e[3] * e[3]);
                        n2 = wave_max(half_sum(n2));
                        if (lane == 0) *(LAS float*)(lds + F_N2) = n2;
                    } else {
                        const int g = kq - 1, lc = (h * 16 + (r & 15)) * 16;
                        float csv = 0.f, uc[4];
#pragma unroll
                        for (int i = 0; i < 4; ++i) { ta[i] = lds_ldf(lds, L_KVRED + i * 4096 + 2048 + (2 * g) * 512 + lc); te[i] = lds_ldf(lds, L_KVRED + i * 4096 + 2048 + (2 * g + 1) * 512 + lc);
                                                      uc[i] = cst[i * 96 + 64 + 16 + (r & 15)]; }
                        stat_sums<4>(lds, tb * 8, r, t1, t2); ln_from_sums(t1, t2, mu, rstd);
#pragma unroll
                        for (int i = 0; i < 4; ++i) { a += ta[i]; e += te[i]; csv += uc[i]; }
#pragma unroll
                        for (int i = 0; i < 8; ++i) {
                            const int tok = 16 * g + (i & 3) + 8 * (i >> 2) + 4 * h;
                            const float m2 = __uint_as_float(__builtin_amdgcn_ds_bpermute(tok * 4, __float_as_uint(mu))), r2 = __uint_as_float(__builtin_amdgcn_ds_bpermute(tok * 4, __float_as_uint(rstd)));
                            if (i < 4) a[i] = (a[i] - m2 * csv) * r2; else e[i - 4] = (e[i - 4] - m2 * csv) * r2;
                        }
                        if (r >= 16) st_sc1((u32x4){pk(a[0], a[1]), pk(a[2], a[3]), pk(e[0], e[1]), pk(e[2], e[3])}, rw, h * 256 + (r - 16) * 16, OFF_VF + L * 65536 + (2 * tbg + g) * 512);
                    }
                }
                if (kvteam && kq < 3) {
                    DRAIN();
                    unsigned old = 0;
                    if (lane == 0) old = __hip_atomic_fetch_add((LAS unsigned*)(lds + F_KVCNT), 1u, __ATOMIC_RELAXED, __HIP_MEMORY_SCOPE_WORKGROUP);
                    if (__builtin_amdgcn_readfirstlane(old) == 2u && lane < 32)
                        __hip_atomic_store((gu64*)(ws + OFF_KVFL) + (L * 32 + lane) * 64 + 2 * c + j,
                                           ((unsigned long long)(epoch + 1u + L) << 32) | __float_as_uint(*(volatile LAS float*)(lds + F_N2)), RLX_AGENT);
                }
                __builtin_amdgcn_s_setprio(0);
                do_q();
                if (!kvteam) store_stats();
            } else {
                do_q(); store_stats();
            }
            if (L == 0 && !kvm) {
#pragma unroll
                for (int i = 0; i < 4; ++i) {
                    const int d = prep_dst(i);
                    if (d < 0) continue;
                    const uint16_t* src; int ld, cb, s;
                    if (d >= OFF_W2) { const int g = (d - OFF_W2) >> 10; src = kp->w2; ld = D; cb = g / KSFP; s = min(g % KSFP, KSF - 1); }
                    else if (d >= OFF_W1) { const int g = (d - OFF_W1) >> 10; src = kp->w1; ld = F; cb = g >> 5; s = g & 31; }
                    else { const int g = (d - OFF_WO) >> 10; src = kp->wo; ld = D; cb = g >> 5; s = g & 31; }
                    glds16(src + (size_t)(16 * s + (lane >> 2)) * ld + 32 * cb + (lane & 3) * 8, lbase + L_PREP + (w * 4 + i) * 1024);
                }
            }
            float q[8];
            reduce4(lds, kvm ? (kvteam ? 0 : 131072) : (TB == 0 ? 32768 : 131072), aA, aB, kq, lane, q);
            float t1, t2;
            f32x4 cs0 = {}, cs1 = {}, u0[4], u1[4];
#pragma unroll
            for (int i = 0; i < 4; ++i) { u0[i] = lds_ldf(lds, L_CS + (i * 96 + 16 * kq + 4 * h) * 4); u1[i] = lds_ldf(lds, L_CS + (i * 96 + 16 * kq + 8 + 4 * h) * 4); }
            stat_sums<4>(lds, tb * 8, r, t1, t2);
#pragma unroll
            for (int i = 0; i < 4; ++i) { cs0 += u0[i]; cs1 += u1[i]; }
            float mu, rstd; ln_from_sums(t1, t2, mu, rstd);
            float n2 = 0.f;
#pragma unroll
            for (int i = 0; i < 4; ++i) { q[i] = (q[i] - mu * cs0[i]) * (rstd * QSCALE); q[4 + i] = (q[4 + i] - mu * cs1[i]) * (rstd * QSCALE); }
#pragma unroll
            for (int i = 0; i < 8; ++i) n2 += q[i] * q[i];
            q2own = half_sum(n2);
            qown = (u32x4){pk(q[0], q[1]), pk(q[2], q[3]), pk(q[4], q[5]), pk(q[6], q[7])};
            lds_st(lds, L_XCHQ + w * 1024 + vl, qown);
            *(LAS float*)(lds + L_STAT + (tb * 8 + 4 + kq) * 256 + lane * 4) = q2own;
            if (L == 0 && !kvm) {
                WAITVM(0);
                ColSum cp; cp.clear();
#pragma unroll
                for (int i = 0; i < 4; ++i) {
                    const int d = prep_dst(i);
                    if (d < 0) continue;
                    u32x4 fr = tr_frag(lds, L_PREP + (w * 4 + i) * 1024 + (8 * h + tq) * 64 + tg * 32 + tp * 8, 256);
                    if (d >= OFF_W2 && ((d - OFF_W2) >> 10) % KSFP >= KSF) fr = (u32x4){0u, 0u, 0u, 0u};
                    st_sc1(fr, rw, vl, d);
                    cp.add(fr);
                }
                const int u = c * 6 + (j - 2);
                if (u < 43) {
                    const float cs = half_sum(cp.sum());
                    LAS float* t = (LAS float*)(lds + L_WQ);
                    if (h == 0) t[w * 32 + r] = cs;
                    LDS_BARRIER();
                    if (tid < 32) { float v = 0.f, pv[8];
#pragma unroll
                        for (int i = 0; i < 8; ++i) pv[i] = t[i * 32 + tid];
                        SBAR();
#pragma unroll
                        for (int i = 0; i < 8; ++i) v += pv[i];
                        __hip_atomic_store((gu32*)(ws + OFF_CS1 + (32 * u + tid) * 4), __float_as_uint(v), RLX_AGENT); }
                }
                DRAIN();
            }
        }
        u32x4 woA[8], woB[8];
        u32x4 w1f[32];
        {
            PHASE_IDS();
            const unsigned char* kvsrc = ws + (TB == 0 ? OFF_KF : OFF_VF) + L * 65536 + ((w >> 1) & 1) * 32768 + (w & 1) * 6144;
            unsigned kvdst = lbase + (TB == 0 ? L_K : L_V) + ((w >> 1) & 1) * 32768 + (w & 1) * 6144, kvoff = vl;
            asm volatile("" : "+s"(kvsrc), "+s"(kvdst)); asm volatile("" : "+v"(kvoff));
            const float km2 = wait_flags(tid, lds, (gu64*)(ws + OFF_KVFL) + (L * 32 + c) * 64, epoch + 1u + L, tmo);
            dma_run_sc1<6>(kvsrc, kvoff, kvdst);
            if (L == 0 && j >= 2 && tid == 0) __hip_atomic_fetch_add((gu32*)(ws + OFF_PREPC) + c * CW, 1u, RLX_AGENT);
            { unsigned one2 = ((tid & 31) == 0) ? 0x3f803f80u : 0u; asm volatile("" : "+v"(one2)); if (tid < 272) lds_st(lds, L_ONES + tid * 16, (u32x4){one2, one2, one2, one2}); }
            const u32x4 qpar = lds_ld(lds, L_XCHQ + (w ^ 1) * 1024 + vl);
            const float q2par = *(LAS float*)(lds + L_STAT + (tb * 8 + 4 + (kq ^ 1)) * 256 + lane * 4);
            float ma = __builtin_amdgcn_sqrtf(q2own * km2) * 1.02f, mb = __builtin_amdgcn_sqrtf(q2par * km2) * 1.02f;
            const bool unsafe = __any(!(ma <= SAFE_BOUND) || !(mb <= SAFE_BOUND));
            {
                LAS unsigned* fl = (LAS unsigned*)(lds + F_UNSAFE);
                if (lane == 0) fl[w] = unsafe ? 1u : 0u;
            }
            WAITVM(0);
            LDS_BARRIER();
            {
                LAS unsigned* fl = (LAS unsigned*)(lds + F_UNSAFE);
                const u32x4 f0 = *(LAS u32x4*)fl, f1 = *(LAS u32x4*)(fl + 4);
                if (__builtin_amdgcn_readfirstlane(f0[0] | f0[1] | f0[2] | f0[3] | f1[0] | f1[1] | f1[2] | f1[3])) {
                    {
                        const unsigned char* src = ws + (TB == 0 ? OFF_KF : OFF_VF) + L * 65536 + ((w >> 1) & 1) * 32768 + (12 + (w & 1) * 10) * 1024 + vl;
                        const unsigned dst = lbase + (TB == 0 ? L_K : L_V) + ((w >> 1) & 1) * 32768 + (12 + (w & 1) * 10) * 1024;
#pragma unroll 1
                        for (int i = 0; i < 10; ++i) glds16_sc1(src + i * 1024, __builtin_amdgcn_readfirstlane(dst + i * 1024));
                    }
                    DRAIN();
                    LDS_BARRIER();
                    if (unsafe) {
                        float xa = -INFINITY, xb = -INFINITY;
#pragma unroll 1
                        for (int t = 0; t < 64; ++t) {
                            const u32x4 kf = lds_ld(lds, L_K + t * 1024 + vl);
                            const f32x16 sa = mfma(kf, qown, (f32x16){}), sb = mfma(kf, qpar, (f32x16){});
#pragma unroll
                            for (int i = 0; i < 16; ++i) { xa = fmaxf(xa, sa[i]); xb = fmaxf(xb, sb[i]); }
                        }
                        ma = half_max(xa); mb = half_max(xb);
                    }
                }
            }
            const int kh = kq & 1;
            const bool real = r < 16;
            const unsigned kaddr = L_K + kh * 32768 + vl;
            const unsigned vaddr = real ? (unsigned)(L_V + kh * 32768 + h * 256 + r * 16) : (unsigned)(L_ONES + ((r >= 20 && r < 28) ? 64 : r != 16 ? 16 : 0));
            if (TB == 1) __builtin_amdgcn_s_setprio(1);
            AttnState st;
            attn_begin(st, lds, qown, qpar, ma, mb, kaddr, vaddr, real ? 4096u : 0u);
            attn_first(st, (unsigned long long)(uintptr_t)(ws + (TB == 0 ? OFF_KF : OFF_VF) + L * 65536 + ((w >> 1) & 1) * 32768 + (12 + (w & 1) * 10) * 1024),
                       lbase + (TB == 0 ? L_K : L_V) + ((w >> 1) & 1) * 32768 + (12 + (w & 1) * 10) * 1024, (unsigned)vl);
            if (L == 0) {
                unsigned spins = 0;
                while (!__all(__hip_atomic_load((gu32*)(ws + OFF_PREPC) + (lane & 31) * CW, RLX_AGENT) >= 6u)) {
                    if (++spins > SPIN_CAP) { if (lane == 0) __hip_atomic_store(tmo, 1u, RLX_AGENT); break; }
                }
                __builtin_amdgcn_fence(__ATOMIC_ACQUIRE, "wavefront");
            }
            Prefetch pf;
            { W1_IDS();
              const unsigned long long wsa = (unsigned long long)(uintptr_t)ws;
              const u32x4 rs = {(unsigned)wsa, (unsigned)(wsa >> 32) & 0xffffu, (unsigned)WS_TOTAL, 0x00020000u};
              attn_second<TB>(st, pf, rs, (unsigned)vw, (unsigned)vl, OFF_WO + (cbA * KSD + kq * 8) * 1024, OFF_WO + (cbB * KSD + kq * 8) * 1024, OFF_W1 + (cbg1 * KSD + par) * 1024); }
            if (TB == 1) __builtin_amdgcn_s_setprio(0);
            const f32x16 oa = st.oa, ob = st.ob;
            const int xo = L_XCHO + w * 2304;
            lds_stf(lds, xo + vl, (f32x4){ob[0], ob[1], ob[2], ob[3]}); lds_stf(lds, xo + 1024 + vl, (f32x4){ob[4], ob[5], ob[6], ob[7]});
            *(LAS float*)(lds + xo + 2048 + lane * 4) = half_sum(ob[8]);
            asm volatile("s_waitcnt lgkmcnt(0)" ::: "memory");
            *(volatile LAS unsigned*)(lds + F_PAIR + w * 4) = L + 1u;
            while (*(volatile LAS unsigned*)(lds + F_PAIR + (w ^ 1) * 4) != L + 1u) { }
            asm volatile("" ::: "memory");
            const int xi = L_XCHO + (w ^ 1) * 2304;
            const f32x4 e0 = lds_ldf(lds, xi + vl), e1 = lds_ldf(lds, xi + 1024 + vl);
            const float lpar = *(LAS float*)(lds + xi + 2048 + lane * 4);
            SBAR();
            const float rl = __builtin_amdgcn_rcpf(half_sum(oa[8]) + lpar);
            const u32x4 of = {pk((oa[0] + e0[0]) * rl, (oa[1] + e0[1]) * rl), pk((oa[2] + e0[2]) * rl, (oa[3] + e0[3]) * rl),
                              pk((oa[4] + e1[0]) * rl, (oa[5] + e1[1]) * rl), pk((oa[6] + e1[2]) * rl, (oa[7] + e1[3]) * rl)};
            st_sc1(swap_order(of), rw, vl, OFF_O + L * ACT_BYTES + (tbg * KSD + sown) * 1024);
            if (TB == 0) asm volatile("s_waitcnt vmcnt(0)" : "+{v[152:167]}"(pf.a0), "+{v[168:183]}"(pf.a1), "+{v[184:199]}"(pf.b0), "+{v[200:215]}"(pf.b1) :: "memory");
            else asm volatile("s_waitcnt vmcnt(0)" : "+{v[152:167]}"(pf.a0), "+{v[168:183]}"(pf.a1), "+{v[184:199]}"(pf.b0), "+{v[200:215]}"(pf.b1), "+{v[216:231]}"(pf.w0), "+{v[232:247]}"(pf.w1) :: "memory");
            unsigned old = 0;
            if (lane == 0) old = __hip_atomic_fetch_add((LAS unsigned*)(lds + F_OCNT), 1u, __ATOMIC_RELAXED, __HIP_MEMORY_SCOPE_WORKGROUP);
            if (__builtin_amdgcn_readfirstlane(old) == 8u * L + 7u && lane == 0) __hip_atomic_fetch_add(cnt + cnt_word(L, CNT_O, c), 1u, RLX_AGENT);
            woA[0] = quad<0>(pf.a0); woA[1] = quad<1>(pf.a0); woA[2] = quad<2>(pf.a0); woA[3] = quad<3>(pf.a0);
            woA[4] = quad<0>(pf.a1); woA[5] = quad<1>(pf.a1); woA[6] = quad<2>(pf.a1); woA[7] = quad<3>(pf.a1);
            woB[0] = quad<0>(pf.b0); woB[1] = quad<1>(pf.b0); woB[2] = quad<2>(pf.b0); woB[3] = quad<3>(pf.b0);
            woB[4] = quad<0>(pf.b1); woB[5] = quad<1>(pf.b1); woB[6] = quad<2>(pf.b1); woB[7] = quad<3>(pf.b1);
            if (TB == 1) {
                w1f[0] = quad<0>(pf.w0); w1f[1] = quad<1>(pf.w0); w1f[2] = quad<2>(pf.w0); w1f[3] = quad<3>(pf.w0);
                w1f[4] = quad<0>(pf.w1); w1f[5] = quad<1>(pf.w1); w1f[6] = quad<2>(pf.w1); w1f[7] = quad<3>(pf.w1);
            }
            if (TB == 0) { W1_IDS();
#pragma unroll
                for (int i = 0; i < W1_EARLY; ++i) { w1f[i] = ld_sc1(rw, vl, OFF_W1 + (cbg1 * KSD + i) * 1024); SBAR(); __builtin_amdgcn_s_sleep(W1_PAUSE); SBAR(); }
            }
        }
        u32x4 resid;
        {
            PHASE_IDS();
            if (L == 0) { const rsrc_t rx = __builtin_amdgcn_make_buffer_rsrc((void*)kp->x, 0, S * D * 2, 0x00020000);
                          resid = ld_plain(rx, r * 1024 + h * 16, (64 * c + 32 * tb) * 1024 + sown * 32); }
            else resid = ld_sc1(rw, vl, OFF_X2 + (tbg * KSD + sown) * 1024);
            wait_cnt(tid, cnt + cnt_word(L, CNT_O, c), 8u, tmo);
            u32x4 xa[8];
#pragma unroll
            for (int i = 0; i < 8; ++i) xa[i] = ld_sc1(rw, vl, OFF_O + L * ACT_BYTES + (tbg * KSD + kq * 8 + i) * 1024);
            SBAR();
            f32x16 aA = {}, aB = {};
#pragma unroll
            for (int i = 0; i < 8; ++i) { aA = mfma(woA[i], xa[i], aA); aB = mfma(woB[i], xa[i], aB); }
            PIN2(aA, aB);
            SBAR();
            {
                auto run_src = [&](int run) { return ws + OFF_W2 + ((2 * j + (run & 1)) * KSFP + (run >> 1) * 22 + W2_REG) * 1024; };
                if (TB == 0) { dma_run_sc1<6>(run_src(kq), vl, lbase + L_W2A + kq * W2_A * 1024); dma_run_sc1<3>(run_src(kq + 4), vl, lbase + L_W2A + (kq + 4) * W2_A * 1024); }
                else dma_run_sc1<3>(run_src(w) + 3072, vl, lbase + L_W2A + (w * W2_A + 3) * 1024);
            }
            float o[8];
            reduce4(lds, tb * 24576, aA, aB, kq, lane, o);
            resid = swap_order(resid);
#pragma unroll
            for (int i = 0; i < 4; ++i) resid[i] = pk(bflo(resid[i]) + o[2 * i], bfhi(resid[i]) + o[2 * i + 1]);
            st_sc1(swap_order(resid), rw, vl, OFF_X1 + L * ACT_BYTES + (tbg * KSD + sown) * 1024);
            publish(tid, cnt + cnt_word(L, CNT_X1, c));
        }
        u32x4 w2A[W2_REG], w2B[W2_REG];
        {
            PHASE_IDS(); W1_IDS();
#pragma unroll
            for (int n = 0; n < 8; ++n) {
                if (TB == 0) w1f[W1_EARLY + n] = ld_sc1(rw, vl, OFF_W1 + (cbg1 * KSD + W1_EARLY + n) * 1024);
                else w1f[16 + n] = ld_sc1(rw, vl, OFF_W1 + (cbg1 * KSD + 16 + 2 * n + par) * 1024);
            }
            unsigned tab_cs = 0; unsigned short tab_b = 0;
            if (w >= 1 && w <= 3) { const int col = min(32 * cb0 + tid - 64, F - 1);
                tab_cs = __builtin_amdgcn_raw_buffer_load_b32(rw, col * 4, OFF_CS1, AUX_SC1); tab_b = kp->b1[col]; }
            SBAR();
            const unsigned char* xsrc = ws + OFF_X1 + L * ACT_BYTES + tbg * KSD * 1024;
            unsigned xdst = lbase + L_ACT + tb * KSD * 1024, xoff = vl;
            asm volatile("" : "+s"(xsrc), "+s"(xdst)); asm volatile("" : "+v"(xoff));
            wait_cnt(tid, cnt + cnt_word(L, CNT_X1, c), 8u, tmo);
            if (w >= 1 && w <= 3) { *(LAS float*)(lds + L_TAB + (tid - 64) * 4) = __uint_as_float(tab_cs); *(LAS float*)(lds + L_TAB + 768 + (tid - 64) * 4) = bf2f(tab_b); }
            if (TB == 0) asm volatile("" : "+v"(w1f[24]), "+v"(w1f[25]), "+v"(w1f[26]), "+v"(w1f[27]), "+v"(w1f[28]), "+v"(w1f[29]), "+v"(w1f[30]), "+v"(w1f[31]));
            else asm volatile("" : "+v"(w1f[16]), "+v"(w1f[17]), "+v"(w1f[18]), "+v"(w1f[19]), "+v"(w1f[20]), "+v"(w1f[21]), "+v"(w1f[22]), "+v"(w1f[23]));
            dma_run_sc1<2>(xsrc + 2 * kq * 1024, xoff, xdst + 2 * kq * 1024);
            asm volatile("s_barrier" ::: "memory");
            dma_run_sc1<6>(xsrc + (8 + 6 * kq) * 1024, xoff, xdst + (8 + 6 * kq) * 1024);
            WAITVM(6);
            LDS_BARRIER();
            f32x16 a0 = {}, a1 = {};
            Sums sm; sm.clear();
            const unsigned char* w2a_src = ws + OFF_W2 + ((2 * j + (w & 1)) * KSFP + (w >> 1) * 22 + W2_REG) * 1024;
            auto req = [&](int n) {
                if (n < 5) dma4_sc1<1>(w2a_src + (6 + n) * 1024, vl, lbase + L_W2A + (w * W2_A + 6 + n) * 1024);
                else if (n < 5 + W2_REG) w2A[n - 5] = ld_sc1(rw, vw, OFF_W2 + (cbA * KSFP + kq * 22 + n - 5) * 1024);
                else if (n < 5 + 2 * W2_REG) w2B[n - 5 - W2_REG] = ld_sc1(rw, vw, OFF_W2 + (cbB * KSFP + kq * 22 + n - 5 - W2_REG) * 1024);
            };
            u32x4 sv[6];
            auto stat_read = [&](int k0, int n) {
#pragma unroll
                for (int i = 0; i < 6; ++i) if (i < n) sv[i] = lds_ld(lds, L_ACT + (tb * KSD + k0 + i) * 1024 + vl);
            };
            auto stat_step = [&](int first, int n, int s) { if (s >= first && s < first + n) sm.add(sv[s - first]); };
            const int act = L_ACT + vl;
            {
                constexpr int N = TB == 0 ? 8 : 4, ST = TB == 0 ? 1 : 2;
                const int ab = act + par * 1024;
                constexpr int PD = 4;
                u32x4 b0[PD + 1], b1[PD + 1];
                stat_read(2 * kq, 2);
#pragma unroll
                for (int i = 0; i < PD; ++i) { b0[i] = lds_ld(lds, ab + ST * i * 1024); b1[i] = lds_ld(lds, ab + (KSD + ST * i) * 1024); }
#pragma unroll
                for (int s = 0; s < N; ++s) {
                    if (s + PD < N) { b0[(s + PD) % (PD + 1)] = lds_ld(lds, ab + ST * (s + PD) * 1024); b1[(s + PD) % (PD + 1)] = lds_ld(lds, ab + (KSD + ST * (s + PD)) * 1024); }
                    SBAR();
                    a0 = mfma(w1f[s], b0[s % (PD + 1)], a0); a1 = mfma(w1f[s], b1[s % (PD + 1)], a1);
                    PIN2(a0, a1);
                    stat_step(N - 2, 2, s);
                    SBAR();
                }
            }
            WAITVM(0);
            LDS_BARRIER();
            if (TB == 0) {
                constexpr int PD = 7;
                u32x4 b0[PD + 1];
                stat_read(8 + 6 * kq, 6);
#pragma unroll
                for (int i = 0; i < PD; ++i) b0[i] = lds_ld(lds, act + (8 + i) * 1024);
#pragma unroll
                for (int s = 0; s < 24; ++s) {
                    if (s + PD < 24) b0[(s + PD) % (PD + 1)] = lds_ld(lds, act + (8 + s + PD) * 1024);
                    SBAR();
                    a0 = mfma(w1f[8 + s], b0[s % (PD + 1)], a0);
                    asm volatile("" : "+v"(a0));
                    if ((s & 1) == 0) req(s / 2);
                    stat_step(10, 6, s);
                    SBAR();
                }
            } else {
                const int ab = act + (8 + par) * 1024;
                constexpr int PD = 4;
                u32x4 b0[PD + 1], b1[PD + 1];
                stat_read(8 + 6 * kq, 6);
#pragma unroll
                for (int i = 0; i < PD; ++i) { b0[i] = lds_ld(lds, ab + 2 * i * 1024); b1[i] = lds_ld(lds, ab + (KSD + 2 * i) * 1024); }
#pragma unroll
                for (int s = 0; s < 12; ++s) {
                    if (s + PD < 12) { b0[(s + PD) % (PD + 1)] = lds_ld(lds, ab + 2 * (s + PD) * 1024); b1[(s + PD) % (PD + 1)] = lds_ld(lds, ab + (KSD + 2 * (s + PD)) * 1024); }
                    SBAR();
                    const int f = 4 + s < 8 ? 4 + s : 12 + s;
                    a0 = mfma(w1f[f], b0[s % (PD + 1)], a0); a1 = mfma(w1f[f], b1[s % (PD + 1)], a1);
                    PIN2(a0, a1);
                    stat_step(5, 6, s);
                    SBAR();
                }
            }
            { const float u1 = half_sum(sm.sum()), u2 = half_sum(sm.sumsq()); if (h == 0) ((LAS f32x2*)(lds + L_STAT))[(tb * 8 + kq) * 32 + r] = (f32x2){u1, u2}; }
            const int xslot = L_ACT + (kq >> 1) * KSD * 1024 + (kq & 1) * 4096 + vl, xslot_p = L_ACT + (kq >> 1) * KSD * 1024 + ((kq & 1) ^ 1) * 4096 + vl;
            if (TB == 1) {
#pragma unroll
                for (int g = 0; g < 4; ++g) {
                    const f32x4 v = par ? (f32x4){a0[4 * g], a0[4 * g + 1], a0[4 * g + 2], a0[4 * g + 3]} : (f32x4){a1[4 * g], a1[4 * g + 1], a1[4 * g + 2], a1[4 * g + 3]};
                    lds_stf(lds, xslot + g * 1024, v);
                }
            }
            LDS_BARRIER();
            constexpr float G1 = -2.f * 0.7978845608028654f * 1.4426950408889634f, G3 = G1 * 0.044715f;
            f32x4 tcs[4], tbb[4];
#pragma unroll
            for (int g = 0; g < 4; ++g) { tcs[g] = lds_ldf(lds, L_TAB + (32 * cb1 + 8 * g + 4 * h) * 4); tbb[g] = lds_ldf(lds, L_TAB + 768 + (32 * cb1 + 8 * g + 4 * h) * 4); }
            f32x2 gx[8], ge[8];
            float g_rstd = 0.f, g_nmr = 0.f;
            auto gelu_slice = [&](const f32x16& acc, int t, int n) {
                if (n == 0) { float t1, t2; stat_sums<4>(lds, t * 8, r, t1, t2); float mu; ln_from_sums(t1, t2, mu, g_rstd); g_nmr = -mu * g_rstd; }
                else if (n <= 3) {
#pragma unroll
                    for (int i = (n - 1) * 3; i < (n == 3 ? 8 : n * 3); ++i) {
                        const f32x2 a2 = {acc[2 * i], acc[2 * i + 1]}, c2 = {tcs[i >> 1][2 * (i & 1)], tcs[i >> 1][2 * (i & 1) + 1]}, b2 = {tbb[i >> 1][2 * (i & 1)], tbb[i >> 1][2 * (i & 1) + 1]};
                        gx[i] = __builtin_elementwise_fma(a2, (f32x2){g_rstd, g_rstd}, __builtin_elementwise_fma(c2, (f32x2){g_nmr, g_nmr}, b2));
                        asm volatile("" : "+v"(gx[i]));
                    }
                } else if (n <= 7) {
#pragma unroll
                    for (int i = (n - 4) * 2; i < (n - 4) * 2 + 2; ++i) { ge[i] = gx[i] * __builtin_elementwise_fma(gx[i] * gx[i], (f32x2){G3, G3}, (f32x2){G1, G1}); asm volatile("" : "+v"(ge[i])); }
                } else if (n <= 13) {
#pragma unroll
                    for (int i = (n - 8) * 3; i < min((n - 8) * 3 + 3, 16); ++i) ge[i >> 1][i & 1] = __builtin_amdgcn_exp2f(ge[i >> 1][i & 1]);
                    asm volatile("" : "+v"(ge[min((n - 8) * 3, 15) >> 1]), "+v"(ge[min((n - 8) * 3 + 2, 15) >> 1]));
                } else if (n == 14) {
#pragma unroll
                    for (int i = 0; i < 8; ++i) ge[i] = ge[i] + (f32x2){1.f, 1.f};
                    PIN8(ge);
                } else if (n <= 20) {
#pragma unroll
                    for (int i = (n - 15) * 3; i < min((n - 15) * 3 + 3, 16); ++i) ge[i >> 1][i & 1] = __builtin_amdgcn_rcpf(ge[i >> 1][i & 1]);
                    asm volatile("" : "+v"(ge[min((n - 15) * 3, 15) >> 1]), "+v"(ge[min((n - 15) * 3 + 2, 15) >> 1]));
                } else if (n == 21) {
#pragma unroll
                    for (int i = 0; i < 8; ++i) gx[i] = gx[i] * ge[i];
                    PIN8(gx);
                } else if (n == 22 && d_active) {
                    const int so = OFF_H + L * H_BYTES + ((2 * c + t) * KSF + 2 * cbg1) * 1024;
                    st_sc1(swap_order((u32x4){pk(gx[0][0], gx[0][1]), pk(gx[1][0], gx[1][1]), pk(gx[2][0], gx[2][1]), pk(gx[3][0], gx[3][1])}), rw, vl, so);
                    st_sc1(swap_order((u32x4){pk(gx[4][0], gx[4][1]), pk(gx[5][0], gx[5][1]), pk(gx[6][0], gx[6][1]), pk(gx[7][0], gx[7][1])}), rw, vl, so + 1024);
                }
            };
            if (TB == 0) {
                constexpr int PD = 5;
                u32x4 b1[PD + 1];
#pragma unroll
                for (int i = 0; i < PD; ++i) b1[i] = lds_ld(lds, act + (KSD + 8 + i) * 1024);
#pragma unroll
                for (int s = 0; s < 24; ++s) {
                    if (s + PD < 24) b1[(s + PD) % (PD + 1)] = lds_ld(lds, act + (KSD + 8 + s + PD) * 1024);
                    SBAR();
                    a1 = mfma(w1f[8 + s], b1[s % (PD + 1)], a1);
                    asm volatile("" : "+v"(a1));
                    gelu_slice(a0, 0, s);
                    SBAR();
                }
#pragma unroll
                for (int n = 0; n < 23; ++n) gelu_slice(a1, 1, n);
            } else {
                f32x4 v[4];
#pragma unroll
                for (int g = 0; g < 4; ++g) v[g] = lds_ldf(lds, xslot_p + g * 1024);
                SBAR();
#pragma unroll
                for (int g = 0; g < 4; ++g)
#pragma unroll
                    for (int i = 0; i < 4; ++i) a0[4 * g + i] = (par ? a1[4 * g + i] : a0[4 * g + i]) + v[g][i];
#pragma unroll
                for (int n = 0; n < 23; ++n) { req(n); gelu_slice(a0, par, n); SBAR(); }
            }
            publish(tid, cnt + cnt_word(L, CNT_H, c));
        }
        {
            PHASE_IDS();
            dma_run_sc1<W2_B>(ws + OFF_W2 + ((2 * j + (w & 1)) * KSFP + (w >> 1) * 22 + W2_REG + W2_A) * 1024, vl, lbase + L_W2B + W2_B * w * 1024);
            const u32x2 bb0 = *(const GAS u32x2*)(kp->b2 + 16 * sown + 4 * h), bb1 = *(const GAS u32x2*)(kp->b2 + 16 * sown + 8 + 4 * h);
            auto ld_h = [&](int i) { return ld_sc1(rw, vl, OFF_H + L * H_BYTES + tbg * KSF * 1024 + min(kq * 22 + i, KSF - 1) * 1024); };
            if (tid == 0) poll_ge(cnt + cnt_word(L, CNT_H, c), 8u, tmo);
            __builtin_amdgcn_fence(__ATOMIC_ACQUIRE, "wavefront");
            LDS_BARRIER();
            u32x4 ha[22];
#pragma unroll
            for (int i = 0; i < EWIN; ++i) ha[i] = ld_h(i);
            SBAR();
            f32x16 aA = {}, aB = {};
            const int aA0 = L_W2A + ((kq * 2 + (kq >> 1)) * W2_A) * 1024 + vw, aB0 = L_W2A + ((kq * 2 + ((kq >> 1) ^ 1)) * W2_A) * 1024 + vw;
            const int bA0 = L_W2B + ((kq * 2 + (kq >> 1)) * W2_B) * 1024 + vw, bB0 = L_W2B + ((kq * 2 + ((kq >> 1) ^ 1)) * W2_B) * 1024 + vw;
            constexpr int PD = 4;
            u32x4 la[PD + 1], lb[PD + 1];
            auto w_lds = [&](int n) {
                if (n >= W2_REG && n < W2_REG + W2_A) { la[n % (PD + 1)] = lds_ld(lds, aA0 + (n - W2_REG) * 1024); lb[n % (PD + 1)] = lds_ld(lds, aB0 + (n - W2_REG) * 1024); }
                else if (n >= W2_REG + W2_A && n < 22) { la[n % (PD + 1)] = lds_ld(lds, bA0 + (n - W2_REG - W2_A) * 1024); lb[n % (PD + 1)] = lds_ld(lds, bB0 + (n - W2_REG - W2_A) * 1024); }
            };
#pragma unroll
            for (int n = 0; n < PD; ++n) w_lds(n);
#pragma unroll
            for (int i = 0; i < 22; ++i) {
                if (i + EWIN < 22) ha[i + EWIN] = ld_h(i + EWIN);
                if (i + PD == W2_REG + W2_A) asm volatile("s_barrier" ::: "memory");
                w_lds(i + PD);
                SBAR();
                if (i < W2_REG) { aA = mfma(w2A[i], ha[i], aA); aB = mfma(w2B[i], ha[i], aB); }
                else { aA = mfma(la[i % (PD + 1)], ha[i], aA); aB = mfma(lb[i % (PD + 1)], ha[i], aB); }
                PIN2(aA, aB);
                SBAR();
            }
            LDS_BARRIER();
            if (L == 0) {
                const int row = lane >> 3, pc = lane & 7;
                const uint16_t* src = kp->wq + (size_t)(64 * w + row) * D + 64 * j + (((pc >> 2) ^ ((row >> 1) & 1)) * 32 + (pc & 3) * 8);
                const bool mine = !(j == tb);
                const uint16_t* src2 = kp->wq + (size_t)(64 * (w ^ 4) + row) * D + 64 * j + (((pc >> 2) ^ ((row >> 1) & 1)) * 32 + (pc & 3) * 8);
                if (!mine) REQ_WKV();
                if (mine) {
#pragma unroll
                    for (int i = 0; i < 4; ++i) glds16(src + i * 8 * D, lbase + L_WQ + (w * 8 + i) * 1024);
                    if (j < 2) {
#pragma unroll
                        for (int i = 0; i < 4; ++i) glds16(src2 + i * 8 * D, lbase + L_WQ + ((w ^ 4) * 8 + i) * 1024);
                    }
                }
            }
            float o[8];
            reduce4(lds, tb * 24576, aA, aB, kq, lane, o);
#pragma unroll
            for (int i = 0; i < 2; ++i) { o[2 * i] += bflo(bb0[i]); o[2 * i + 1] += bfhi(bb0[i]); o[4 + 2 * i] += bflo(bb1[i]); o[4 + 2 * i + 1] += bfhi(bb1[i]); }
#pragma unroll
            for (int i = 0; i < 4; ++i) resid[i] = pk(bflo(resid[i]) + o[2 * i], bfhi(resid[i]) + o[2 * i + 1]);
            const u32x4 xo = swap_order(resid);
            if (L == 0) {
                st_sc1(xo, rw, vl, OFF_X2 + (tbg * KSD + sown) * 1024);
                publish(tid, cnt + cnt_word(0, CNT_X2, c));
            } else {
                *(GAS u32x4*)(kp->out + (size_t)(64 * c + 32 * tb + r) * D + 16 * sown + 8 * h) = xo;
            }
        }
    }
    { unsigned e = epoch0; asm volatile("" : "+s"(e)); if (blockIdx.x == 0 && threadIdx.x == 0) __hip_atomic_store((gu32*)(ws0 + OFF_SYNC), e + 2u, RLX_AGENT); }
}


__global__ void __launch_bounds__(512, 2) main_kernel(Params p) {
    extern __shared__ __attribute__((aligned(16))) char lds_dyn[];
    LAS char* lds = (LAS char*)lds_dyn;
    asm volatile("" :: "s"(p.x), "s"(p.wq), "s"(p.wk), "s"(p.wv), "s"(p.w1), "s"(p.b2), "s"(p.out));
    if (__builtin_amdgcn_readfirstlane(threadIdx.x >> 8) == 0) run_layers<0>(p.ws, lds); else run_layers<1>(p.ws, lds);
}

}

extern "C" void kernel_launch(void* const* d_in, const int* in_sizes, int n_in, void* d_out, int out_size, void* d_ws, size_t ws_size, hipStream_t stream) {
    Params p;
    p.x = (const uint16_t*)d_in[0]; p.wq = (const uint16_t*)d_in[1]; p.wk = (const uint16_t*)d_in[2]; p.wv = (const uint16_t*)d_in[3]; p.wo = (const uint16_t*)d_in[4];
    p.w1 = (const uint16_t*)d_in[6]; p.b1 = (const uint16_t*)d_in[7]; p.w2 = (const uint16_t*)d_in[8]; p.b2 = (const uint16_t*)d_in[9];
    p.out = (uint16_t*)d_out; p.ws = (unsigned char*)d_ws;
    (void)hipFuncSetAttribute((const void*)main_kernel, hipFuncAttributeMaxDynamicSharedMemorySize, LDS_BYTES);
    hipLaunchKernelGGL(main_kernel, dim3(256), dim3(512), LDS_BYTES, stream, p);
}
```

```cpp
#include <hip/hip_runtime.h>
#include <hip/hip_bf16.h>
#include <stdint.h>

#define GAS __attribute__((address_space(1)))
#define LAS __attribute__((address_space(3)))
typedef __bf16 bf16x8 __attribute__((ext_vector_type(8)));
typedef __bf16 bf16x2 __attribute__((ext_vector_type(2)));
typedef float f32x16 __attribute__((ext_vector_type(16)));
typedef float f32x4 __attribute__((ext_vector_type(4)));
typedef float f32x2 __attribute__((ext_vector_type(2)));
typedef unsigned u32x4 __attribute__((ext_vector_type(4)));
typedef GAS unsigned gu32;
#define RLX_AGENT __ATOMIC_RELAXED, __HIP_MEMORY_SCOPE_AGENT

namespace {

constexpr int S = 2048, D = 512, F = 1376;
constexpr int KSD = 32;
constexpr int KSF = 86;
constexpr int KSFP = 88;
constexpr float LN_EPS = 1e-5f;
constexpr float QSCALE = 0.25f * 1.4426950408889634f;
constexpr float SAFE_BOUND = 50.f;

constexpr int CW = 64;
constexpr int OFF_CNT = 0;
constexpr int CNT_O = 0, CNT_X1 = 1, CNT_H = 2, CNT_X2 = 3;
__device__ __forceinline__ int cnt_word(int layer, int kind, int c) { return ((layer * 4 + kind) * 32 + c) * CW; }
constexpr int OFF_PREPC = 65536;
constexpr int OFF_SYNC = OFF_PREPC + 32 * 256;
constexpr int OFF_KVFL = OFF_SYNC + 1024;
constexpr int OFF_WO = OFF_KVFL + 2 * 32 * 512;
constexpr int OFF_W1 = OFF_WO + 16 * KSD * 1024;
constexpr int OFF_W2 = OFF_W1 + 43 * KSD * 1024;
constexpr int OFF_CS1 = OFF_W2 + 16 * KSFP * 1024;
constexpr int OFF_KF = (OFF_CS1 + 1408 * 4 + 1023) / 1024 * 1024;
constexpr int OFF_VF = OFF_KF + 2 * 65536;
constexpr int OFF_O = OFF_VF + 2 * 65536;
constexpr int ACT_BYTES = 64 * KSD * 1024;
constexpr int OFF_X1 = OFF_O + 2 * ACT_BYTES;
constexpr int OFF_X2 = OFF_X1 + 2 * ACT_BYTES;
constexpr int H_BYTES = 64 * KSF * 1024;
constexpr int OFF_H = OFF_X2 + ACT_BYTES;
constexpr int WS_TOTAL = OFF_H + 2 * H_BYTES;
constexpr unsigned SPIN_CAP = 1u << 15;

constexpr int L_K = 0, L_V = 65536, L_ONES = 131072, L_XCHO = 135424;
constexpr int L_WQ = 65536, L_WKV0 = 49152, L_WKV1 = 131072, L_PREP = 0, L_KVRED = 32768, L_XCHQ = 57344;
constexpr int L_ACT = 0, L_W2A = 65536, L_W2B = 0;
constexpr int L_STAT = 155648, L_TAB = 159744, L_CS = L_TAB, L_FLAG = 163584;
constexpr int F_UNSAFE = L_FLAG, F_KM2 = L_FLAG + 32, F_N2 = L_FLAG + 36, F_KVCNT = L_FLAG + 40, F_OCNT = L_FLAG + 44, F_PAIR = L_FLAG + 64;
constexpr int LDS_BYTES = 163840;
constexpr int W2_REG = 3, W2_A = 11, W2_B = 8;
constexpr int W1_EARLY = 24;

constexpr int EWIN = 5;
constexpr int W1_PAUSE = 3;
constexpr int NCH = 4;

struct Params {
    const uint16_t *x, *wq, *wk, *wv, *wo, *w1, *b1, *w2, *b2;
    uint16_t* out;
    unsigned char* ws;
};

__device__ __forceinline__ f32x16 mfma(u32x4 a, u32x4 b, f32x16 c) {
    return __builtin_amdgcn_mfma_f32_32x32x16_bf16(__builtin_bit_cast(bf16x8, a), __builtin_bit_cast(bf16x8, b), c, 0, 0, 0);
}
__device__ __forceinline__ unsigned pk(float a, float b) { f32x2 v = {a, b}; return __builtin_bit_cast(unsigned, __builtin_convertvector(v, bf16x2)); }
__device__ __forceinline__ float bflo(unsigned u) { return __uint_as_float(u << 16); }
__device__ __forceinline__ float bfhi(unsigned u) { return __uint_as_float(u & 0xffff0000u); }
__device__ __forceinline__ float bf2f(uint16_t v) { return __uint_as_float((unsigned)v << 16); }
__device__ __forceinline__ float dot2(unsigned a, unsigned b, float c) {
    return __builtin_amdgcn_fdot2_f32_bf16(__builtin_bit_cast(bf16x2, a), __builtin_bit_cast(bf16x2, b), c, false);
}
__device__ __forceinline__ float half_sum(float v) {
    auto r = __builtin_amdgcn_permlane32_swap(__float_as_uint(v), __float_as_uint(v), false, false);
    return __uint_as_float(r[0]) + __uint_as_float(r[1]);
}
__device__ __forceinline__ float half_max(float v) {
    auto r = __builtin_amdgcn_permlane32_swap(__float_as_uint(v), __float_as_uint(v), false, false);
    return fmaxf(__uint_as_float(r[0]), __uint_as_float(r[1]));
}
__device__ __forceinline__ u32x4 swap_order(u32x4 a) {
    auto r0 = __builtin_amdgcn_permlane32_swap(a[0], a[2], false, false);
    auto r1 = __builtin_amdgcn_permlane32_swap(a[1], a[3], false, false);
    return (u32x4){r0[0], r1[0], r0[1], r1[1]};
}
template <int CTRL> __device__ __forceinline__ float dpp(float v) {
    return __uint_as_float(__builtin_amdgcn_update_dpp(0u, __float_as_uint(v), CTRL, 0xf, 0xf, true));
}
__device__ __forceinline__ float wave_max(float v) {
    v = fmaxf(v, dpp<0xB1>(v));
    v = fmaxf(v, dpp<0x4E>(v));
    v = fmaxf(v, dpp<0x141>(v));
    v = fmaxf(v, dpp<0x140>(v));
    auto r = __builtin_amdgcn_permlane16_swap(__float_as_uint(v), __float_as_uint(v), false, false);
    return half_max(fmaxf(__uint_as_float(r[0]), __uint_as_float(r[1])));
}
#define SBAR() __builtin_amdgcn_sched_barrier(0)
#define PIN8(x) asm volatile("" : "+v"(x[0]), "+v"(x[1]), "+v"(x[2]), "+v"(x[3]), "+v"(x[4]), "+v"(x[5]), "+v"(x[6]), "+v"(x[7]))
#define PIN2(a, b) asm volatile("" : "+v"(a), "+v"(b))
#define LDS_BARRIER() asm volatile("s_waitcnt lgkmcnt(0)\n\ts_barrier" ::: "memory")
#define DRAIN() asm volatile("s_waitcnt vmcnt(0)" ::: "memory")
constexpr int AUX_SC1 = 16;
typedef __amdgpu_buffer_rsrc_t rsrc_t;
__device__ __forceinline__ u32x4 ld_sc1(rsrc_t r, int voff, int soff) { return __builtin_amdgcn_raw_buffer_load_b128(r, voff, soff, AUX_SC1); }
__device__ __forceinline__ u32x4 ld_plain(rsrc_t r, int voff, int soff) { return __builtin_amdgcn_raw_buffer_load_b128(r, voff, soff, 0); }
__device__ __forceinline__ void st_sc1(u32x4 v, rsrc_t r, int voff, int soff) {
    __builtin_amdgcn_raw_buffer_store_b128(v, r, voff, soff, AUX_SC1);
    asm volatile("s_nop 1" :: "v"(v) : "memory");
}
__device__ __forceinline__ u32x4 lds_ld(LAS char* l, int off) { return *(LAS u32x4*)(l + off); }
__device__ __forceinline__ void lds_st(LAS char* l, int off, u32x4 v) { *(LAS u32x4*)(l + off) = v; }
__device__ __forceinline__ f32x4 lds_ldf(LAS char* l, int off) { return *(LAS f32x4*)(l + off); }
__device__ __forceinline__ void lds_stf(LAS char* l, int off, f32x4 v) { *(LAS f32x4*)(l + off) = v; }

__device__ __forceinline__ void publish(int tid, gu32* cnt) {
    asm volatile("" : "+s"(cnt));
    DRAIN();
    LDS_BARRIER();
    if (tid == 0) __hip_atomic_fetch_add(cnt, 1u, RLX_AGENT);
}
__device__ __forceinline__ void poll_ge(gu32* cnt, unsigned want, gu32* tmo) {
    unsigned spins = 0;
    while (__hip_atomic_load(cnt, RLX_AGENT) < want) {
        if (++spins > SPIN_CAP) { __hip_atomic_store(tmo, 1u, RLX_AGENT); break; }
    }
}
__device__ __forceinline__ void wait_cnt(int tid, gu32* cnt, unsigned want, gu32* tmo) {
    if (tid == 0) poll_ge(cnt, want, tmo);
    __builtin_amdgcn_fence(__ATOMIC_ACQUIRE, "wavefront");
    LDS_BARRIER();
}
typedef GAS unsigned long long gu64;
__device__ __forceinline__ float wait_flags(int tid, LAS char* lds, gu64* fl, unsigned tag, gu32* tmo) {
    if (tid < 64) {
        unsigned spins = 0; unsigned long long v;
        while (v = __hip_atomic_load(fl + tid, RLX_AGENT), !__all((unsigned)(v >> 32) == tag)) {
            if (++spins > SPIN_CAP) { if (tid == 0) __hip_atomic_store(tmo, 1u, RLX_AGENT); break; }
        }
        const float m = wave_max(__uint_as_float((unsigned)v));
        if (tid == 0) *(LAS float*)(lds + F_KM2) = m;
    }
    __builtin_amdgcn_fence(__ATOMIC_ACQUIRE, "wavefront");
    LDS_BARRIER();
    return *(LAS float*)(lds + F_KM2);
}
__device__ __forceinline__ void glds16(const void* g, unsigned lds_dst) {
    unsigned keep;
    asm volatile("s_mov_b32 %0, m0\n\ts_mov_b32 m0, %2\n\ts_nop 0\n\tglobal_load_lds_dwordx4 %1, off\n\ts_mov_b32 m0, %0" : "=&s"(keep) : "v"(g), "s"(lds_dst) : "memory");
}
__device__ __forceinline__ void glds16_sc1(const void* g, unsigned lds_dst) {
    unsigned keep;
    asm volatile("s_mov_b32 %0, m0\n\ts_mov_b32 m0, %2\n\ts_nop 0\n\tglobal_load_lds_dwordx4 %1, off sc1\n\ts_mov_b32 m0, %0" : "=&s"(keep) : "v"(g), "s"(lds_dst) : "memory");
}
template <int N>
__device__ __forceinline__ void dma4_sc1(const unsigned char* g, unsigned voff, unsigned lds_dst) {
    if constexpr (N == 1) asm volatile("s_mov_b32 m0, %2\n\ts_nop 0\n\tglobal_load_lds_dwordx4 %0, %1 sc1" :: "v"(voff), "s"(g), "s"(lds_dst) : "memory");
    if constexpr (N == 2) asm volatile("s_mov_b32 m0, %2\n\ts_nop 0\n\tglobal_load_lds_dwordx4 %0, %1 sc1\n\tglobal_load_lds_dwordx4 %0, %1 offset:1024 sc1" :: "v"(voff), "s"(g), "s"(lds_dst) : "memory");
    if constexpr (N == 3) asm volatile("s_mov_b32 m0, %2\n\ts_nop 0\n\tglobal_load_lds_dwordx4 %0, %1 sc1\n\tglobal_load_lds_dwordx4 %0, %1 offset:1024 sc1\n\t"
                                       "global_load_lds_dwordx4 %0, %1 offset:2048 sc1" :: "v"(voff), "s"(g), "s"(lds_dst) : "memory");
    if constexpr (N == 4) asm volatile("s_mov_b32 m0, %2\n\ts_nop 0\n\tglobal_load_lds_dwordx4 %0, %1 sc1\n\tglobal_load_lds_dwordx4 %0, %1 offset:1024 sc1\n\t"
                                       "global_load_lds_dwordx4 %0, %1 offset:2048 sc1\n\tglobal_load_lds_dwordx4 %0, %1 offset:3072 sc1" :: "v"(voff), "s"(g), "s"(lds_dst) : "memory");
}
__device__ __forceinline__ void dma1(const void* g, unsigned voff, unsigned lds_dst) {
    asm volatile("s_mov_b32 m0, %2\n\ts_nop 0\n\tglobal_load_lds_dwordx4 %0, %1" :: "v"(voff), "s"(g), "s"(lds_dst) : "memory");
}
template <int N>
__device__ __forceinline__ void dma_run_sc1(const unsigned char* g, unsigned voff, unsigned lds_dst) {
    if constexpr (N <= 4) dma4_sc1<N>(g, voff, lds_dst);
    else { dma4_sc1<4>(g, voff, lds_dst); dma_run_sc1<N - 4>(g + 4096, voff, lds_dst + 4096); }
}
#define WAITVM(n) asm volatile("s_waitcnt vmcnt(" #n ")" ::: "memory")

__device__ __forceinline__ void reduce4(LAS char* lds, int region, const f32x16& aA, const f32x16& aB, int kq, int lane, float (&o)[8]) {
    const int base = region + lane * 16;
    { const int a = base + ((kq ^ 1) * 3 + 0) * 2048;
      lds_stf(lds, a, (f32x4){aA[8], aA[9], aA[10], aA[11]}); lds_stf(lds, a + 1024, (f32x4){aA[12], aA[13], aA[14], aA[15]}); }
    { const int a = base + ((kq ^ 2) * 3 + 1) * 2048;
      lds_stf(lds, a, (f32x4){aB[0], aB[1], aB[2], aB[3]}); lds_stf(lds, a + 1024, (f32x4){aB[4], aB[5], aB[6], aB[7]}); }
    { const int a = base + ((kq ^ 3) * 3 + 2) * 2048;
      lds_stf(lds, a, (f32x4){aB[8], aB[9], aB[10], aB[11]}); lds_stf(lds, a + 1024, (f32x4){aB[12], aB[13], aB[14], aB[15]}); }
    LDS_BARRIER();
    f32x4 lo = {aA[0], aA[1], aA[2], aA[3]}, hi = {aA[4], aA[5], aA[6], aA[7]}, t[6];
#pragma unroll
    for (int x = 0; x < 6; ++x) t[x] = lds_ldf(lds, base + kq * 3 * 2048 + x * 1024);
    SBAR();
#pragma unroll
    for (int x = 0; x < 3; ++x) { lo += t[2 * x]; hi += t[2 * x + 1]; }
#pragma unroll
    for (int i = 0; i < 4; ++i) { o[i] = lo[i]; o[4 + i] = hi[i]; }
}
typedef short v4i16 __attribute__((ext_vector_type(4)));
typedef unsigned u32x2 __attribute__((ext_vector_type(2)));
__device__ __forceinline__ u32x4 tr_frag(LAS char* lds, int a, int step) {
    const u32x2 lo = __builtin_bit_cast(u32x2, __builtin_amdgcn_ds_read_tr16_b64_v4i16((LAS v4i16*)(lds + a)));
    const u32x2 hi = __builtin_bit_cast(u32x2, __builtin_amdgcn_ds_read_tr16_b64_v4i16((LAS v4i16*)(lds + a + step)));
    return (u32x4){lo[0], lo[1], hi[0], hi[1]};
}
struct ColSum { float a[4];
    __device__ __forceinline__ void clear() { _Pragma("unroll") for (int d = 0; d < 4; ++d) a[d] = 0.f; }
    __device__ __forceinline__ void add(u32x4 f) { _Pragma("unroll") for (int d = 0; d < 4; ++d) a[d % NCH] = dot2(f[d], 0x3f803f80u, a[d % NCH]); }
    __device__ __forceinline__ float sum() const { return (a[0] + a[1]) + (a[2] + a[3]); }
};

#define A_ALIGN ".p2align 5\n\t"
#define A_E1(a) "v_exp_f32 v" #a ", v" #a "\n\t"
#define A_E4(a, b, c, d) A_E1(a) A_E1(b) A_E1(c) A_E1(d)
#define A_CVT(d, a, b) "v_cvt_pk_bf16_f32 v" #d ", v" #a ", v" #b "\n\t"
#define A_DSK(off) "ds_read_b128 v[92:95], v112 offset:" #off "\n\t"
#define A_DSV0(off) "ds_read_b128 v[96:99], v113 offset:" #off "\n\t"
#define A_DSV1(off) "ds_read_b128 v[100:103], v113 offset:" #off "\n\t"
#define A_WAIT(n) "s_waitcnt lgkmcnt(" #n ")\n\t"
#define A_MSB "v_mfma_f32_32x32x16_bf16 v[40:55], v[92:95], v[148:151], v[116:131]\n\t"
#define A_MSA "v_mfma_f32_32x32x16_bf16 v[24:39], v[92:95], v[88:91], v[56:71]\n\t"
#define A_MPV(O, V, P) "v_mfma_f32_32x32x16_bf16 " O ", " V ", " P ", " O "\n\t"
#define A_OA "v[72:87]"
#define A_OB "v[132:147]"
#define A_V0 "v[96:99]"
#define A_V1 "v[100:103]"
#define A_P0 "v[104:107]"
#define A_P1 "v[108:111]"
#define A_STEP(koff, v1off, v0off) \
    A_MSB A_DSK(koff) A_E4(24, 25, 26, 27) \
    A_MPV(A_OB, A_V1, A_P1) A_DSV1(v1off) A_E4(28, 29, 30, 31) \
    A_CVT(104, 24, 25) A_CVT(105, 26, 27) A_CVT(106, 28, 29) A_CVT(107, 30, 31) \
    A_E4(32, 33, 34, 35) \
    A_WAIT(2) A_MPV(A_OA, A_V0, A_P0) A_E4(36, 37, 38, 39) \
    A_CVT(108, 32, 33) A_CVT(109, 34, 35) A_CVT(110, 36, 37) A_CVT(111, 38, 39) \
    A_WAIT(1) A_MSA A_E4(40, 41, 42, 43) \
    A_WAIT(0) A_MPV(A_OA, A_V1, A_P1) A_E4(44, 45, 46, 47) \
    A_CVT(104, 40, 41) A_CVT(105, 42, 43) A_CVT(106, 44, 45) A_CVT(107, 46, 47) \
    A_E4(48, 49, 50, 51) \
    A_MPV(A_OB, A_V0, A_P0) A_DSV0(v0off) A_E4(52, 53, 54, 55) \
    A_CVT(108, 48, 49) A_CVT(109, 50, 51) A_CVT(110, 52, 53) A_CVT(111, 54, 55)

struct AttnState { f32x16 sa, sb, oa, ob, negma, negmb; u32x4 qa, qb, kf, v0, v1, p1; unsigned kaddr, vaddr, vstride; };
#define A_ITER \
        A_STEP(1024, 512, 1024) \
        A_STEP(2048, 1536, 2048) \
        A_STEP(3072, 2560, 3072) \
        A_STEP(4096, 3584, 4096) \
        "v_add_u32 v112, 4096, v112\n\t" \
        "v_add_u32 v113, v114, v113\n\t"
__device__ __forceinline__ void attn_begin(AttnState& st, LAS char* lds, u32x4 qa, u32x4 qb, u32x4 kf0, u32x4 vf0, float ma, float mb, unsigned kaddr, unsigned vaddr, unsigned vstride) {
#pragma unroll
    for (int i = 0; i < 16; ++i) { st.negma[i] = -ma; st.negmb[i] = -mb; }
    st.qa = qa; st.qb = qb;
    st.kf = kf0; st.v0 = vf0; st.v1 = st.v0; st.p1 = (u32x4){0u, 0u, 0u, 0u};
    st.sa = mfma(st.kf, qa, st.negma); st.sb = st.sa;
    st.oa = (f32x16){}; st.ob = (f32x16){};
    const unsigned lbase = (unsigned)(uintptr_t)lds;
    st.kaddr = kaddr + lbase; st.vaddr = vaddr + lbase; st.vstride = vstride;
}
#define A_DMA \
    "s_mov_b32 m0, s92\n\t" \
    "s_add_u32 s92, s92, 1024\n\t" \
    "global_load_lds_dwordx4 v115, s[90:91] sc1\n\t" \
    "s_add_u32 s90, s90, 1024\n\t" \
    "s_addc_u32 s91, s91, 0\n\t"
#define A_ITER_DMA \
        A_DMA A_DMA A_STEP(1024, 512, 1024) \
        A_DMA A_STEP(2048, 1536, 2048) \
        A_DMA A_STEP(3072, 2560, 3072) \
        A_DMA A_STEP(4096, 3584, 4096) \
        "v_add_u32 v112, 4096, v112\n\t" \
        "v_add_u32 v113, v114, v113\n\t"
__device__ __forceinline__ void attn_first(AttnState& st, unsigned long long src, unsigned dst, unsigned voff) {
    unsigned cnt = 2, keep; u32x4 p0;
    asm volatile(
        "s_mov_b32 %[keep], m0\n\t"
        "s_nop 15\n\t"
        A_ALIGN
        A_ITER_DMA
        A_ITER_DMA
        "s_mov_b32 m0, %[keep]\n\t"
        "s_waitcnt vmcnt(0)\n\t"
        "s_barrier\n\t"
        A_ALIGN
        "ATT1_LOOP_%=:\n\t"
        A_ITER
        "s_sub_u32 %[n], %[n], 1\n\t"
        "s_cmp_lg_u32 %[n], 0\n\t"
        "s_cbranch_scc1 ATT1_LOOP_%=\n\t"
        : "+{v[24:39]}"(st.sa), "+{v[40:55]}"(st.sb), "+{v[72:87]}"(st.oa), "+{v[132:147]}"(st.ob), "+{v[92:95]}"(st.kf), "+{v[96:99]}"(st.v0), "+{v[100:103]}"(st.v1),
          "={v[104:107]}"(p0), "+{v[108:111]}"(st.p1), "+{v112}"(st.kaddr), "+{v113}"(st.vaddr), [n] "+s"(cnt), [keep] "=&s"(keep), "+{s[90:91]}"(src), "+{s92}"(dst)
        : "{v[56:71]}"(st.negma), "{v[116:131]}"(st.negmb), "{v[88:91]}"(st.qa), "{v[148:151]}"(st.qb), "{v114}"(st.vstride), "{v115}"(voff)
        : "memory", "scc");
}
typedef unsigned u32x16 __attribute__((ext_vector_type(16)));
struct Prefetch { u32x16 a0, a1, b0, b1, w0, w1; };
template <int TB>
__device__ __forceinline__ void attn_second(AttnState& st, Prefetch& pf, u32x4 rs, unsigned voffw, unsigned voffl, unsigned soa, unsigned sob, unsigned sow) {
    u32x4 p0;
    if constexpr (TB == 0) {
    asm volatile(
        A_ALIGN
        "buffer_load_dwordx4 v[152:155], v115, %[rs], s93 offen offset:0 sc1\n\t" A_STEP(1024, 512, 1024)
        "buffer_load_dwordx4 v[156:159], v115, %[rs], s93 offen offset:1024 sc1\n\t" A_STEP(2048, 1536, 2048)
        "buffer_load_dwordx4 v[160:163], v115, %[rs], s93 offen offset:2048 sc1\n\t" A_STEP(3072, 2560, 3072)
        "buffer_load_dwordx4 v[164:167], v115, %[rs], s93 offen offset:3072 sc1\n\t" "s_add_u32 s93, s93, 4096\n\t" A_STEP(4096, 3584, 4096)
        "v_add_u32 v112, 4096, v112\n\t" "v_add_u32 v113, v114, v113\n\t"
        "buffer_load_dwordx4 v[168:171], v115, %[rs], s93 offen offset:0 sc1\n\t" A_STEP(1024, 512, 1024)
        "buffer_load_dwordx4 v[172:175], v115, %[rs], s93 offen offset:1024 sc1\n\t" A_STEP(2048, 1536, 2048)
        "buffer_load_dwordx4 v[176:179], v115, %[rs], s93 offen offset:2048 sc1\n\t" A_STEP(3072, 2560, 3072)
        "buffer_load_dwordx4 v[180:183], v115, %[rs], s93 offen offset:3072 sc1\n\t" "s_add_u32 s93, s93, 4096\n\t" A_STEP(4096, 3584, 4096)
        "v_add_u32 v112, 4096, v112\n\t" "v_add_u32 v113, v114, v113\n\t"
        "buffer_load_dwordx4 v[184:187], v115, %[rs], s94 offen offset:0 sc1\n\t" A_STEP(1024, 512, 1024)
        "buffer_load_dwordx4 v[188:191], v115, %[rs], s94 offen offset:1024 sc1\n\t" A_STEP(2048, 1536, 2048)
        "buffer_load_dwordx4 v[192:195], v115, %[rs], s94 offen offset:2048 sc1\n\t" A_STEP(3072, 2560, 3072)
        "buffer_load_dwordx4 v[196:199], v115, %[rs], s94 offen offset:3072 sc1\n\t" "s_add_u32 s94, s94, 4096\n\t" A_STEP(4096, 3584, 4096)
        "v_add_u32 v112, 4096, v112\n\t" "v_add_u32 v113, v114, v113\n\t"
        "buffer_load_dwordx4 v[200:203], v115, %[rs], s94 offen offset:0 sc1\n\t" A_STEP(1024, 512, 1024)
        "buffer_load_dwordx4 v[204:207], v115, %[rs], s94 offen offset:1024 sc1\n\t" A_STEP(2048, 1536, 2048)
        "buffer_load_dwordx4 v[208:211], v115, %[rs], s94 offen offset:2048 sc1\n\t" A_STEP(3072, 2560, 3072)
        "buffer_load_dwordx4 v[212:215], v115, %[rs], s94 offen offset:3072 sc1\n\t" "s_add_u32 s94, s94, 4096\n\t" A_STEP(4096, 3584, 4096)
        "v_add_u32 v112, 4096, v112\n\t" "v_add_u32 v113, v114, v113\n\t"
        "s_waitcnt lgkmcnt(0)\n\t"
        A_MPV(A_OB, A_V1, A_P1)
        "s_nop 15\n\t"
        : "+{v[24:39]}"(st.sa), "+{v[40:55]}"(st.sb), "+{v[72:87]}"(st.oa), "+{v[132:147]}"(st.ob), "+{v[92:95]}"(st.kf), "+{v[96:99]}"(st.v0), "+{v[100:103]}"(st.v1),
          "={v[104:107]}"(p0), "+{v[108:111]}"(st.p1), "+{v112}"(st.kaddr), "+{v113}"(st.vaddr), "+{s93}"(soa), "+{s94}"(sob),
          "=&{v[152:167]}"(pf.a0), "=&{v[168:183]}"(pf.a1), "=&{v[184:199]}"(pf.b0), "=&{v[200:215]}"(pf.b1)
        : "{v[56:71]}"(st.negma), "{v[116:131]}"(st.negmb), "{v[88:91]}"(st.qa), "{v[148:151]}"(st.qb), "{v114}"(st.vstride), "{v115}"(voffw), [rs] "s"(rs)
        : "memory", "scc");
    } else {
    asm volatile(
        A_ALIGN
        "buffer_load_dwordx4 v[152:155], v115, %[rs], s93 offen offset:0 sc1\n\t" "buffer_load_dwordx4 v[216:219], %[vl], %[rs], s95 offen offset:0 sc1\n\t" A_STEP(1024, 512, 1024)
        "buffer_load_dwordx4 v[156:159], v115, %[rs], s93 offen offset:1024 sc1\n\t" "buffer_load_dwordx4 v[220:223], %[vl], %[rs], s95 offen offset:2048 sc1\n\t" "s_add_u32 s95, s95, 4096\n\t" A_STEP(2048, 1536, 2048)
        "buffer_load_dwordx4 v[160:163], v115, %[rs], s93 offen offset:2048 sc1\n\t" "buffer_load_dwordx4 v[224:227], %[vl], %[rs], s95 offen offset:0 sc1\n\t" A_STEP(3072, 2560, 3072)
        "buffer_load_dwordx4 v[164:167], v115, %[rs], s93 offen offset:3072 sc1\n\t" "s_add_u32 s93, s93, 4096\n\t" "buffer_load_dwordx4 v[228:231], %[vl], %[rs], s95 offen offset:2048 sc1\n\t" "s_add_u32 s95, s95, 4096\n\t" A_STEP(4096, 3584, 4096)
        "v_add_u32 v112, 4096, v112\n\t" "v_add_u32 v113, v114, v113\n\t"
        "s_setprio 0\n\t"
        "buffer_load_dwordx4 v[168:171], v115, %[rs], s93 offen offset:0 sc1\n\t" "buffer_load_dwordx4 v[232:235], %[vl], %[rs], s95 offen offset:0 sc1\n\t" A_STEP(1024, 512, 1024)
        "buffer_load_dwordx4 v[172:175], v115, %[rs], s93 offen offset:1024 sc1\n\t" "buffer_load_dwordx4 v[236:239], %[vl], %[rs], s95 offen offset:2048 sc1\n\t" "s_add_u32 s95, s95, 4096\n\t" A_STEP(2048, 1536, 2048)
        "buffer_load_dwordx4 v[176:179], v115, %[rs], s93 offen offset:2048 sc1\n\t" "buffer_load_dwordx4 v[240:243], %[vl], %[rs], s95 offen offset:0 sc1\n\t" A_STEP(3072, 2560, 3072)
        "buffer_load_dwordx4 v[180:183], v115, %[rs], s93 offen offset:3072 sc1\n\t" "s_add_u32 s93, s93, 4096\n\t" "buffer_load_dwordx4 v[244:247], %[vl], %[rs], s95 offen offset:2048 sc1\n\t" "s_add_u32 s95, s95, 4096\n\t" A_STEP(4096, 3584, 4096)
        "v_add_u32 v112, 4096, v112\n\t" "v_add_u32 v113, v114, v113\n\t"
        "buffer_load_dwordx4 v[184:187], v115, %[rs], s94 offen offset:0 sc1\n\t" A_STEP(1024, 512, 1024)
        "buffer_load_dwordx4 v[188:191], v115, %[rs], s94 offen offset:1024 sc1\n\t" A_STEP(2048, 1536, 2048)
        "buffer_load_dwordx4 v[192:195], v115, %[rs], s94 offen offset:2048 sc1\n\t" A_STEP(3072, 2560, 3072)
        "buffer_load_dwordx4 v[196:199], v115, %[rs], s94 offen offset:3072 sc1\n\t" "s_add_u32 s94, s94, 4096\n\t" A_STEP(4096, 3584, 4096)
        "v_add_u32 v112, 4096, v112\n\t" "v_add_u32 v113, v114, v113\n\t"
        "buffer_load_dwordx4 v[200:203], v115, %[rs], s94 offen offset:0 sc1\n\t" A_STEP(1024, 512, 1024)
        "buffer_load_dwordx4 v[204:207], v115, %[rs], s94 offen offset:1024 sc1\n\t" A_STEP(2048, 1536, 2048)
        "buffer_load_dwordx4 v[208:211], v115, %[rs], s94 offen offset:2048 sc1\n\t" A_STEP(3072, 2560, 3072)
        "buffer_load_dwordx4 v[212:215], v115, %[rs], s94 offen offset:3072 sc1\n\t" "s_add_u32 s94, s94, 4096\n\t" A_STEP(4096, 3584, 4096)
        "v_add_u32 v112, 4096, v112\n\t" "v_add_u32 v113, v114, v113\n\t"
        "s_waitcnt lgkmcnt(0)\n\t"
        A_MPV(A_OB, A_V1, A_P1)
        "s_nop 15\n\t"
        : "+{v[24:39]}"(st.sa), "+{v[40:55]}"(st.sb), "+{v[72:87]}"(st.oa), "+{v[132:147]}"(st.ob), "+{v[92:95]}"(st.kf), "+{v[96:99]}"(st.v0), "+{v[100:103]}"(st.v1),
          "={v[104:107]}"(p0), "+{v[108:111]}"(st.p1), "+{v112}"(st.kaddr), "+{v113}"(st.vaddr), "+{s93}"(soa), "+{s94}"(sob), "+{s95}"(sow),
          "=&{v[152:167]}"(pf.a0), "=&{v[168:183]}"(pf.a1), "=&{v[184:199]}"(pf.b0), "=&{v[200:215]}"(pf.b1), "=&{v[216:231]}"(pf.w0), "=&{v[232:247]}"(pf.w1)
        : "{v[56:71]}"(st.negma), "{v[116:131]}"(st.negmb), "{v[88:91]}"(st.qa), "{v[148:151]}"(st.qb), "{v114}"(st.vstride), "{v115}"(voffw), [rs] "s"(rs), [vl] "v"(voffl)
        : "memory", "scc");
    }
}
template <int I> __device__ __forceinline__ u32x4 quad(const u32x16& v) { return (u32x4){v[4 * I], v[4 * I + 1], v[4 * I + 2], v[4 * I + 3]}; }

struct Sums { float a[4], b[4];
    __device__ __forceinline__ void clear() { _Pragma("unroll") for (int d = 0; d < 4; ++d) { a[d] = 0.f; b[d] = 0.f; } }
    __device__ __forceinline__ void add(u32x4 f) { _Pragma("unroll") for (int d = 0; d < 4; ++d) { a[d % NCH] = dot2(f[d], 0x3f803f80u, a[d % NCH]); b[d % NCH] = dot2(f[d], f[d], b[d % NCH]); } }
    __device__ __forceinline__ float sum() const { return (a[0] + a[1]) + (a[2] + a[3]); }
    __device__ __forceinline__ float sumsq() const { return (b[0] + b[1]) + (b[2] + b[3]); }
};
template <int N>
__device__ __forceinline__ void stat_sums(LAS char* lds, int slot0, int tok, float& t1, float& t2) {
    f32x2 v[N];
#pragma unroll
    for (int i = 0; i < N; ++i) v[i] = *(LAS f32x2*)(lds + L_STAT + ((slot0 + i) * 32 + tok) * 8);
    SBAR();
    t1 = 0.f; t2 = 0.f;
#pragma unroll
    for (int i = 0; i < N; ++i) { t1 += v[i][0]; t2 += v[i][1]; }
}
__device__ __forceinline__ void ln_from_sums(float t1, float t2, float& mu, float& rstd) {
    mu = t1 * (1.f / D);
    rstd = __builtin_amdgcn_rsqf(fmaxf(t2 * (1.f / D) - mu * mu, 0.f) + LN_EPS);
}

template <int TB>
__device__ __forceinline__ void run_layers(unsigned char* ws0, LAS char* lds) {
    const unsigned lbase = (unsigned)(uintptr_t)lds;
    const int tid0 = threadIdx.x, w0 = __builtin_amdgcn_readfirstlane(tid0 >> 6);
    const int kk0 = blockIdx.x >> 3, c0 = (blockIdx.x & 7) * 4 + (kk0 >> 3), j0 = kk0 & 7;
    typedef const __attribute__((address_space(4))) Params* kargs_t;
#define PHASE_IDS() \
    int tid = tid0, w = w0, c = c0, j = j0; unsigned epoch = epoch0; unsigned char* ws = ws0; \
    kargs_t kp = (kargs_t)__builtin_amdgcn_kernarg_segment_ptr();        \
    asm volatile("" : "+v"(tid)); asm volatile("" : "+s"(w), "+s"(c), "+s"(j), "+s"(epoch), "+s"(ws), "+s"(kp)); \
    const rsrc_t rw = __builtin_amdgcn_make_buffer_rsrc(ws, 0, WS_TOTAL, 0x00020000); \
    gu32* cnt = (gu32*)(ws + OFF_CNT); gu32* sync = (gu32*)(ws + OFF_SYNC); gu32* tmo = sync + 2 * CW; \
    (void)rw; (void)cnt; (void)tmo; (void)kp; \
    constexpr int tb = TB; const int lane = tid & 63, r = lane & 31, h = lane >> 5, kq = w & 3, tbg = 2 * c + tb; \
    const int vl = lane * 16;                                     \
    const int vw = (lane ^ ((kq & 1) << 4)) * 16;                 \
    const int cbA = 2 * j + (kq >> 1), cbB = 2 * j + ((kq >> 1) ^ 1); \
    const int sown = 4 * j + kq;                                  \
    (void)epoch; (void)r; (void)h; (void)tbg; (void)vl; (void)vw; (void)cbA; (void)cbB; (void)sown; (void)tb;
#define W1_IDS() \
    const int ncb = j < 3 ? 6 : 5, cb0 = j < 3 ? 6 * j : 18 + 5 * (j - 3);     \
    const int cb1 = TB == 0 ? kq : 4 + (kq >> 1);     \
    const bool d_active = cb1 < ncb; \
    const int cbg1 = cb0 + (d_active ? cb1 : 0); (void)cbg1; \
    const int par = TB == 0 ? 0 : (kq & 1); (void)par;

#define REQ_WQ(ww, lo, hi) do { \
        const unsigned vo_ = (lane >> 3) * (D * 2) + (((((lane & 7) >> 2) ^ ((lane >> 4) & 1)) * 32 + (lane & 3) * 8) * 2); \
        const uint16_t* g_ = kp->wq + (size_t)(64 * (ww)) * D + 64 * j; \
        _Pragma("unroll") for (int i = (lo); i < (hi); ++i) dma1(g_ + i * 8 * D, vo_, lbase + L_WQ + ((ww) * 8 + i) * 1024); } while (0)
#define REQ_WKV() do { \
        const uint16_t* src_ = ((lane & 2) ? kp->wv : kp->wk) + (128 * kq + (lane >> 2)) * 16 + (lane & 1) * 8; \
        _Pragma("unroll") for (int i = 0; i < 8; ++i) glds16(src_ + i * 256, lbase + ((kq & 2) ? L_WKV1 : L_WKV0) + ((kq & 1) * 8 + i) * 1024); } while (0)

    unsigned epoch_v, epoch0 = 0;
    { gu32* e = (gu32*)(ws0 + OFF_SYNC); asm volatile("" : "+s"(e)); epoch_v = __hip_atomic_load(e, RLX_AGENT); }
    {
        PHASE_IDS();
        if (tid < 16) *(LAS unsigned*)(lds + (tid < 8 ? F_PAIR + tid * 4 : F_OCNT)) = 0u;
        if (j == tb && kq == 0 && lane < 4) __hip_atomic_store(cnt + cnt_word(j, lane, c), 0u, RLX_AGENT);
        if (j == 0 && tid == 4) __hip_atomic_store((gu32*)(ws + OFF_PREPC) + c * CW, 0u, RLX_AGENT);
        if (c == 0 && j == 0 && tid == 5) __hip_atomic_store(tmo, 0u, RLX_AGENT);
    }

#pragma unroll 1
    for (int L = 0; L < 2; ++L) {
        u32x4 qown; float q2own;
        {
            PHASE_IDS();
            const bool kvm = j < 2;
            const bool kvteam = j == tb;
            if (kvm && kvteam) {
                __builtin_amdgcn_s_setprio(2);
                if (L == 0) REQ_WKV();
            } else {
                if (L == 0) REQ_WQ(w, 0, 4);
                REQ_WQ(w, 4, 8);
                if (kvm) {
                    if (L == 0) REQ_WQ(w ^ 4, 0, 4);
                    REQ_WQ(w ^ 4, 4, 8);
                }
            }
            auto prep_dst = [&](int i) -> int {
                const int u = c * 6 + (j - 2);
                if (u < 43) return OFF_W1 + (u * KSD + 4 * w + i) * 1024;
                const int f = ((u - 43) * 8 + w) + i * 1192;
                if (i >= 2 || f >= 1920) return -1;
                return f < 512 ? OFF_WO + f * 1024 : OFF_W2 + (f - 512) * 1024;
            };
            u32x4 xa[8] = {};
            if (L == 0) {
                const rsrc_t rx = __builtin_amdgcn_make_buffer_rsrc((void*)kp->x, 0, S * D * 2, 0x00020000);
#pragma unroll
                for (int i = 0; i < 8; ++i) xa[i] = ld_plain(rx, r * 1024 + h * 16, (64 * c + 32 * tb) * 1024 + (kq * 8 + i) * 32);
                SBAR();
                epoch0 = __builtin_amdgcn_readfirstlane(epoch_v); asm volatile("" : "+s"(epoch0)); epoch = epoch0;
                if (!kvm || kvteam) WAITVM(8);
            } else {
                if (tid == (kvm ? 256 * j : 0)) poll_ge(cnt + cnt_word(0, CNT_X2, c), 8u, tmo);
                __builtin_amdgcn_fence(__ATOMIC_ACQUIRE, "wavefront");
                if (!kvm || kvteam) WAITVM(0);
            }
            if (!(L == 0 && kvm)) LDS_BARRIER();
            if (L == 1) {
#pragma unroll
                for (int i = 0; i < 8; ++i) xa[i] = ld_sc1(rw, vl, OFF_X2 + (tbg * KSD + kq * 8 + i) * 1024);
                SBAR();
            }
            LAS f32x2* st = (LAS f32x2*)(lds + L_STAT);
            LAS float* cst = (LAS float*)(lds + L_CS);
            const int tq = (lane & 15) >> 2, tp = lane & 3, tg = (lane >> 4) & 1;
            f32x16 aA = {}, aB = {};
            auto store_stats = [&]() {
                Sums sm; sm.clear();
#pragma unroll
                for (int i = 0; i < 8; ++i) sm.add(xa[i]);
                const float s1 = half_sum(sm.sum()), s2 = half_sum(sm.sumsq());
                if (h == 0) st[(tb * 8 + kq) * 32 + r] = (f32x2){s1, s2};
            };
            auto do_q = [&]() {
                u32x4 fa[8], fb[8];
                ColSum cA, cB; cA.clear(); cB.clear();
                const int sw = (tq >> 1) & 1, col = (tg ^ (kq & 1)) * 32 + tp * 8;
                const int a0 = L_WQ + (kq * 128 + 8 * h + tq) * 128 + col;
                const int aa = a0 + (((kq >> 1) ^ sw) * 64), ab = a0 + (((kq >> 1) ^ 1 ^ sw) * 64);
#pragma unroll
                for (int i = 0; i < 8; ++i) { fa[i] = tr_frag(lds, aa + i * 2048, 512); fb[i] = tr_frag(lds, ab + i * 2048, 512); }
                SBAR();
#pragma unroll
                for (int i = 0; i < 8; ++i) { cA.add(fa[i]); cB.add(fb[i]); }
                const float csA = half_sum(cA.sum()), csB = half_sum(cB.sum());
                if (TB == 0 && h == 0) { const int n = r ^ ((kq & 1) << 4); cst[kq * 96 + (kq >> 1) * 32 + n] = csA; cst[kq * 96 + ((kq >> 1) ^ 1) * 32 + n] = csB; }
                SBAR();
#pragma unroll
                for (int i = 0; i < 8; ++i) { aA = mfma(fa[i], xa[i], aA); aB = mfma(fb[i], xa[i], aB); }
                PIN2(aA, aB);
            };
            if (kvm) {
                if (kvteam) {
                    u32x4 wk[8];
                    const int a = ((kq & 2) ? L_WKV1 : L_WKV0) + ((kq & 1) * 128 + 8 * h + tq) * 64 + tg * 32 + tp * 8;
#pragma unroll
                    for (int i = 0; i < 8; ++i) wk[i] = tr_frag(lds, a + i * 1024, 256);
                    SBAR();
                    ColSum ck; ck.clear();
#pragma unroll
                    for (int i = 0; i < 8; ++i) ck.add(wk[i]);
                    const float cs = half_sum(ck.sum());
                    if (h == 0) cst[kq * 96 + 64 + r] = cs;
                    SBAR();
                    f32x16 ak = {}, av = {};
#pragma unroll
                    for (int i = 0; i < 8; ++i) { ak = mfma(wk[i], xa[i], ak); av = mfma(xa[i], wk[i], av); }
                    store_stats();
                    if (kq == 3 && lane == 0) *(LAS unsigned*)(lds + F_KVCNT) = 0u;
                    const int o = L_KVRED + kq * 4096;
                    lds_stf(lds, o + vl, (f32x4){ak[0], ak[1], ak[2], ak[3]}); lds_stf(lds, o + 1024 + vl, (f32x4){ak[4], ak[5], ak[6], ak[7]});
                    if (r >= 16) {
#pragma unroll
                        for (int g = 0; g < 4; ++g) lds_stf(lds, o + 2048 + g * 512 + (h * 16 + r - 16) * 16, (f32x4){av[4 * g], av[4 * g + 1], av[4 * g + 2], av[4 * g + 3]});
                    }
                } else WAITVM(8);
                LDS_BARRIER();
                if (kvteam && kq < 3) {
                    f32x4 a = {}, e = {}, ta[4], te[4];
                    float t1, t2, mu, rstd;
                    if (kq == 0) {
                        f32x4 k0 = {}, k1 = {}, u0[4], u1[4];
#pragma unroll
                        for (int i = 0; i < 4; ++i) { ta[i] = lds_ldf(lds, L_KVRED + i * 4096 + vl); te[i] = lds_ldf(lds, L_KVRED + i * 4096 + 1024 + vl);
                                                      u0[i] = lds_ldf(lds, L_CS + (i * 96 + 64 + 4 * h) * 4); u1[i] = lds_ldf(lds, L_CS + (i * 96 + 64 + 8 + 4 * h) * 4); }
                        stat_sums<4>(lds, tb * 8, r, t1, t2); ln_from_sums(t1, t2, mu, rstd);
#pragma unroll
                        for (int i = 0; i < 4; ++i) { a += ta[i]; e += te[i]; k0 += u0[i]; k1 += u1[i]; }
#pragma unroll
                        for (int i = 0; i < 4; ++i) { a[i] = (a[i] - mu * k0[i]) * rstd; e[i] = (e[i] - mu * k1[i]) * rstd; }
                        st_sc1((u32x4){pk(a[0], a[1]), pk(a[2], a[3]), pk(e[0], e[1]), pk(e[2], e[3])}, rw, vl, OFF_KF + L * 65536 + tbg * 1024);
                        float n2 = (a[0] * a[0] + a[1] * a[1]) + (a[2] * a[2] + a[3] * a[3]) + (e[0] * e[0] + e[1] * e[1]) + (e[2] * e[2] + e[3] * e[3]);
                        n2 = wave_max(half_sum(n2));
                        if (lane == 0) *(LAS float*)(lds + F_N2) = n2;
                    } else {
                        const int g = kq - 1, lc = (h * 16 + (r & 15)) * 16;
                        float csv = 0.f, uc[4];
#pragma unroll
                        for (int i = 0; i < 4; ++i) { ta[i] = lds_ldf(lds, L_KVRED + i * 4096 + 2048 + (2 * g) * 512 + lc); te[i] = lds_ldf(lds, L_KVRED + i * 4096 + 2048 + (2 * g + 1) * 512 + lc);
                                                      uc[i] = cst[i * 96 + 64 + 16 + (r & 15)]; }
                        stat_sums<4>(lds, tb * 8, r, t1, t2); ln_from_sums(t1, t2, mu, rstd);
#pragma unroll
                        for (int i = 0; i < 4; ++i) { a += ta[i]; e += te[i]; csv += uc[i]; }
#pragma unroll
                        for (int i = 0; i < 8; ++i) {
                            const int tok = 16 * g + (i & 3) + 8 * (i >> 2) + 4 * h;
                            const float m2 = __uint_as_float(__builtin_amdgcn_ds_bpermute(tok * 4, __float_as_uint(mu))), r2 = __uint_as_float(__builtin_amdgcn_ds_bpermute(tok * 4, __float_as_uint(rstd)));
                            if (i < 4) a[i] = (a[i] - m2 * csv) * r2; else e[i - 4] = (e[i - 4] - m2 * csv) * r2;
                        }
                        if (r >= 16) st_sc1((u32x4){pk(a[0], a[1]), pk(a[2], a[3]), pk(e[0], e[1]), pk(e[2], e[3])}, rw, h * 256 + (r - 16) * 16, OFF_VF + L * 65536 + (2 * tbg + g) * 512);
                    }
                }
                if (kvteam && kq < 3) {
                    DRAIN();
                    unsigned old = 0;
                    if (lane == 0) old = __hip_atomic_fetch_add((LAS unsigned*)(lds + F_KVCNT), 1u, __ATOMIC_RELAXED, __HIP_MEMORY_SCOPE_WORKGROUP);
                    if (__builtin_amdgcn_readfirstlane(old) == 2u && lane < 32)
                        __hip_atomic_store((gu64*)(ws + OFF_KVFL) + (L * 32 + lane) * 64 + 2 * c + j,
                                           ((unsigned long long)(epoch + 1u + L) << 32) | __float_as_uint(*(volatile LAS float*)(lds + F_N2)), RLX_AGENT);
                }
                __builtin_amdgcn_s_setprio(0);
                do_q();
                if (!kvteam) store_stats();
            } else {
                do_q(); store_stats();
            }
            if (L == 0 && !kvm) {
#pragma unroll
                for (int i = 0; i < 4; ++i) {
                    const int d = prep_dst(i);
                    if (d < 0) continue;
                    const uint16_t* src; int ld, cb, s;
                    if (d >= OFF_W2) { const int g = (d - OFF_W2) >> 10; src = kp->w2; ld = D; cb = g / KSFP; s = min(g % KSFP, KSF - 1); }
                    else if (d >= OFF_W1) { const int g = (d - OFF_W1) >> 10; src = kp->w1; ld = F; cb = g >> 5; s = g & 31; }
                    else { const int g = (d - OFF_WO) >> 10; src = kp->wo; ld = D; cb = g >> 5; s = g & 31; }
                    glds16(src + (size_t)(16 * s + (lane >> 2)) * ld + 32 * cb + (lane & 3) * 8, lbase + L_PREP + (w * 4 + i) * 1024);
                }
            }
            float q[8];
            reduce4(lds, kvm ? (kvteam ? 0 : 131072) : (TB == 0 ? 32768 : 131072), aA, aB, kq, lane, q);
            float t1, t2;
            f32x4 cs0 = {}, cs1 = {}, u0[4], u1[4];
#pragma unroll
            for (int i = 0; i < 4; ++i) { u0[i] = lds_ldf(lds, L_CS + (i * 96 + 16 * kq + 4 * h) * 4); u1[i] = lds_ldf(lds, L_CS + (i * 96 + 16 * kq + 8 + 4 * h) * 4); }
            stat_sums<4>(lds, tb * 8, r, t1, t2);
#pragma unroll
            for (int i = 0; i < 4; ++i) { cs0 += u0[i]; cs1 += u1[i]; }
            float mu, rstd; ln_from_sums(t1, t2, mu, rstd);
            float n2 = 0.f;
#pragma unroll
            for (int i = 0; i < 4; ++i) { q[i] = (q[i] - mu * cs0[i]) * (rstd * QSCALE); q[4 + i] = (q[4 + i] - mu * cs1[i]) * (rstd * QSCALE); }
#pragma unroll
            for (int i = 0; i < 8; ++i) n2 += q[i] * q[i];
            q2own = half_sum(n2);
            qown = (u32x4){pk(q[0], q[1]), pk(q[2], q[3]), pk(q[4], q[5]), pk(q[6], q[7])};
            lds_st(lds, L_XCHQ + w * 1024 + vl, qown);
            *(LAS float*)(lds + L_STAT + (tb * 8 + 4 + kq) * 256 + lane * 4) = q2own;
            if (L == 0 && !kvm) {
                WAITVM(0);
                ColSum cp; cp.clear();
#pragma unroll
                for (int i = 0; i < 4; ++i) {
                    const int d = prep_dst(i);
                    if (d < 0) continue;
                    u32x4 fr = tr_frag(lds, L_PREP + (w * 4 + i) * 1024 + (8 * h + tq) * 64 + tg * 32 + tp * 8, 256);
                    if (d >= OFF_W2 && ((d - OFF_W2) >> 10) % KSFP >= KSF) fr = (u32x4){0u, 0u, 0u, 0u};
                    st_sc1(fr, rw, vl, d);
                    cp.add(fr);
                }
                const int u = c * 6 + (j - 2);
                if (u < 43) {
                    const float cs = half_sum(cp.sum());
                    LAS float* t = (LAS float*)(lds + L_WQ);
                    if (h == 0) t[w * 32 + r] = cs;
                    LDS_BARRIER();
                    if (tid < 32) { float v = 0.f, pv[8];
#pragma unroll
                        for (int i = 0; i < 8; ++i) pv[i] = t[i * 32 + tid];
                        SBAR();
#pragma unroll
                        for (int i = 0; i < 8; ++i) v += pv[i];
                        __hip_atomic_store((gu32*)(ws + OFF_CS1 + (32 * u + tid) * 4), __float_as_uint(v), RLX_AGENT); }
                }
                DRAIN();
            }
        }
        u32x4 woA[8], woB[8];
        u32x4 w1f[32];
        {
            PHASE_IDS();
            const unsigned char* kvsrc = ws + (TB == 0 ? OFF_KF : OFF_VF) + L * 65536 + ((w >> 1) & 1) * 32768 + (w & 1) * 6144;
            unsigned kvdst = lbase + (TB == 0 ? L_K : L_V) + ((w >> 1) & 1) * 32768 + (w & 1) * 6144, kvoff = vl;
            asm volatile("" : "+s"(kvsrc), "+s"(kvdst)); asm volatile("" : "+v"(kvoff));
            const float km2 = wait_flags(tid, lds, (gu64*)(ws + OFF_KVFL) + (L * 32 + c) * 64, epoch + 1u + L, tmo);
            dma_run_sc1<6>(kvsrc, kvoff, kvdst);
            if (L == 0 && j >= 2 && tid == 0) __hip_atomic_fetch_add((gu32*)(ws + OFF_PREPC) + c * CW, 1u, RLX_AGENT);
            { unsigned one2 = ((tid & 31) == 0) ? 0x3f803f80u : 0u; asm volatile("" : "+v"(one2)); if (tid < 272) lds_st(lds, L_ONES + tid * 16, (u32x4){one2, one2, one2, one2}); }
            const u32x4 qpar = lds_ld(lds, L_XCHQ + (w ^ 1) * 1024 + vl);
            const float q2par = *(LAS float*)(lds + L_STAT + (tb * 8 + 4 + (kq ^ 1)) * 256 + lane * 4);
            float ma = __builtin_amdgcn_sqrtf(q2own * km2) * 1.02f, mb = __builtin_amdgcn_sqrtf(q2par * km2) * 1.02f;
            const bool unsafe = __any(!(ma <= SAFE_BOUND) || !(mb <= SAFE_BOUND));
            {
                LAS unsigned* fl = (LAS unsigned*)(lds + F_UNSAFE);
                if (lane == 0) fl[w] = unsafe ? 1u : 0u;
            }
            WAITVM(0);
            LDS_BARRIER();
            const int kh = kq & 1;
            const bool real = r < 16;
            const unsigned kaddr = L_K + kh * 32768 + vl;
            const unsigned vaddr = real ? (unsigned)(L_V + kh * 32768 + h * 256 + r * 16) : (unsigned)(L_ONES + ((r >= 20 && r < 28) ? 64 : r != 16 ? 16 : 0));
            const u32x4 kf0 = lds_ld(lds, kaddr), vf0 = lds_ld(lds, vaddr);
            {
                LAS unsigned* fl = (LAS unsigned*)(lds + F_UNSAFE);
                const u32x4 f0 = *(LAS u32x4*)fl, f1 = *(LAS u32x4*)(fl + 4);
                if (__builtin_amdgcn_readfirstlane(f0[0] | f0[1] | f0[2] | f0[3] | f1[0] | f1[1] | f1[2] | f1[3])) {
                    {
                        const unsigned char* src = ws + (TB == 0 ? OFF_KF : OFF_VF) + L * 65536 + ((w >> 1) & 1) * 32768 + (12 + (w & 1) * 10) * 1024 + vl;
                        const unsigned dst = lbase + (TB == 0 ? L_K : L_V) + ((w >> 1) & 1) * 32768 + (12 + (w & 1) * 10) * 1024;
#pragma unroll 1
                        for (int i = 0; i < 10; ++i) glds16_sc1(src + i * 1024, __builtin_amdgcn_readfirstlane(dst + i * 1024));
                    }
                    DRAIN();
                    LDS_BARRIER();
                    if (unsafe) {
                        float xa = -INFINITY, xb = -INFINITY;
#pragma unroll 1
                        for (int t = 0; t < 64; ++t) {
                            const u32x4 kf = lds_ld(lds, L_K + t * 1024 + vl);
                            const f32x16 sa = mfma(kf, qown, (f32x16){}), sb = mfma(kf, qpar, (f32x16){});
#pragma unroll
                            for (int i = 0; i < 16; ++i) { xa = fmaxf(xa, sa[i]); xb = fmaxf(xb, sb[i]); }
                        }
                        ma = half_max(xa); mb = half_max(xb);
                    }
                }
            }
            if (TB == 1) __builtin_amdgcn_s_setprio(1);
            AttnState st;
            attn_begin(st, lds, qown, qpar, kf0, vf0, ma, mb, kaddr, vaddr, real ? 4096u : 0u);
            attn_first(st, (unsigned long long)(uintptr_t)(ws + (TB == 0 ? OFF_KF : OFF_VF) + L * 65536 + ((w >> 1) & 1) * 32768 + (12 + (w & 1) * 10) * 1024),
                       lbase + (TB == 0 ? L_K : L_V) + ((w >> 1) & 1) * 32768 + (12 + (w & 1) * 10) * 1024, (unsigned)vl);
            if (L == 0) {
                unsigned spins = 0;
                while (!__all(__hip_atomic_load((gu32*)(ws + OFF_PREPC) + (lane & 31) * CW, RLX_AGENT) >= 6u)) {
                    if (++spins > SPIN_CAP) { if (lane == 0) __hip_atomic_store(tmo, 1u, RLX_AGENT); break; }
                }
                __builtin_amdgcn_fence(__ATOMIC_ACQUIRE, "wavefront");
            }
            Prefetch pf;
            { W1_IDS();
              const unsigned long long wsa = (unsigned long long)(uintptr_t)ws;
              const u32x4 rs = {(unsigned)wsa, (unsigned)(wsa >> 32) & 0xffffu, (unsigned)WS_TOTAL, 0x00020000u};
              attn_second<TB>(st, pf, rs, (unsigned)vw, (unsigned)vl, OFF_WO + (cbA * KSD + kq * 8) * 1024, OFF_WO + (cbB * KSD + kq * 8) * 1024, OFF_W1 + (cbg1 * KSD + par) * 1024); }
            if (TB == 1) __builtin_amdgcn_s_setprio(0);
            const f32x16 oa = st.oa, ob = st.ob;
            const int xo = L_XCHO + w * 2304;
            lds_stf(lds, xo + vl, (f32x4){ob[0], ob[1], ob[2], ob[3]}); lds_stf(lds, xo + 1024 + vl, (f32x4){ob[4], ob[5], ob[6], ob[7]});
            *(LAS float*)(lds + xo + 2048 + lane * 4) = half_sum(ob[8]);
            asm volatile("s_waitcnt lgkmcnt(0)" ::: "memory");
            *(volatile LAS unsigned*)(lds + F_PAIR + w * 4) = L + 1u;
            while (*(volatile LAS unsigned*)(lds + F_PAIR + (w ^ 1) * 4) != L + 1u) { }
            asm volatile("" ::: "memory");
            const int xi = L_XCHO + (w ^ 1) * 2304;
            const f32x4 e0 = lds_ldf(lds, xi + vl), e1 = lds_ldf(lds, xi + 1024 + vl);
            const float lpar = *(LAS float*)(lds + xi + 2048 + lane * 4);
            SBAR();
            const float rl = __builtin_amdgcn_rcpf(half_sum(oa[8]) + lpar);
            const u32x4 of = {pk((oa[0] + e0[0]) * rl, (oa[1] + e0[1]) * rl), pk((oa[2] + e0[2]) * rl, (oa[3] + e0[3]) * rl),
                              pk((oa[4] + e1[0]) * rl, (oa[5] + e1[1]) * rl), pk((oa[6] + e1[2]) * rl, (oa[7] + e1[3]) * rl)};
            st_sc1(swap_order(of), rw, vl, OFF_O + L * ACT_BYTES + (tbg * KSD + sown) * 1024);
            if (TB == 0) asm volatile("s_waitcnt vmcnt(0)" : "+{v[152:167]}"(pf.a0), "+{v[168:183]}"(pf.a1), "+{v[184:199]}"(pf.b0), "+{v[200:215]}"(pf.b1) :: "memory");
            else asm volatile("s_waitcnt vmcnt(0)" : "+{v[152:167]}"(pf.a0), "+{v[168:183]}"(pf.a1), "+{v[184:199]}"(pf.b0), "+{v[200:215]}"(pf.b1), "+{v[216:231]}"(pf.w0), "+{v[232:247]}"(pf.w1) :: "memory");
            unsigned old = 0;
            if (lane == 0) old = __hip_atomic_fetch_add((LAS unsigned*)(lds + F_OCNT), 1u, __ATOMIC_RELAXED, __HIP_MEMORY_SCOPE_WORKGROUP);
            if (__builtin_amdgcn_readfirstlane(old) == 8u * L + 7u && lane == 0) __hip_atomic_fetch_add(cnt + cnt_word(L, CNT_O, c), 1u, RLX_AGENT);
            woA[0] = quad<0>(pf.a0); woA[1] = quad<1>(pf.a0); woA[2] = quad<2>(pf.a0); woA[3] = quad<3>(pf.a0);
            woA[4] = quad<0>(pf.a1); woA[5] = quad<1>(pf.a1); woA[6] = quad<2>(pf.a1); woA[7] = quad<3>(pf.a1);
            woB[0] = quad<0>(pf.b0); woB[1] = quad<1>(pf.b0); woB[2] = quad<2>(pf.b0); woB[3] = quad<3>(pf.b0);
            woB[4] = quad<0>(pf.b1); woB[5] = quad<1>(pf.b1); woB[6] = quad<2>(pf.b1); woB[7] = quad<3>(pf.b1);
            if (TB == 1) {
                w1f[0] = quad<0>(pf.w0); w1f[1] = quad<1>(pf.w0); w1f[2] = quad<2>(pf.w0); w1f[3] = quad<3>(pf.w0);
                w1f[4] = quad<0>(pf.w1); w1f[5] = quad<1>(pf.w1); w1f[6] = quad<2>(pf.w1); w1f[7] = quad<3>(pf.w1);
            }
            if (TB == 0) { W1_IDS();
#pragma unroll
                for (int i = 0; i < W1_EARLY; ++i) { w1f[i] = ld_sc1(rw, vl, OFF_W1 + (cbg1 * KSD + i) * 1024); SBAR(); __builtin_amdgcn_s_sleep(W1_PAUSE); SBAR(); }
            }
        }
        u32x4 resid;
        {
            PHASE_IDS();
            if (L == 0) { const rsrc_t rx = __builtin_amdgcn_make_buffer_rsrc((void*)kp->x, 0, S * D * 2, 0x00020000);
                          resid = ld_plain(rx, r * 1024 + h * 16, (64 * c + 32 * tb) * 1024 + sown * 32); }
            else resid = ld_sc1(rw, vl, OFF_X2 + (tbg * KSD + sown) * 1024);
            wait_cnt(tid, cnt + cnt_word(L, CNT_O, c), 8u, tmo);
            u32x4 xa[8];
#pragma unroll
            for (int i = 0; i < 8; ++i) xa[i] = ld_sc1(rw, vl, OFF_O + L * ACT_BYTES + (tbg * KSD + kq * 8 + i) * 1024);
            SBAR();
            f32x16 aA = {}, aB = {};
#pragma unroll
            for (int i = 0; i < 8; ++i) { aA = mfma(woA[i], xa[i], aA); aB = mfma(woB[i], xa[i], aB); }
            PIN2(aA, aB);
            SBAR();
            if (TB == 0) { W1_IDS();
#pragma unroll
                for (int n = W1_EARLY; n < 32; ++n) w1f[n] = ld_sc1(rw, vl, OFF_W1 + (cbg1 * KSD + n) * 1024);
                SBAR();
            } else dma_run_sc1<3>(ws + OFF_W2 + ((2 * j + (w & 1)) * KSFP + (w >> 1) * 22 + W2_REG + 3) * 1024, vl, lbase + L_W2A + (w * W2_A + 3) * 1024);
            float o[8];
            reduce4(lds, tb * 24576, aA, aB, kq, lane, o);
            resid = swap_order(resid);
#pragma unroll
            for (int i = 0; i < 4; ++i) resid[i] = pk(bflo(resid[i]) + o[2 * i], bfhi(resid[i]) + o[2 * i + 1]);
            st_sc1(swap_order(resid), rw, vl, OFF_X1 + L * ACT_BYTES + (tbg * KSD + sown) * 1024);
            publish(tid, cnt + cnt_word(L, CNT_X1, c));
        }
        u32x4 w2A[W2_REG], w2B[W2_REG];
        {
            PHASE_IDS(); W1_IDS();
            if (TB == 1) {
#pragma unroll
                for (int n = 0; n < 8; ++n) w1f[16 + n] = ld_sc1(rw, vl, OFF_W1 + (cbg1 * KSD + 16 + 2 * n + par) * 1024);
            }
            unsigned tab_cs = 0; unsigned short tab_b = 0;
            if (w >= 1 && w <= 3) { const int col = min(32 * cb0 + tid - 64, F - 1);
                tab_cs = __builtin_amdgcn_raw_buffer_load_b32(rw, col * 4, OFF_CS1, AUX_SC1); tab_b = kp->b1[col]; }
            SBAR();
            const unsigned char* xsrc = ws + OFF_X1 + L * ACT_BYTES + tbg * KSD * 1024;
            unsigned xdst = lbase + L_ACT + tb * KSD * 1024, xoff = vl;
            asm volatile("" : "+s"(xsrc), "+s"(xdst)); asm volatile("" : "+v"(xoff));
            wait_cnt(tid, cnt + cnt_word(L, CNT_X1, c), 8u, tmo);
            if (w >= 1 && w <= 3) { *(LAS float*)(lds + L_TAB + (tid - 64) * 4) = __uint_as_float(tab_cs); *(LAS float*)(lds + L_TAB + 768 + (tid - 64) * 4) = bf2f(tab_b); }
            if (TB == 0) asm volatile("" : "+v"(w1f[24]), "+v"(w1f[25]), "+v"(w1f[26]), "+v"(w1f[27]), "+v"(w1f[28]), "+v"(w1f[29]), "+v"(w1f[30]), "+v"(w1f[31]));
            else asm volatile("" : "+v"(w1f[16]), "+v"(w1f[17]), "+v"(w1f[18]), "+v"(w1f[19]), "+v"(w1f[20]), "+v"(w1f[21]), "+v"(w1f[22]), "+v"(w1f[23]));
            dma_run_sc1<2>(xsrc + 2 * kq * 1024, xoff, xdst + 2 * kq * 1024);
            asm volatile("s_barrier" ::: "memory");
            dma_run_sc1<6>(xsrc + (8 + 6 * kq) * 1024, xoff, xdst + (8 + 6 * kq) * 1024);
            WAITVM(6);
            LDS_BARRIER();
            f32x16 a0 = {}, a1 = {};
            Sums sm; sm.clear();
            const unsigned char* w2a_src = ws + OFF_W2 + ((2 * j + (w & 1)) * KSFP + (w >> 1) * 22 + W2_REG) * 1024;
            auto req = [&](int n) {
                if (n < 5) dma4_sc1<1>(w2a_src + (6 + n) * 1024, vl, lbase + L_W2A + (w * W2_A + 6 + n) * 1024);
                else if (n < 5 + W2_REG) w2A[n - 5] = ld_sc1(rw, vw, OFF_W2 + (cbA * KSFP + kq * 22 + n - 5) * 1024);
                else if (n < 5 + 2 * W2_REG) w2B[n - 5 - W2_REG] = ld_sc1(rw, vw, OFF_W2 + (cbB * KSFP + kq * 22 + n - 5 - W2_REG) * 1024);
                else if (TB == 0 && n < 20) {
                    const int m = n - 11, run = m < 6 ? kq : kq + 4, pc = m < 6 ? m : m - 6;
                    dma4_sc1<1>(ws + OFF_W2 + ((2 * j + (run & 1)) * KSFP + (run >> 1) * 22 + W2_REG + pc) * 1024, vl, lbase + L_W2A + (run * W2_A + pc) * 1024); }
            };
            u32x4 sv[6];
            auto stat_read = [&](int k0, int n) {
#pragma unroll
                for (int i = 0; i < 6; ++i) if (i < n) sv[i] = lds_ld(lds, L_ACT + (tb * KSD + k0 + i) * 1024 + vl);
            };
            auto stat_step = [&](int first, int n, int s) { if (s >= first && s < first + n) sm.add(sv[s - first]); };
            const int act = L_ACT + vl;
            {
                constexpr int N = TB == 0 ? 8 : 4, ST = TB == 0 ? 1 : 2;
                const int ab = act + par * 1024;
                constexpr int PD = 4;
                u32x4 b0[PD + 1], b1[PD + 1];
                stat_read(2 * kq, 2);
#pragma unroll
                for (int i = 0; i < PD; ++i) { b0[i] = lds_ld(lds, ab + ST * i * 1024); b1[i] = lds_ld(lds, ab + (KSD + ST * i) * 1024); }
#pragma unroll
                for (int s = 0; s < N; ++s) {
                    if (s + PD < N) { b0[(s + PD) % (PD + 1)] = lds_ld(lds, ab + ST * (s + PD) * 1024); b1[(s + PD) % (PD + 1)] = lds_ld(lds, ab + (KSD + ST * (s + PD)) * 1024); }
                    SBAR();
                    a0 = mfma(w1f[s], b0[s % (PD + 1)], a0); a1 = mfma(w1f[s], b1[s % (PD + 1)], a1);
                    PIN2(a0, a1);
                    stat_step(N - 2, 2, s);
                    SBAR();
                }
            }
            WAITVM(0);
            LDS_BARRIER();
            if (TB == 0) {
                constexpr int PD = 7;
                u32x4 b0[PD + 1];
                stat_read(8 + 6 * kq, 6);
#pragma unroll
                for (int i = 0; i < PD; ++i) b0[i] = lds_ld(lds, act + (8 + i) * 1024);
#pragma unroll
                for (int s = 0; s < 24; ++s) {
                    if (s + PD < 24) b0[(s + PD) % (PD + 1)] = lds_ld(lds, act + (8 + s + PD) * 1024);
                    SBAR();
                    a0 = mfma(w1f[8 + s], b0[s % (PD + 1)], a0);
                    asm volatile("" : "+v"(a0));
                    if ((s & 1) == 0) req(s / 2);
                    stat_step(10, 6, s);
                    SBAR();
                }
            } else {
                const int ab = act + (8 + par) * 1024;
                constexpr int PD = 4;
                u32x4 b0[PD + 1], b1[PD + 1];
                stat_read(8 + 6 * kq, 6);
#pragma unroll
                for (int i = 0; i < PD; ++i) { b0[i] = lds_ld(lds, ab + 2 * i * 1024); b1[i] = lds_ld(lds, ab + (KSD + 2 * i) * 1024); }
#pragma unroll
                for (int s = 0; s < 12; ++s) {
                    if (s + PD < 12) { b0[(s + PD) % (PD + 1)] = lds_ld(lds, ab + 2 * (s + PD) * 1024); b1[(s + PD) % (PD + 1)] = lds_ld(lds, ab + (KSD + 2 * (s + PD)) * 1024); }
                    SBAR();
                    const int f = 4 + s < 8 ? 4 + s : 12 + s;
                    a0 = mfma(w1f[f], b0[s % (PD + 1)], a0); a1 = mfma(w1f[f], b1[s % (PD + 1)], a1);
                    PIN2(a0, a1);
                    stat_step(5, 6, s);
                    SBAR();
                }
            }
            { const float u1 = half_sum(sm.sum()), u2 = half_sum(sm.sumsq()); if (h == 0) ((LAS f32x2*)(lds + L_STAT))[(tb * 8 + kq) * 32 + r] = (f32x2){u1, u2}; }
            const int xslot = L_ACT + (kq >> 1) * KSD * 1024 + (kq & 1) * 4096 + vl, xslot_p = L_ACT + (kq >> 1) * KSD * 1024 + ((kq & 1) ^ 1) * 4096 + vl;
            if (TB == 1) {
#pragma unroll
                for (int g = 0; g < 4; ++g) {
                    const f32x4 v = par ? (f32x4){a0[4 * g], a0[4 * g + 1], a0[4 * g + 2], a0[4 * g + 3]} : (f32x4){a1[4 * g], a1[4 * g + 1], a1[4 * g + 2], a1[4 * g + 3]};
                    lds_stf(lds, xslot + g * 1024, v);
                }
            }
            LDS_BARRIER();
            constexpr float G1 = -2.f * 0.7978845608028654f * 1.4426950408889634f, G3 = G1 * 0.044715f;
            f32x4 tcs[4], tbb[4];
#pragma unroll
            for (int g = 0; g < 4; ++g) { tcs[g] = lds_ldf(lds, L_TAB + (32 * cb1 + 8 * g + 4 * h) * 4); tbb[g] = lds_ldf(lds, L_TAB + 768 + (32 * cb1 + 8 * g + 4 * h) * 4); }
            f32x2 gx[8], ge[8];
            float g_rstd = 0.f, g_nmr = 0.f;
            auto gelu_slice = [&](const f32x16& acc, int t, int n) {
                if (n == 0) { float t1, t2; stat_sums<4>(lds, t * 8, r, t1, t2); float mu; ln_from_sums(t1, t2, mu, g_rstd); g_nmr = -mu * g_rstd; }
                else if (n <= 3) {
#pragma unroll
                    for (int i = (n - 1) * 3; i < (n == 3 ? 8 : n * 3); ++i) {
                        const f32x2 a2 = {acc[2 * i], acc[2 * i + 1]}, c2 = {tcs[i >> 1][2 * (i & 1)], tcs[i >> 1][2 * (i & 1) + 1]}, b2 = {tbb[i >> 1][2 * (i & 1)], tbb[i >> 1][2 * (i & 1) + 1]};
                        gx[i] = __builtin_elementwise_fma(a2, (f32x2){g_rstd, g_rstd}, __builtin_elementwise_fma(c2, (f32x2){g_nmr, g_nmr}, b2));
                        asm volatile("" : "+v"(gx[i]));
                    }
                } else if (n <= 7) {
#pragma unroll
                    for (int i = (n - 4) * 2; i < (n - 4) * 2 + 2; ++i) { ge[i] = gx[i] * __builtin_elementwise_fma(gx[i] * gx[i], (f32x2){G3, G3}, (f32x2){G1, G1}); asm volatile("" : "+v"(ge[i])); }
                } else if (n <= 13) {
#pragma unroll
                    for (int i = (n - 8) * 3; i < min((n - 8) * 3 + 3, 16); ++i) ge[i >> 1][i & 1] = __builtin_amdgcn_exp2f(ge[i >> 1][i & 1]);
                    asm volatile("" : "+v"(ge[min((n - 8) * 3, 15) >> 1]), "+v"(ge[min((n - 8) * 3 + 2, 15) >> 1]));
                } else if (n == 14) {
#pragma unroll
                    for (int i = 0; i < 8; ++i) ge[i] = ge[i] + (f32x2){1.f, 1.f};
                    PIN8(ge);
                } else if (n <= 20) {
#pragma unroll
                    for (int i = (n - 15) * 3; i < min((n - 15) * 3 + 3, 16); ++i) ge[i >> 1][i & 1] = __builtin_amdgcn_rcpf(ge[i >> 1][i & 1]);
                    asm volatile("" : "+v"(ge[min((n - 15) * 3, 15) >> 1]), "+v"(ge[min((n - 15) * 3 + 2, 15) >> 1]));
                } else if (n == 21) {
#pragma unroll
                    for (int i = 0; i < 8; ++i) gx[i] = gx[i] * ge[i];
                    PIN8(gx);
                } else if (n == 22 && d_active) {
                    const int so = OFF_H + L * H_BYTES + ((2 * c + t) * KSF + 2 * cbg1) * 1024;
                    st_sc1(swap_order((u32x4){pk(gx[0][0], gx[0][1]), pk(gx[1][0], gx[1][1]), pk(gx[2][0], gx[2][1]), pk(gx[3][0], gx[3][1])}), rw, vl, so);
                    st_sc1(swap_order((u32x4){pk(gx[4][0], gx[4][1]), pk(gx[5][0], gx[5][1]), pk(gx[6][0], gx[6][1]), pk(gx[7][0], gx[7][1])}), rw, vl, so + 1024);
                }
            };
            if (TB == 0) {
                constexpr int PD = 5;
                u32x4 b1[PD + 1];
#pragma unroll
                for (int i = 0; i < PD; ++i) b1[i] = lds_ld(lds, act + (KSD + 8 + i) * 1024);
#pragma unroll
                for (int s = 0; s < 24; ++s) {
                    if (s + PD < 24) b1[(s + PD) % (PD + 1)] = lds_ld(lds, act + (KSD + 8 + s + PD) * 1024);
                    SBAR();
                    a1 = mfma(w1f[8 + s], b1[s % (PD + 1)], a1);
                    asm volatile("" : "+v"(a1));
                    if ((s & 1) == 0) req(12 + s / 2);
                    gelu_slice(a0, 0, s);
                    SBAR();
                }
#pragma unroll
                for (int n = 0; n < 23; ++n) gelu_slice(a1, 1, n);
            } else {
                f32x4 v[4];
#pragma unroll
                for (int g = 0; g < 4; ++g) v[g] = lds_ldf(lds, xslot_p + g * 1024);
                SBAR();
#pragma unroll
                for (int g = 0; g < 4; ++g)
#pragma unroll
                    for (int i = 0; i < 4; ++i) a0[4 * g + i] = (par ? a1[4 * g + i] : a0[4 * g + i]) + v[g][i];
#pragma unroll
                for (int n = 0; n < 23; ++n) { req(n); gelu_slice(a0, par, n); SBAR(); }
            }
            publish(tid, cnt + cnt_word(L, CNT_H, c));
        }
        {
            PHASE_IDS();
            dma_run_sc1<W2_B>(ws + OFF_W2 + ((2 * j + (w & 1)) * KSFP + (w >> 1) * 22 + W2_REG + W2_A) * 1024, vl, lbase + L_W2B + W2_B * w * 1024);
            const u32x2 bb0 = *(const GAS u32x2*)(kp->b2 + 16 * sown + 4 * h), bb1 = *(const GAS u32x2*)(kp->b2 + 16 * sown + 8 + 4 * h);
            auto ld_h = [&](int i) { return ld_sc1(rw, vl, OFF_H + L * H_BYTES + tbg * KSF * 1024 + min(kq * 22 + i, KSF - 1) * 1024); };
            if (tid == 0) poll_ge(cnt + cnt_word(L, CNT_H, c), 8u, tmo);
            __builtin_amdgcn_fence(__ATOMIC_ACQUIRE, "wavefront");
            LDS_BARRIER();
            u32x4 ha[22];
#pragma unroll
            for (int i = 0; i < EWIN; ++i) ha[i] = ld_h(i);
            SBAR();
            f32x16 aA = {}, aB = {};
            const int aA0 = L_W2A + ((kq * 2 + (kq >> 1)) * W2_A) * 1024 + vw, aB0 = L_W2A + ((kq * 2 + ((kq >> 1) ^ 1)) * W2_A) * 1024 + vw;
            const int bA0 = L_W2B + ((kq * 2 + (kq >> 1)) * W2_B) * 1024 + vw, bB0 = L_W2B + ((kq * 2 + ((kq >> 1) ^ 1)) * W2_B) * 1024 + vw;
            constexpr int PD = 4;
            u32x4 la[PD + 1], lb[PD + 1];
            auto w_lds = [&](int n) {
                if (n >= W2_REG && n < W2_REG + W2_A) { la[n % (PD + 1)] = lds_ld(lds, aA0 + (n - W2_REG) * 1024); lb[n % (PD + 1)] = lds_ld(lds, aB0 + (n - W2_REG) * 1024); }
                else if (n >= W2_REG + W2_A && n < 22) { la[n % (PD + 1)] = lds_ld(lds, bA0 + (n - W2_REG - W2_A) * 1024); lb[n % (PD + 1)] = lds_ld(lds, bB0 + (n - W2_REG - W2_A) * 1024); }
            };
#pragma unroll
            for (int n = 0; n < PD; ++n) w_lds(n);
#pragma unroll
            for (int i = 0; i < 22; ++i) {
                if (i + EWIN < 22) ha[i + EWIN] = ld_h(i + EWIN);
                if (i + PD == W2_REG + W2_A) asm volatile("s_barrier" ::: "memory");
                w_lds(i + PD);
                SBAR();
                if (i < W2_REG) { aA = mfma(w2A[i], ha[i], aA); aB = mfma(w2B[i], ha[i], aB); }
                else { aA = mfma(la[i % (PD + 1)], ha[i], aA); aB = mfma(lb[i % (PD + 1)], ha[i], aB); }
                PIN2(aA, aB);
                SBAR();
            }
            LDS_BARRIER();
            if (L == 0) {
                if (j == tb) REQ_WKV();
                else { REQ_WQ(w, 0, 4); if (j < 2) REQ_WQ(w ^ 4, 0, 4); }
            }
            float o[8];
            reduce4(lds, tb * 24576, aA, aB, kq, lane, o);
#pragma unroll
            for (int i = 0; i < 2; ++i) { o[2 * i] += bflo(bb0[i]); o[2 * i + 1] += bfhi(bb0[i]); o[4 + 2 * i] += bflo(bb1[i]); o[4 + 2 * i + 1] += bfhi(bb1[i]); }
#pragma unroll
            for (int i = 0; i < 4; ++i) resid[i] = pk(bflo(resid[i]) + o[2 * i], bfhi(resid[i]) + o[2 * i + 1]);
            const u32x4 xo = swap_order(resid);
            if (L == 0) {
                st_sc1(xo, rw, vl, OFF_X2 + (tbg * KSD + sown) * 1024);
                publish(tid, cnt + cnt_word(0, CNT_X2, c));
            } else {
                *(GAS u32x4*)(kp->out + (size_t)(64 * c + 32 * tb + r) * D + 16 * sown + 8 * h) = xo;
            }
        }
    }
    { unsigned e = epoch0; asm volatile("" : "+s"(e)); if (blockIdx.x == 0 && threadIdx.x == 0) __hip_atomic_store((gu32*)(ws0 + OFF_SYNC), e + 2u, RLX_AGENT); }
}


__global__ void __launch_bounds__(512, 2) main_kernel(Params p) {
    extern __shared__ __attribute__((aligned(16))) char lds_dyn[];
    LAS char* lds = (LAS char*)lds_dyn;
    asm volatile("" :: "s"(p.x), "s"(p.wq), "s"(p.wk), "s"(p.wv), "s"(p.w1), "s"(p.b2), "s"(p.out));
    if (__builtin_amdgcn_readfirstlane(threadIdx.x >> 8) == 0) run_layers<0>(p.ws, lds); else run_layers<1>(p.ws, lds);
}

}

extern "C" void kernel_launch(void* const* d_in, const int* in_sizes, int n_in, void* d_out, int out_size, void* d_ws, size_t ws_size, hipStream_t stream) {
    Params p;
    p.x = (const uint16_t*)d_in[0]; p.wq = (const uint16_t*)d_in[1]; p.wk = (const uint16_t*)d_in[2]; p.wv = (const uint16_t*)d_in[3]; p.wo = (const uint16_t*)d_in[4];
    p.w1 = (const uint16_t*)d_in[6]; p.b1 = (const uint16_t*)d_in[7]; p.w2 = (const uint16_t*)d_in[8]; p.b2 = (const uint16_t*)d_in[9];
    p.out = (uint16_t*)d_out; p.ws = (unsigned char*)d_ws;
    (void)hipFuncSetAttribute((const void*)main_kernel, hipFuncAttributeMaxDynamicSharedMemorySize, LDS_BYTES);
    hipLaunchKernelGGL(main_kernel, dim3(256), dim3(512), LDS_BYTES, stream, p);
}
```
